```python
import torch

def _softmax(x, axis=-1):
    return torch.softmax(x, dim=axis)

def _gelu(x):
    return torch.nn.functional.gelu(x, approximate='tanh')

def _silu(x):
    return torch.nn.functional.silu(x)

def _sigmoid(x):
    return torch.sigmoid(x)

def _layernorm(x, axis=-1, eps=1e-05):
    m = x.mean(dim=axis, keepdim=True)
    v = x.var(dim=axis, keepdim=True, unbiased=False)
    return (x - m) / torch.sqrt(v + eps)

def _scatter_add(tgt, idx, src, axis=0):
    return tgt.clone().index_add_(axis, idx, src)

def _segment_sum(src, seg, n):
    out = torch.zeros((n,) + src.shape[1:], dtype=src.dtype)
    return out.index_add_(0, seg, src)

def _pad(x, pw):
    flat = tuple((v for pair in reversed(pw) for v in pair))
    return torch.nn.functional.pad(x, flat)
kernel_name = 'adaLN_cond__compressed_kv_cla__dense_activation_silu__prompt'

def setup_inputs():

    _w = lambda a: (lambda t: t.to(torch.bfloat16) if t.is_floating_point() else t)(torch.as_tensor(a))

    def _A_setup():
        B, H, Sq, Sk, D, M = (1, 32, 2048, 2048, 128, 512)
        Dc = 4 * D
        return {'x': _w((lambda *s: torch.randn(*s, dtype=torch.bfloat16) * 0.05)(B, Sq, M)), 'c_kv': _w((lambda *s: torch.randn(*s, dtype=torch.bfloat16) * 0.8)(B, Sk, Dc)), 'w_q': _w((lambda *s: torch.randn(*s, dtype=torch.bfloat16) * 1.6)(M, H, D) * M ** (-0.5)), 'w_uk': _w((lambda *s: torch.randn(*s, dtype=torch.bfloat16) * 1.6)(Dc, H, D) * Dc ** (-0.5)), 'w_uv': _w((lambda *s: torch.randn(*s, dtype=torch.bfloat16) * 0.8)(Dc, H, D) * Dc ** (-0.5)), 'w_o': _w((lambda *s: torch.randn(*s, dtype=torch.bfloat16) * 0.8)(H, D, M) * (H * D) ** (-0.5))}
    _A_in = _A_setup()

    def _B_setup():
        B, S, D, F = (1, 2048, 512, 1792)
        return {'x': _w((lambda *s: torch.randn(*s, dtype=torch.bfloat16) * 0.05)(B, S, D)), 'w1': _w((lambda *s: torch.randn(*s, dtype=torch.bfloat16) * 1.6)(D, F) * D ** (-0.5)), 'w2': _w((lambda *s: torch.randn(*s, dtype=torch.bfloat16) * 0.2)(F, D) * F ** (-0.5))}
    _B_in = _B_setup()
    return {'A_x': _A_in['x'], 'A_c_kv': _A_in['c_kv'], 'A_w_q': _A_in['w_q'], 'A_w_uk': _A_in['w_uk'], 'A_w_uv': _A_in['w_uv'], 'A_w_o': _A_in['w_o'], 'B_x': _B_in['x'], 'B_w1': _B_in['w1'], 'B_w2': _B_in['w2'], 'c': _w((lambda *s: torch.randn(*s, dtype=torch.bfloat16) * 0.1)(1, 512) * 8.0), 'w_mod': _w((lambda *s: torch.randn(*s, dtype=torch.bfloat16) * 0.1)(512, 6 * 512) * 512 ** (-0.5) * 8.0)}

def reference(A_x, A_c_kv, A_w_q, A_w_uk, A_w_uv, A_w_o, B_x, B_w1, B_w2, c, w_mod):

    def _A_ref(x, c_kv, w_q, w_uk, w_uv, w_o):
        B, Sq, M = (x.shape[0], x.shape[1], x.shape[2])
        Sk = c_kv.shape[1]
        H, D = (w_q.shape[1], w_q.shape[2])
        q = torch.einsum('bqm,mhd->bhqd', x, w_q)
        k = torch.einsum('bkc,chd->bhkd', c_kv, w_uk)
        v = torch.einsum('bkc,chd->bhkd', c_kv, w_uv)
        scores = torch.einsum('bhqd,bhkd->bhqk', q, k) * D ** (-0.5)
        off = Sk - Sq
        qi = torch.arange(Sq)[:, None]
        kj = torch.arange(Sk)[None, :]
        cmask = kj <= qi + off
        scores = torch.where(cmask[None, None], scores, -1000000000.0)
        p = _softmax(scores, axis=-1)
        ctx = torch.einsum('bhqk,bhkd->bhqd', p, v)
        return torch.einsum('bhqd,hdm->bqm', ctx, w_o)

    def _B_ref(x, w1, w2):
        h = torch.einsum('bsd,df->bsf', x, w1)
        h = _silu(h)
        out = torch.einsum('bsf,fd->bsd', h, w2)
        return out
    x = A_x
    mod = (_silu(c) @ w_mod).reshape(c.shape[0], 6, 1, -1)
    sa, ca, ga, sb, cb, gb = (mod[:, 0].clone(), mod[:, 1].clone(), mod[:, 2].clone(), mod[:, 3].clone(), mod[:, 4].clone(), mod[:, 5].clone())
    h = _layernorm(x, axis=-1) * (1 + ca) + sa
    x = x + ga * _A_ref(h, A_c_kv, A_w_q, A_w_uk, A_w_uv, A_w_o)
    h = _layernorm(x, axis=-1) * (1 + cb) + sb
    x = x + gb * _B_ref(h, B_w1, B_w2)
    return x
```

```cpp
#include <hip/hip_runtime.h>
#include <hip/hip_bf16.h>
typedef unsigned short u16;
typedef short bf16x8 __attribute__((ext_vector_type(8)));
typedef float f32x16 __attribute__((ext_vector_type(16)));
typedef float f32x4 __attribute__((ext_vector_type(4)));
constexpr int S=2048, M=512, H=32, D=128, HD=4096, F=1792;
__device__ __forceinline__ float bf2f(u16 v){ return __uint_as_float(((unsigned)v)<<16); }
__device__ __forceinline__ u16 f2bf(float f){ __bf16 b=(__bf16)f; return __builtin_bit_cast(u16,b); }

__global__ void k_mod(const u16* c, const u16* w, float* mod){
  __shared__ float sc[512]; __shared__ float red[4][64];
  int t=threadIdx.x;
  for(int i=t;i<512;i+=256){ float v=bf2f(c[i]); sc[i]=bf2f(f2bf(v/(1.f+__expf(-v)))); }
  __syncthreads();
  int col=blockIdx.x*64+(t&63), g=t>>6; float s=0;
  for(int k=g*128;k<g*128+128;++k) s+=sc[k]*bf2f(w[(size_t)k*3072+col]);
  red[g][t&63]=s; __syncthreads();
  if(t<64) mod[col]=bf2f(f2bf(red[0][t]+red[1][t]+red[2][t]+red[3][t]));
}
__global__ void k_ln(const u16* x, const float* sh, const float* sc, u16* h){
  int row=blockIdx.x*4+(threadIdx.x>>6), l=threadIdx.x&63;
  float v[8]; float s=0;
  for(int j=0;j<8;++j){ v[j]=bf2f(x[(size_t)row*M+l*8+j]); s+=v[j]; }
  for(int o=32;o;o>>=1) s+=__shfl_xor(s,o);
  float m=s/512; float q=0; for(int j=0;j<8;++j){ float d=v[j]-m; q+=d*d; }
  for(int o=32;o;o>>=1) q+=__shfl_xor(q,o);
  float r=rsqrtf(q/512+1e-5f);
  for(int j=0;j<8;++j){ int cidx=l*8+j; h[(size_t)row*M+cidx]=f2bf((v[j]-m)*r*(1.f+sc[cidx])+sh[cidx]); }
}
template<int MODE> __global__ void k_gemm(const u16* A,const u16* B,u16* C,int lda,int ldb,int ldc,int K,const u16* res,const float* gate){
  __shared__ u16 sa[64][40]; __shared__ u16 sb[64][40];
  int t=threadIdx.x,w=t>>6,l=t&63; int bm=blockIdx.y*64,bn=blockIdx.x*64; int wm=(w>>1)*32,wn=(w&1)*32;
  f32x16 acc={};
  for(int k0=0;k0<K;k0+=32){
    { int r=t>>2,c=(t&3)*8; *(bf16x8*)&sa[r][c]=*(const bf16x8*)&A[(size_t)(bm+r)*lda+k0+c]; }
    { int k=t>>3,c=(t&7)*8; bf16x8 v=*(const bf16x8*)&B[(size_t)(k0+k)*ldb+bn+c]; for(int j=0;j<8;++j) sb[c+j][k]=(u16)v[j]; }
    __syncthreads();
    for(int s=0;s<2;++s){ bf16x8 a=*(bf16x8*)&sa[wm+(l&31)][s*16+(l>>5)*8]; bf16x8 b=*(bf16x8*)&sb[wn+(l&31)][s*16+(l>>5)*8];
      acc=__builtin_amdgcn_mfma_f32_32x32x16_bf16(a,b,acc,0,0,0); }
    __syncthreads();
  }
  for(int r=0;r<16;++r){ int row=bm+wm+(r&3)+8*(r>>2)+4*(l>>5), col=bn+wn+(l&31); float v=acc[r];
    if(MODE==1){ v=bf2f(res[(size_t)row*ldc+col])+gate[col]*v; }
    if(MODE==2){ v=v/(1.f+__expf(-v)); }
    C[(size_t)row*ldc+col]=f2bf(v); }
}
__global__ __launch_bounds__(256,1) void k_attn(const u16* Q,const u16* Kk,const u16* V,u16* O){
  __shared__ u16 sk[32][136]; __shared__ u16 svt[128][40]; __shared__ u16 sp[4][32][40];
  int t=threadIdx.x,w=t>>6,l=t&63,h=blockIdx.y,q0=blockIdx.x*128+w*32; int r=l&31,hf=l>>5;
  bf16x8 qf[8]; _Pragma("unroll") for(int s=0;s<8;++s) qf[s]=*(const bf16x8*)&Q[(size_t)(q0+r)*HD+h*D+s*16+hf*8];
  f32x16 o[4]={}; float m=-1e30f,ls=0; const float c2=0.08838834764831845f*1.4426950408889634f;
  int nk=(blockIdx.x*128+128)/32;
  for(int kt=0;kt<nk;++kt){
    __syncthreads();
    for(int i=t;i<32*16;i+=256){ int k=i>>4,c=(i&15)*8; *(bf16x8*)&sk[k][c]=*(const bf16x8*)&Kk[(size_t)(kt*32+k)*HD+h*D+c];
      bf16x8 v=*(const bf16x8*)&V[(size_t)(kt*32+k)*HD+h*D+c]; for(int j=0;j<8;++j) svt[c+j][k]=(u16)v[j]; }
    __syncthreads();
    f32x16 s={}; _Pragma("unroll") for(int d=0;d<8;++d){ bf16x8 a=*(bf16x8*)&sk[r][d*16+hf*8]; s=__builtin_amdgcn_mfma_f32_32x32x16_bf16(a,qf[d],s,0,0,0);}
    float mx=-1e30f; _Pragma("unroll") for(int i=0;i<16;++i){ int key=kt*32+(i&3)+8*(i>>2)+4*hf; if(key>q0+r) s[i]=-INFINITY; mx=fmaxf(mx,s[i]); }
    mx=fmaxf(mx,__shfl_xor(mx,32)); float mn=fmaxf(m,mx); float al=exp2f((m-mn)*c2); m=mn; float ps=0;
    _Pragma("unroll") for(int i=0;i<16;++i){ float p=exp2f((s[i]-mn)*c2); ps+=p; sp[w][r][(i&3)+8*(i>>2)+4*hf]=f2bf(p); }
    ps+=__shfl_xor(ps,32); ls=ls*al+ps;
    _Pragma("unroll") for(int d=0;d<4;++d) _Pragma("unroll") for(int i=0;i<16;++i) o[d][i]*=al;
    __builtin_amdgcn_s_waitcnt(0xc07f); __builtin_amdgcn_wave_barrier();
    _Pragma("unroll") for(int ks=0;ks<2;++ks){ bf16x8 b=*(bf16x8*)&sp[w][r][ks*16+hf*8]; _Pragma("unroll") for(int d=0;d<4;++d){ bf16x8 a=*(bf16x8*)&svt[d*32+r][ks*16+hf*8]; o[d]=__builtin_amdgcn_mfma_f32_32x32x16_bf16(a,b,o[d],0,0,0);} }
  }
  float il=1.f/ls;
  _Pragma("unroll") for(int d=0;d<4;++d) _Pragma("unroll") for(int i=0;i<16;++i){ int dd=d*32+(i&3)+8*(i>>2)+4*hf; O[(size_t)(q0+r)*HD+h*D+dd]=f2bf(o[d][i]*il); }
}
extern "C" void kernel_launch(void* const* d_in, const int* in_sizes, int n_in, void* d_out, int out_size, void* d_ws, size_t ws_size, hipStream_t stream){
  const u16 *x=(const u16*)d_in[0],*ckv=(const u16*)d_in[1],*wq=(const u16*)d_in[2],*wuk=(const u16*)d_in[3],*wuv=(const u16*)d_in[4],*wo=(const u16*)d_in[5];
  const u16 *w1=(const u16*)d_in[7],*w2=(const u16*)d_in[8],*c=(const u16*)d_in[9],*wmod=(const u16*)d_in[10];
  char* ws=(char*)d_ws; float* mod=(float*)ws; u16* h=(u16*)(ws+(1<<20)); u16* q=(u16*)(ws+(4<<20)); u16* k=q+(size_t)S*HD; u16* v=k+(size_t)S*HD; u16* ctx=v+(size_t)S*HD;
  u16* x1=ctx+(size_t)S*HD; u16* hid=x1+(size_t)S*M;
  k_mod<<<48,256,0,stream>>>(c,wmod,mod);
  k_ln<<<S/4,256,0,stream>>>(x,mod,mod+512,h);
  k_gemm<0><<<dim3(HD/64,S/64),256,0,stream>>>(h,wq,q,M,HD,HD,M,nullptr,nullptr);
  k_gemm<0><<<dim3(HD/64,S/64),256,0,stream>>>(ckv,wuk,k,M,HD,HD,M,nullptr,nullptr);
  k_gemm<0><<<dim3(HD/64,S/64),256,0,stream>>>(ckv,wuv,v,M,HD,HD,M,nullptr,nullptr);
  k_attn<<<dim3(S/128,H),256,0,stream>>>(q,k,v,ctx);
  k_gemm<1><<<dim3(M/64,S/64),256,0,stream>>>(ctx,wo,x1,HD,M,M,HD,x,mod+1024);
  k_ln<<<S/4,256,0,stream>>>(x1,mod+1536,mod+2048,h);
  k_gemm<2><<<dim3(F/64,S/64),256,0,stream>>>(h,w1,hid,M,F,F,M,nullptr,nullptr);
  k_gemm<1><<<dim3(M/64,S/64),256,0,stream>>>(hid,w2,(u16*)d_out,F,M,M,F,x1,mod+2560);
}
```

```cpp
#include <hip/hip_runtime.h>
#include <hip/hip_bf16.h>
typedef unsigned short u16;

__global__ __launch_bounds__(256, 1) void k_f(const void* x, const void* ckv, const void* wq, const void* wuk, const void* wuv, const void* wo, const void* w1, const void* w2, const void* c, const void* wmod, void* out, void* ws) {
  __shared__ __attribute__((aligned(16))) char lds[163840];
  asm volatile(
R"ASM(s_mov_b64 s[36:37], %0
s_mov_b32 s38, %1
v_mov_b32 v3, %2
s_load_dwordx16 s[8:23], s[36:37], 0
s_load_dwordx8 s[24:31], s[36:37], 64
s_getpc_b64 s[72:73]
L_cp0%=:
s_and_b32 s73, s73, 0xffff
s_mov_b32 s74, L_end%=-L_cp0%=
s_mov_b32 s75, 0x20000
s_lshr_b32 s78, s38, 3
s_mul_i32 s78, s78, 3072
v_lshrrev_b32 v28, 6, v3
v_mul_u32_u24 v28, 6, v28
v_and_b32 v29, 63, v3
v_add_lshl_u32 v28, v28, v29, 7
v_add_u32 v28, s78, v28
s_mov_b64 exec, 0x3f
buffer_load_dword a200, v28, s[72:75], 0 offen
s_mov_b64 exec, -1
v_and_b32 v2, 63, v3
v_lshrrev_b32 v4, 6, v3
v_lshlrev_b32 v1, 4, v2
v_readfirstlane_b32 s39, v4
s_and_b32 s62, s38, 7
s_lshr_b32 s63, s38, 3
s_and_b32 s61, s63, 7
s_lshr_b32 s70, s63, 3
s_lshl_b32 s71, s62, 2
s_add_u32 s60, s71, s70
s_mul_i32 s79, s62, 24
s_add_u32 s79, s79, s63
s_mov_b32 s78, s63
s_mov_b32 s32, 0
s_cmp_eq_u32 s39, 0
s_cselect_b32 s34, 1, 0
s_mov_b32 s35, 0
s_add_u32 s64, s61, s38
s_and_b32 s64, s64, 3
s_lshl_b32 s64, s64, 1
s_lshl_b32 s65, s64, 19
s_lshl_b32 s64, s64, 7
s_lshl_b32 s66, s39, 13
s_lshl_b32 s67, s39, 12
s_add_u32 s67, s67, 32768
s_lshl_b32 s68, s60, 20
s_lshl_b32 s62, s61, 17
s_add_u32 s68, s68, s62
s_lshr_b32 s62, s39, 1
s_lshl_b32 s62, s62, 16
s_add_u32 s68, s68, s62
s_and_b32 s63, s39, 1
s_lshl_b32 s63, s63, 11
s_add_u32 s68, s68, s63
s_add_u32 s69, s68, s63
v_lshrrev_b32 v23, 1, v3
v_mul_u32_u24 v24, 24576, v23
v_and_b32 v25, 1, v3
v_lshl_add_u32 v24, v25, 4, v24
v_lshlrev_b32 v23, 3, v23
s_waitcnt lgkmcnt(0)
s_add_u32 s40, s30, 68157440
s_addc_u32 s41, s31, 0
s_and_b32 s41, s41, 0xffff
s_mov_b32 s42, 229376
s_mov_b32 s43, 0x20000
s_add_u32 s70, s30, 68354048
s_addc_u32 s71, s31, 0
s_load_dword s33, s[70:71], 0
s_mov_b32 s72, s26
s_mov_b32 s73, s27
s_and_b32 s73, s73, 0xffff
s_mov_b32 s74, 3145728
s_mov_b32 s75, 0x20000
s_lshl_b32 s62, s79, 5
buffer_load_dwordx4 v[32:35], v24, s[72:75], s62 offen
s_add_u32 s62, s62, 6144
buffer_load_dwordx4 v[36:39], v24, s[72:75], s62 offen
s_add_u32 s62, s62, 6144
buffer_load_dwordx4 v[40:43], v24, s[72:75], s62 offen
s_add_u32 s62, s62, 6144
buffer_load_dwordx4 v[44:47], v24, s[72:75], s62 offen
s_mov_b32 s56, s24
s_mov_b32 s57, s25
s_and_b32 s57, s57, 0xffff
s_mov_b32 s58, 1024
s_mov_b32 s59, 0x20000
buffer_load_dwordx2 v[48:49], v23, s[56:59], 0 offen
s_mov_b32 s56, s8
s_mov_b32 s57, s9
s_and_b32 s57, s57, 0xffff
s_mov_b32 s58, 2097152
s_mov_b32 s59, 0x20000
s_lshl_b32 s62, s38, 13
s_lshl_b32 s63, s39, 11
s_add_u32 s62, s62, s63
buffer_load_dwordx4 v[50:53], v1, s[56:59], s62 offen offset:0
buffer_load_dwordx4 v[54:57], v1, s[56:59], s62 offen offset:1024
v_lshrrev_b32 v23, 3, v2
v_lshlrev_b32 v23, 10, v23
v_lshrrev_b32 v24, 4, v2
v_and_b32 v25, 7, v2
v_xor_b32 v26, v24, v25
v_lshl_add_u32 v4, v26, 4, v23
v_xor_b32 v26, 4, v26
v_lshl_add_u32 v5, v26, 4, v23
s_lshl_b32 s62, s39, 16
v_add_u32 v4, s62, v4
v_add_u32 v5, s62, v5
v_add_u32 v11, 54272, v5
v_add_u32 v10, 47104, v4
v_add_u32 v9, 39936, v5
v_add_u32 v8, 32768, v4
v_add_u32 v7, 21504, v5
v_add_u32 v6, 14336, v4
v_add_u32 v5, 7168, v5
v_lshrrev_b32 v23, 2, v2
v_and_b32 v23, 7, v23
v_lshlrev_b32 v23, 13, v23
s_lshl_b32 s62, s39, 17
v_add_u32 v23, s62, v23
v_lshrrev_b32 v24, 5, v2
v_lshlrev_b32 v24, 6, v24
v_add_u32 v23, v23, v24
v_bfe_u32 v24, v2, 4, 1
v_and_b32 v25, 3, v2
v_xor_b32 v26, v24, v25
v_lshl_add_u32 v12, v26, 4, v23
v_add_u32 v12, 4096, v12
v_xor_b32 v26, v24, v25
v_lshl_add_u32 v13, v26, 4, v23
v_add_u32 v13, 3200, v13
v_xor_b32 v26, v24, v25
v_xor_b32 v26, 2, v26
v_lshl_add_u32 v14, v26, 4, v23
v_add_u32 v14, 67584, v14
v_xor_b32 v26, v24, v25
v_xor_b32 v26, 2, v26
v_lshl_add_u32 v15, v26, 4, v23
v_add_u32 v15, 66688, v15
s_lshl_b32 s62, s61, 18
s_add_u32 s44, s10, s62
s_addc_u32 s45, s11, 0
s_and_b32 s45, s45, 0xffff
s_mov_b32 s46, 262144
s_mov_b32 s47, 0x20000
s_lshl_b32 s62, s60, 8
s_sub_u32 s62, s62, 4096
s_ashr_i32 s63, s62, 31
s_add_u32 s48, s14, s62
s_addc_u32 s49, s15, s63
s_and_b32 s49, s49, 0xffff
s_sub_u32 s50, 4194304, s62
s_mov_b32 s51, 0x20000
s_add_u32 s52, s16, s62
s_addc_u32 s53, s17, s63
s_and_b32 s53, s53, 0xffff
s_sub_u32 s54, 4194304, s62
s_mov_b32 s55, 0x20000
s_add_u32 m0, s66, 0
s_nop 0
buffer_load_dwordx4 v4, s[44:47], s64 offen offset:0 lds
s_lshl_b32 s62, s38, 2
s_add_u32 s62, s62, s39
s_and_b32 s63, s62, 7
s_lshr_b32 s70, s62, 3
s_lshl_b32 s76, s63, 7
s_add_u32 s76, s76, s70
s_lshl_b32 s76, s76, 12
buffer_load_dwordx4 v5, s[44:47], s64 offen offset:1024 lds
s_mul_i32 s77, s63, 56
s_add_u32 s77, s77, s70
s_lshl_b32 s77, s77, 12
s_add_u32 s77, s77, 4194304
v_lshrrev_b32 v100, 3, v2
v_bfe_u32 v101, v2, 4, 2
v_lshlrev_b32 v101, 1, v101
buffer_load_dwordx4 v6, s[44:47], s64 offen offset:2048 lds
v_and_b32 v102, 7, v2
v_xor_b32 v101, v101, v102
v_lshlrev_b32 v101, 4, v101
v_lshl_add_u32 v100, v100, 10, v101
s_lshl_b32 s71, s63, 7
v_add_u32 v100, s71, v100
s_lshl_b32 s71, s70, 15
buffer_load_dwordx4 v7, s[44:47], s64 offen offset:3072 lds
v_add_u32 v100, s71, v100
s_cmp_lt_u32 s62, 448
s_cselect_b32 s71, 0, 0x80000000
v_add_u32 v22, s71, v100
v_add_u32 v27, s71, v1
v_mov_b32 v3, v100
s_mov_b32 s56, s22
s_add_u32 m0, s66, 4096
s_nop 0
buffer_load_dwordx4 v8, s[44:47], s64 offen offset:0 lds
s_mov_b32 s57, s23
s_and_b32 s57, s57, 0xffff
s_mov_b32 s58, 1835008
s_mov_b32 s59, 0x20000
v_and_b32 v23, 15, v2
v_lshrrev_b32 v24, 4, v2
v_lshrrev_b32 v25, 1, v23
buffer_load_dwordx4 v9, s[44:47], s64 offen offset:1024 lds
v_xor_b32 v25, v24, v25
v_lshlrev_b32 v25, 4, v25
v_lshl_add_u32 v25, v23, 7, v25
s_lshr_b32 s62, s39, 1
s_lshl_b32 s62, s62, 14
v_add_u32 v16, s62, v25
v_xor_b32 v17, 64, v16
buffer_load_dwordx4 v10, s[44:47], s64 offen offset:2048 lds
v_bfe_u32 v23, v2, 2, 2
v_lshlrev_b32 v23, 6, v23
v_lshl_add_u32 v23, v24, 11, v23
v_and_b32 v25, 1, v2
v_lshl_add_u32 v23, v25, 3, v23
s_and_b32 s62, s39, 1
s_lshl_b32 s63, s62, 10
buffer_load_dwordx4 v11, s[44:47], s64 offen offset:3072 lds
s_add_u32 s63, s63, 32768
v_add_u32 v23, s63, v23
v_bfe_u32 v25, v2, 1, 1
v_and_b32 v26, 1, v24
v_lshl_add_u32 v25, v26, 1, v25
v_xor_b32 v26, 0, v25
v_lshl_add_u32 v18, v26, 4, v23
s_add_u32 m0, s67, 0
s_nop 0
buffer_load_dwordx4 v12, s[48:51], s65 offen offset:0 lds
v_xor_b32 v26, 1, v25
v_lshl_add_u32 v19, v26, 4, v23
v_add_u32 v19, 256, v19
v_xor_b32 v26, 2, v25
v_lshl_add_u32 v20, v26, 4, v23
v_xor_b32 v26, 3, v25
v_lshl_add_u32 v21, v26, 4, v23
buffer_load_dwordx4 v13, s[48:51], s65 offen offset:1024 lds
v_add_u32 v21, 256, v21
s_lshl_b32 s71, s39, 12
v_lshrrev_b32 v100, 2, v2
v_lshlrev_b32 v100, 7, v100
v_and_b32 v101, 1, v2
v_lshl_add_u32 v100, v101, 3, v100
v_bfe_u32 v101, v2, 1, 1
buffer_load_dwordx4 v14, s[48:51], s65 offen offset:2048 lds
v_lshl_add_u32 v100, v101, 4, v100
v_bfe_u32 v101, v2, 3, 2
s_add_u32 s71, s71, 131072
v_xor_b32 v102, 0, v101
v_lshl_add_u32 v102, v102, 5, v100
v_add_u32 v23, s71, v102
v_xor_b32 v102, 1, v101
buffer_load_dwordx4 v15, s[48:51], s65 offen offset:3072 lds
v_lshl_add_u32 v102, v102, 5, v100
v_add_u32 v24, s71, v102
v_xor_b32 v102, 2, v101
v_lshl_add_u32 v102, v102, 5, v100
v_add_u32 v25, s71, v102
v_xor_b32 v102, 3, v101
v_lshl_add_u32 v102, v102, 5, v100
v_add_u32 v26, s71, v102
s_waitcnt vmcnt(14)
s_add_u32 m0, s67, 16384
s_nop 0
buffer_load_dwordx4 v12, s[52:55], s65 offen offset:0 lds
v_lshlrev_b32 v84, 16, v48
v_and_b32 v85, 0xffff0000, v48
v_lshlrev_b32 v86, 16, v49
v_and_b32 v87, 0xffff0000, v49
v_mul_f32 v88, 0xbfb8aa3b, v84
v_mul_f32 v89, 0xbfb8aa3b, v85
v_mul_f32 v90, 0xbfb8aa3b, v86
v_mul_f32 v91, 0xbfb8aa3b, v87
v_exp_f32 v88, v88
v_exp_f32 v89, v89
v_exp_f32 v90, v90
v_exp_f32 v91, v91
v_add_f32 v88, 1.0, v88
v_add_f32 v89, 1.0, v89
v_add_f32 v90, 1.0, v90
v_add_f32 v91, 1.0, v91
v_rcp_f32 v88, v88
v_rcp_f32 v89, v89
v_rcp_f32 v90, v90
v_rcp_f32 v91, v91
buffer_load_dwordx4 v13, s[52:55], s65 offen offset:1024 lds
v_mul_f32 v84, v84, v88
v_mul_f32 v85, v85, v89
v_mul_f32 v86, v86, v90
v_mul_f32 v87, v87, v91
v_cvt_pk_bf16_f32 v88, v84, v85
v_cvt_pk_bf16_f32 v89, v86, v87
v_lshlrev_b32 v84, 16, v88
v_and_b32 v85, 0xffff0000, v88
v_lshlrev_b32 v86, 16, v89
v_and_b32 v87, 0xffff0000, v89
v_lshlrev_b32 v100, 16, v32
v_and_b32 v101, 0xffff0000, v32
v_mul_f32 v92, v84, v100
)ASM"
R"ASM(v_mul_f32 v93, v84, v101
v_lshlrev_b32 v100, 16, v33
v_and_b32 v101, 0xffff0000, v33
v_mul_f32 v94, v84, v100
v_mul_f32 v95, v84, v101
v_lshlrev_b32 v100, 16, v34
v_and_b32 v101, 0xffff0000, v34
v_mul_f32 v96, v84, v100
buffer_load_dwordx4 v14, s[52:55], s65 offen offset:2048 lds
v_mul_f32 v97, v84, v101
v_lshlrev_b32 v100, 16, v35
v_and_b32 v101, 0xffff0000, v35
v_mul_f32 v98, v84, v100
v_mul_f32 v99, v84, v101
v_lshlrev_b32 v100, 16, v36
v_and_b32 v101, 0xffff0000, v36
v_fmac_f32 v92, v85, v100
v_fmac_f32 v93, v85, v101
v_lshlrev_b32 v100, 16, v37
v_and_b32 v101, 0xffff0000, v37
v_fmac_f32 v94, v85, v100
v_fmac_f32 v95, v85, v101
v_lshlrev_b32 v100, 16, v38
v_and_b32 v101, 0xffff0000, v38
v_fmac_f32 v96, v85, v100
v_fmac_f32 v97, v85, v101
v_lshlrev_b32 v100, 16, v39
v_and_b32 v101, 0xffff0000, v39
v_fmac_f32 v98, v85, v100
v_fmac_f32 v99, v85, v101
buffer_load_dwordx4 v15, s[52:55], s65 offen offset:3072 lds
s_add_u32 s64, s64, 128
s_add_u32 s65, s65, 524288
s_and_b32 s64, s64, 0x3ff
s_and_b32 s65, s65, 0x3fffff
s_xor_b32 s66, s66, 0x10000
s_xor_b32 s67, s67, 0x10000
v_lshlrev_b32 v100, 16, v40
v_and_b32 v101, 0xffff0000, v40
v_fmac_f32 v92, v86, v100
v_fmac_f32 v93, v86, v101
v_lshlrev_b32 v100, 16, v41
v_and_b32 v101, 0xffff0000, v41
v_fmac_f32 v94, v86, v100
v_fmac_f32 v95, v86, v101
v_lshlrev_b32 v100, 16, v42
v_and_b32 v101, 0xffff0000, v42
v_fmac_f32 v96, v86, v100
v_fmac_f32 v97, v86, v101
v_lshlrev_b32 v100, 16, v43
v_and_b32 v101, 0xffff0000, v43
v_fmac_f32 v98, v86, v100
v_fmac_f32 v99, v86, v101
v_lshlrev_b32 v100, 16, v44
v_and_b32 v101, 0xffff0000, v44
v_fmac_f32 v92, v87, v100
v_fmac_f32 v93, v87, v101
v_lshlrev_b32 v100, 16, v45
s_add_u32 m0, s66, 0
s_nop 0
buffer_load_dwordx4 v4, s[44:47], s64 offen offset:0 lds
v_and_b32 v101, 0xffff0000, v45
v_fmac_f32 v94, v87, v100
v_fmac_f32 v95, v87, v101
v_lshlrev_b32 v100, 16, v46
v_and_b32 v101, 0xffff0000, v46
v_fmac_f32 v96, v87, v100
v_fmac_f32 v97, v87, v101
v_lshlrev_b32 v100, 16, v47
v_and_b32 v101, 0xffff0000, v47
v_fmac_f32 v98, v87, v100
v_fmac_f32 v99, v87, v101
v_add_f32_dpp v92, v92, v92 row_ror:8 row_mask:0xf bank_mask:0xf
v_add_f32_dpp v93, v93, v93 row_ror:8 row_mask:0xf bank_mask:0xf
v_add_f32_dpp v94, v94, v94 row_ror:8 row_mask:0xf bank_mask:0xf
v_add_f32_dpp v95, v95, v95 row_ror:8 row_mask:0xf bank_mask:0xf
v_add_f32_dpp v96, v96, v96 row_ror:8 row_mask:0xf bank_mask:0xf
v_add_f32_dpp v97, v97, v97 row_ror:8 row_mask:0xf bank_mask:0xf
v_add_f32_dpp v98, v98, v98 row_ror:8 row_mask:0xf bank_mask:0xf
v_add_f32_dpp v99, v99, v99 row_ror:8 row_mask:0xf bank_mask:0xf
v_add_f32_dpp v92, v92, v92 row_ror:4 row_mask:0xf bank_mask:0xf
v_add_f32_dpp v93, v93, v93 row_ror:4 row_mask:0xf bank_mask:0xf
buffer_load_dwordx4 v5, s[44:47], s64 offen offset:1024 lds
v_add_f32_dpp v94, v94, v94 row_ror:4 row_mask:0xf bank_mask:0xf
v_add_f32_dpp v95, v95, v95 row_ror:4 row_mask:0xf bank_mask:0xf
v_add_f32_dpp v96, v96, v96 row_ror:4 row_mask:0xf bank_mask:0xf
v_add_f32_dpp v97, v97, v97 row_ror:4 row_mask:0xf bank_mask:0xf
v_add_f32_dpp v98, v98, v98 row_ror:4 row_mask:0xf bank_mask:0xf
v_add_f32_dpp v99, v99, v99 row_ror:4 row_mask:0xf bank_mask:0xf
v_add_f32_dpp v92, v92, v92 row_ror:2 row_mask:0xf bank_mask:0xf
v_add_f32_dpp v93, v93, v93 row_ror:2 row_mask:0xf bank_mask:0xf
v_add_f32_dpp v94, v94, v94 row_ror:2 row_mask:0xf bank_mask:0xf
v_add_f32_dpp v95, v95, v95 row_ror:2 row_mask:0xf bank_mask:0xf
v_add_f32_dpp v96, v96, v96 row_ror:2 row_mask:0xf bank_mask:0xf
v_add_f32_dpp v97, v97, v97 row_ror:2 row_mask:0xf bank_mask:0xf
v_add_f32_dpp v98, v98, v98 row_ror:2 row_mask:0xf bank_mask:0xf
v_add_f32_dpp v99, v99, v99 row_ror:2 row_mask:0xf bank_mask:0xf
v_lshrrev_b32 v100, 4, v2
v_lshlrev_b32 v100, 6, v100
v_and_b32 v101, 1, v2
v_lshl_add_u32 v100, v101, 5, v100
s_lshl_b32 s62, s39, 8
s_add_u32 s62, s62, 131072
v_add_u32 v100, s62, v100
s_mov_b32 exec_lo, 0x00030003
s_mov_b32 exec_hi, 0x00030003
ds_write_b128 v100, v[92:95]
ds_write_b128 v100, v[96:99] offset:16
s_mov_b64 exec, -1
s_waitcnt lgkmcnt(0)
s_barrier
v_lshrrev_b32 v100, 4, v2
v_lshlrev_b32 v100, 8, v100
v_and_b32 v101, 15, v2
v_lshl_add_u32 v100, v101, 2, v100
v_add_u32 v100, 131072, v100
ds_read_b32 v92, v100 offset:0
ds_read_b32 v93, v100 offset:64
ds_read_b32 v94, v100 offset:128
ds_read_b32 v95, v100 offset:192
s_lshr_b32 s62, s79, 5
s_cmp_eq_u32 s62, 1
s_cselect_b32 s63, 1.0, 0
s_cmp_eq_u32 s62, 4
s_cselect_b32 s63, 1.0, s63
s_cmp_eq_u32 s62, 0
s_cselect_b32 s70, 0x3e0293ee, 1.0
s_cmp_eq_u32 s62, 5
s_cselect_b32 s70, 0xbf317218, s70
s_waitcnt lgkmcnt(0)
v_add_f32 v92, v92, v93
v_add_f32 v94, v94, v95
v_add_f32 v92, v92, v94
v_mov_b32 v93, v92
s_nop 1
v_permlane32_swap_b32 v92, v93
s_nop 0
v_add_f32 v92, v92, v93
v_mov_b32 v93, v92
s_nop 1
v_permlane16_swap_b32 v92, v93
s_nop 0
v_add_f32 v92, v92, v93
v_cvt_pk_bf16_f32 v92, v92, v92
v_lshlrev_b32 v92, 16, v92
v_add_f32 v92, s63, v92
v_cvt_pk_bf16_f32 v92, v92, v92
v_lshlrev_b32 v92, 16, v92
v_mul_f32 v93, s70, v92
s_nop 1
v_mov_b32_dpp v94, v92 quad_perm:[1,0,3,2] row_mask:0xf bank_mask:0xf
v_cvt_pk_bf16_f32 v94, v92, v94
s_cmp_lt_u32 s78, 24
s_cselect_b32 s71, 1, 0
s_cmp_eq_u32 s39, 0
s_cselect_b32 s71, s71, 0
s_cmp_eq_u32 s71, 0
s_cbranch_scc1 L_nomod%=
s_add_u32 s72, s30, 52428800
s_addc_u32 s73, s31, 0
s_and_b32 s73, s73, 0xffff
s_mov_b32 s74, 12288
s_mov_b32 s75, 0x20000
s_lshl_b32 s63, s79, 6
v_lshlrev_b32 v101, 2, v101
s_mov_b64 exec, 0xffff
buffer_store_dword v93, v101, s[72:75], s63 offen sc1
s_mov_b64 exec, -1
s_sub_u32 s63, s62, 3
s_cmp_gt_u32 s63, 1
s_cbranch_scc1 L_nomod%=
s_add_u32 s72, s30, 67633152
s_addc_u32 s73, s31, 0
s_and_b32 s73, s73, 0xffff
s_mov_b32 s74, 16384
s_mov_b32 s75, 0x20000
s_and_b32 s70, s79, 31
s_lshr_b32 s71, s70, 1
s_lshl_b32 s71, s71, 10
s_and_b32 s70, s70, 1
s_lshl_b32 s70, s70, 3
s_add_u32 s71, s71, s70
s_sub_u32 s63, 1, s63
s_lshl_b32 s63, s63, 4
s_add_u32 s71, s71, s63
v_bfe_u32 v100, v2, 2, 2
v_lshlrev_b32 v100, 8, v100
v_and_b32 v101, 3, v2
v_lshl_add_u32 v100, v101, 1, v100
s_mov_b64 exec, 0x5555
buffer_store_dword v94, v100, s[72:75], s71 offen sc1
s_mov_b64 exec, -1
L_nomod%=:
s_add_u32 s33, s33, 1
s_waitcnt vmcnt(18)
buffer_load_dwordx4 v6, s[44:47], s64 offen offset:2048 lds
v_lshlrev_b32 v64, 16, v50
v_and_b32 v65, 0xffff0000, v50
v_lshlrev_b32 v66, 16, v51
v_and_b32 v67, 0xffff0000, v51
v_lshlrev_b32 v68, 16, v52
v_and_b32 v69, 0xffff0000, v52
v_lshlrev_b32 v70, 16, v53
v_and_b32 v71, 0xffff0000, v53
v_lshlrev_b32 v72, 16, v54
v_and_b32 v73, 0xffff0000, v54
v_lshlrev_b32 v74, 16, v55
v_and_b32 v75, 0xffff0000, v55
v_lshlrev_b32 v76, 16, v56
v_and_b32 v77, 0xffff0000, v56
v_lshlrev_b32 v78, 16, v57
v_and_b32 v79, 0xffff0000, v57
v_add_f32 v80, v64, v65
v_add_f32 v81, v72, v73
v_add_f32 v80, v80, v66
v_add_f32 v81, v81, v74
buffer_load_dwordx4 v7, s[44:47], s64 offen offset:3072 lds
v_add_f32 v80, v80, v67
v_add_f32 v81, v81, v75
v_add_f32 v80, v80, v68
v_add_f32 v81, v81, v76
v_add_f32 v80, v80, v69
v_add_f32 v81, v81, v77
v_add_f32 v80, v80, v70
v_add_f32 v81, v81, v78
v_add_f32 v80, v80, v71
v_add_f32 v81, v81, v79
s_nop 0
v_add_f32_dpp v80, v80, v80 row_ror:8 row_mask:0xf bank_mask:0xf
v_add_f32_dpp v81, v81, v81 row_ror:8 row_mask:0xf bank_mask:0xf
s_nop 0
v_add_f32_dpp v80, v80, v80 row_ror:4 row_mask:0xf bank_mask:0xf
v_add_f32_dpp v81, v81, v81 row_ror:4 row_mask:0xf bank_mask:0xf
s_nop 0
v_add_f32_dpp v80, v80, v80 row_ror:2 row_mask:0xf bank_mask:0xf
v_add_f32_dpp v81, v81, v81 row_ror:2 row_mask:0xf bank_mask:0xf
s_nop 0
v_add_f32_dpp v80, v80, v80 row_ror:1 row_mask:0xf bank_mask:0xf
s_add_u32 m0, s66, 4096
s_nop 0
buffer_load_dwordx4 v8, s[44:47], s64 offen offset:0 lds
v_add_f32_dpp v81, v81, v81 row_ror:1 row_mask:0xf bank_mask:0xf
s_nop 0
v_mov_b32 v82, v80
v_mov_b32 v83, v81
s_nop 1
v_permlane32_swap_b32 v80, v82
v_permlane32_swap_b32 v81, v83
s_nop 0
v_add_f32 v80, v80, v82
v_add_f32 v81, v81, v83
v_mov_b32 v82, v80
v_mov_b32 v83, v81
s_nop 1
v_permlane16_swap_b32 v80, v82
v_permlane16_swap_b32 v81, v83
s_nop 0
v_add_f32 v80, v80, v82
v_add_f32 v81, v81, v83
v_mul_f32 v80, 0x3b000000, v80
v_mul_f32 v81, 0x3b000000, v81
buffer_load_dwordx4 v9, s[44:47], s64 offen offset:1024 lds
v_sub_f32 v64, v64, v80
v_sub_f32 v72, v72, v81
v_sub_f32 v65, v65, v80
v_sub_f32 v73, v73, v81
v_sub_f32 v66, v66, v80
v_sub_f32 v74, v74, v81
v_sub_f32 v67, v67, v80
v_sub_f32 v75, v75, v81
v_sub_f32 v68, v68, v80
v_sub_f32 v76, v76, v81
v_sub_f32 v69, v69, v80
v_sub_f32 v77, v77, v81
v_sub_f32 v70, v70, v80
v_sub_f32 v78, v78, v81
v_sub_f32 v71, v71, v80
v_sub_f32 v79, v79, v81
v_mul_f32 v80, v64, v64
v_mul_f32 v81, v72, v72
v_fmac_f32 v80, v65, v65
v_fmac_f32 v81, v73, v73
v_fmac_f32 v80, v66, v66
buffer_load_dwordx4 v10, s[44:47], s64 offen offset:2048 lds
v_fmac_f32 v81, v74, v74
v_fmac_f32 v80, v67, v67
v_fmac_f32 v81, v75, v75
v_fmac_f32 v80, v68, v68
v_fmac_f32 v81, v76, v76
v_fmac_f32 v80, v69, v69
v_fmac_f32 v81, v77, v77
v_fmac_f32 v80, v70, v70
v_fmac_f32 v81, v78, v78
v_fmac_f32 v80, v71, v71
v_fmac_f32 v81, v79, v79
s_nop 0
v_add_f32_dpp v80, v80, v80 row_ror:8 row_mask:0xf bank_mask:0xf
v_add_f32_dpp v81, v81, v81 row_ror:8 row_mask:0xf bank_mask:0xf
s_nop 0
v_add_f32_dpp v80, v80, v80 row_ror:4 row_mask:0xf bank_mask:0xf
)ASM"
R"ASM(v_add_f32_dpp v81, v81, v81 row_ror:4 row_mask:0xf bank_mask:0xf
s_nop 0
v_add_f32_dpp v80, v80, v80 row_ror:2 row_mask:0xf bank_mask:0xf
v_add_f32_dpp v81, v81, v81 row_ror:2 row_mask:0xf bank_mask:0xf
buffer_load_dwordx4 v11, s[44:47], s64 offen offset:3072 lds
s_nop 0
v_add_f32_dpp v80, v80, v80 row_ror:1 row_mask:0xf bank_mask:0xf
v_add_f32_dpp v81, v81, v81 row_ror:1 row_mask:0xf bank_mask:0xf
s_nop 0
v_mov_b32 v82, v80
v_mov_b32 v83, v81
s_nop 1
v_permlane32_swap_b32 v80, v82
v_permlane32_swap_b32 v81, v83
s_nop 0
v_add_f32 v80, v80, v82
v_add_f32 v81, v81, v83
v_mov_b32 v82, v80
v_mov_b32 v83, v81
s_nop 1
v_permlane16_swap_b32 v80, v82
v_permlane16_swap_b32 v81, v83
s_nop 0
v_add_f32 v80, v80, v82
v_add_f32 v81, v81, v83
v_mov_b32 v82, 0x3727c5ac
s_add_u32 m0, s67, 0
s_nop 0
buffer_load_dwordx4 v12, s[48:51], s65 offen offset:0 lds
v_mov_b32 v83, 0x3727c5ac
v_fmac_f32 v82, 0x3b000000, v80
v_fmac_f32 v83, 0x3b000000, v81
v_rsq_f32 v82, v82
v_rsq_f32 v83, v83
s_nop 0
v_mul_f32 v82, 0x3e0293ee, v82
v_mul_f32 v83, 0x3e0293ee, v83
v_mul_f32 v64, v64, v82
v_mul_f32 v72, v72, v83
v_mul_f32 v65, v65, v82
v_mul_f32 v73, v73, v83
v_mul_f32 v66, v66, v82
v_mul_f32 v74, v74, v83
v_mul_f32 v67, v67, v82
v_mul_f32 v75, v75, v83
v_mul_f32 v68, v68, v82
v_mul_f32 v76, v76, v83
v_mul_f32 v69, v69, v82
v_mul_f32 v77, v77, v83
buffer_load_dwordx4 v13, s[48:51], s65 offen offset:1024 lds
v_mul_f32 v70, v70, v82
v_mul_f32 v78, v78, v83
v_mul_f32 v71, v71, v82
v_mul_f32 v79, v79, v83
v_accvgpr_write_b32 a160, v64
v_accvgpr_write_b32 a161, v65
v_accvgpr_write_b32 a162, v66
v_accvgpr_write_b32 a163, v67
v_accvgpr_write_b32 a164, v68
v_accvgpr_write_b32 a165, v69
v_accvgpr_write_b32 a166, v70
v_accvgpr_write_b32 a167, v71
v_accvgpr_write_b32 a168, v72
v_accvgpr_write_b32 a169, v73
v_accvgpr_write_b32 a170, v74
v_accvgpr_write_b32 a171, v75
v_accvgpr_write_b32 a172, v76
v_accvgpr_write_b32 a173, v77
v_accvgpr_write_b32 a174, v78
v_accvgpr_write_b32 a175, v79
v_mov_b32 v28, s33
v_mov_b32 v29, 0
s_waitcnt vmcnt(8)
s_lshl_b32 s79, s38, 7
s_add_u32 s79, s79, 0
s_mov_b64 exec, s[34:35]
buffer_store_dword v28, v29, s[40:43], s79 offen sc1
s_mov_b64 exec, -1
s_barrier
ds_read_b128 a[0:3], v16 offset:0
ds_read_b64_tr_b16 a[32:33], v18 offset:0
ds_read_b64_tr_b16 a[34:35], v19 offset:0
ds_read_b64_tr_b16 a[48:49], v18 offset:16384
ds_read_b64_tr_b16 a[50:51], v19 offset:16384
ds_read_b64_tr_b16 a[36:37], v20 offset:0
ds_read_b64_tr_b16 a[38:39], v21 offset:0
ds_read_b64_tr_b16 a[52:53], v20 offset:16384
ds_read_b64_tr_b16 a[54:55], v21 offset:16384
ds_read_b64_tr_b16 a[40:41], v18 offset:512
ds_read_b64_tr_b16 a[42:43], v19 offset:512
ds_read_b64_tr_b16 a[56:57], v18 offset:16896
ds_read_b64_tr_b16 a[58:59], v19 offset:16896
ds_read_b64_tr_b16 a[44:45], v20 offset:512
ds_read_b64_tr_b16 a[46:47], v21 offset:512
ds_read_b64_tr_b16 a[60:61], v20 offset:16896
ds_read_b64_tr_b16 a[62:63], v21 offset:16896
ds_read_b128 a[4:7], v16 offset:2048
ds_read_b128 a[8:11], v16 offset:4096
ds_read_b128 a[12:15], v16 offset:6144
ds_read_b128 a[16:19], v16 offset:8192
ds_read_b128 a[20:23], v16 offset:10240
ds_read_b128 a[24:27], v16 offset:12288
ds_read_b128 a[28:31], v16 offset:14336
v_xor_b32 v16, 0x10000, v16
s_waitcnt lgkmcnt(0)
v_mfma_f32_16x16x32_bf16 a[128:131], a[32:35], a[0:3], 0
ds_read_b128 a[64:67], v17 offset:0
v_mfma_f32_16x16x32_bf16 a[144:147], a[0:3], a[48:51], 0
buffer_load_dwordx4 v14, s[48:51], s65 offen offset:2048 lds
v_mfma_f32_16x16x32_bf16 a[132:135], a[36:39], a[0:3], 0
ds_read_b64_tr_b16 a[96:97], v18 offset:8192
v_mfma_f32_16x16x32_bf16 a[148:151], a[0:3], a[52:55], 0
v_mfma_f32_16x16x32_bf16 a[136:139], a[40:43], a[0:3], 0
ds_read_b64_tr_b16 a[98:99], v19 offset:8192
v_mfma_f32_16x16x32_bf16 a[152:155], a[0:3], a[56:59], 0
v_mfma_f32_16x16x32_bf16 a[140:143], a[44:47], a[0:3], 0
ds_read_b64_tr_b16 a[112:113], v18 offset:24576
v_mfma_f32_16x16x32_bf16 a[156:159], a[0:3], a[60:63], 0
buffer_load_dwordx4 v15, s[48:51], s65 offen offset:3072 lds
v_mfma_f32_16x16x32_bf16 v[32:35], a[32:35], a[4:7], 0
ds_read_b64_tr_b16 a[114:115], v19 offset:24576
v_mfma_f32_16x16x32_bf16 v[48:51], a[4:7], a[48:51], 0
v_mfma_f32_16x16x32_bf16 v[36:39], a[36:39], a[4:7], 0
ds_read_b64_tr_b16 a[100:101], v20 offset:8192
v_mfma_f32_16x16x32_bf16 v[52:55], a[4:7], a[52:55], 0
v_mfma_f32_16x16x32_bf16 v[40:43], a[40:43], a[4:7], 0
s_add_u32 m0, s67, 16384
ds_read_b64_tr_b16 a[102:103], v21 offset:8192
v_mfma_f32_16x16x32_bf16 v[56:59], a[4:7], a[56:59], 0
buffer_load_dwordx4 v12, s[52:55], s65 offen offset:0 lds
v_mfma_f32_16x16x32_bf16 v[44:47], a[44:47], a[4:7], 0
ds_read_b64_tr_b16 a[116:117], v20 offset:24576
v_mfma_f32_16x16x32_bf16 v[60:63], a[4:7], a[60:63], 0
v_mfma_f32_16x16x32_bf16 v[64:67], a[32:35], a[8:11], 0
ds_read_b64_tr_b16 a[118:119], v21 offset:24576
v_mfma_f32_16x16x32_bf16 v[80:83], a[8:11], a[48:51], 0
v_mfma_f32_16x16x32_bf16 v[68:71], a[36:39], a[8:11], 0
ds_read_b64_tr_b16 a[104:105], v18 offset:8704
v_mfma_f32_16x16x32_bf16 v[84:87], a[8:11], a[52:55], 0
buffer_load_dwordx4 v13, s[52:55], s65 offen offset:1024 lds
v_mfma_f32_16x16x32_bf16 v[72:75], a[40:43], a[8:11], 0
ds_read_b64_tr_b16 a[106:107], v19 offset:8704
v_mfma_f32_16x16x32_bf16 v[88:91], a[8:11], a[56:59], 0
v_mfma_f32_16x16x32_bf16 v[76:79], a[44:47], a[8:11], 0
ds_read_b64_tr_b16 a[120:121], v18 offset:25088
v_mfma_f32_16x16x32_bf16 v[92:95], a[8:11], a[60:63], 0
v_mfma_f32_16x16x32_bf16 v[96:99], a[32:35], a[12:15], 0
ds_read_b64_tr_b16 a[122:123], v19 offset:25088
v_mfma_f32_16x16x32_bf16 v[112:115], a[12:15], a[48:51], 0
buffer_load_dwordx4 v14, s[52:55], s65 offen offset:2048 lds
v_mfma_f32_16x16x32_bf16 v[100:103], a[36:39], a[12:15], 0
ds_read_b64_tr_b16 a[108:109], v20 offset:8704
v_mfma_f32_16x16x32_bf16 v[116:119], a[12:15], a[52:55], 0
v_mfma_f32_16x16x32_bf16 v[104:107], a[40:43], a[12:15], 0
ds_read_b64_tr_b16 a[110:111], v21 offset:8704
v_mfma_f32_16x16x32_bf16 v[120:123], a[12:15], a[56:59], 0
v_mfma_f32_16x16x32_bf16 v[108:111], a[44:47], a[12:15], 0
ds_read_b64_tr_b16 a[124:125], v20 offset:25088
v_mfma_f32_16x16x32_bf16 v[124:127], a[12:15], a[60:63], 0
buffer_load_dwordx4 v15, s[52:55], s65 offen offset:3072 lds
v_mfma_f32_16x16x32_bf16 v[128:131], a[32:35], a[16:19], 0
ds_read_b64_tr_b16 a[126:127], v21 offset:25088
v_mfma_f32_16x16x32_bf16 v[144:147], a[16:19], a[48:51], 0
v_mfma_f32_16x16x32_bf16 v[132:135], a[36:39], a[16:19], 0
ds_read_b128 a[68:71], v17 offset:2048
v_mfma_f32_16x16x32_bf16 v[148:151], a[16:19], a[52:55], 0
v_mfma_f32_16x16x32_bf16 v[136:139], a[40:43], a[16:19], 0
ds_read_b128 a[72:75], v17 offset:4096
v_mfma_f32_16x16x32_bf16 v[152:155], a[16:19], a[56:59], 0
s_add_u32 s64, s64, 128
v_mfma_f32_16x16x32_bf16 v[140:143], a[44:47], a[16:19], 0
s_add_u32 s65, s65, 524288
ds_read_b128 a[76:79], v17 offset:6144
v_mfma_f32_16x16x32_bf16 v[156:159], a[16:19], a[60:63], 0
s_and_b32 s64, s64, 0x3ff
v_mfma_f32_16x16x32_bf16 v[160:163], a[32:35], a[20:23], 0
s_and_b32 s65, s65, 0x3fffff
ds_read_b128 a[80:83], v17 offset:8192
v_mfma_f32_16x16x32_bf16 v[176:179], a[20:23], a[48:51], 0
s_xor_b32 s66, s66, 0x10000
v_mfma_f32_16x16x32_bf16 v[164:167], a[36:39], a[20:23], 0
s_xor_b32 s67, s67, 0x10000
ds_read_b128 a[84:87], v17 offset:10240
v_mfma_f32_16x16x32_bf16 v[180:183], a[20:23], a[52:55], 0
v_mfma_f32_16x16x32_bf16 v[168:171], a[40:43], a[20:23], 0
ds_read_b128 a[88:91], v17 offset:12288
v_mfma_f32_16x16x32_bf16 v[184:187], a[20:23], a[56:59], 0
v_mfma_f32_16x16x32_bf16 v[172:175], a[44:47], a[20:23], 0
ds_read_b128 a[92:95], v17 offset:14336
v_mfma_f32_16x16x32_bf16 v[188:191], a[20:23], a[60:63], 0
v_mfma_f32_16x16x32_bf16 v[192:195], a[32:35], a[24:27], 0
v_mfma_f32_16x16x32_bf16 v[208:211], a[24:27], a[48:51], 0
v_mfma_f32_16x16x32_bf16 v[196:199], a[36:39], a[24:27], 0
v_mfma_f32_16x16x32_bf16 v[212:215], a[24:27], a[52:55], 0
v_mfma_f32_16x16x32_bf16 v[200:203], a[40:43], a[24:27], 0
v_mfma_f32_16x16x32_bf16 v[216:219], a[24:27], a[56:59], 0
v_mfma_f32_16x16x32_bf16 v[204:207], a[44:47], a[24:27], 0
v_mfma_f32_16x16x32_bf16 v[220:223], a[24:27], a[60:63], 0
s_waitcnt vmcnt(0) lgkmcnt(0)
s_barrier
v_mfma_f32_16x16x32_bf16 v[224:227], a[32:35], a[28:31], 0
s_add_u32 m0, s66, 0
v_xor_b32 v18, 0x10000, v18
v_mfma_f32_16x16x32_bf16 v[240:243], a[28:31], a[48:51], 0
buffer_load_dwordx4 v4, s[44:47], s64 offen offset:0 lds
v_xor_b32 v19, 0x10000, v19
v_mfma_f32_16x16x32_bf16 v[228:231], a[36:39], a[28:31], 0
v_xor_b32 v20, 0x10000, v20
v_mfma_f32_16x16x32_bf16 v[244:247], a[28:31], a[52:55], 0
v_xor_b32 v21, 0x10000, v21
v_mfma_f32_16x16x32_bf16 v[232:235], a[40:43], a[28:31], 0
v_xor_b32 v17, 0x10000, v17
v_mfma_f32_16x16x32_bf16 v[248:251], a[28:31], a[56:59], 0
buffer_load_dwordx4 v5, s[44:47], s64 offen offset:1024 lds
v_mfma_f32_16x16x32_bf16 v[236:239], a[44:47], a[28:31], 0
v_mfma_f32_16x16x32_bf16 v[252:255], a[28:31], a[60:63], 0
v_mfma_f32_16x16x32_bf16 a[128:131], a[96:99], a[64:67], a[128:131]
ds_read_b128 a[0:3], v16 offset:0
v_mfma_f32_16x16x32_bf16 a[144:147], a[64:67], a[112:115], a[144:147]
buffer_load_dwordx4 v6, s[44:47], s64 offen offset:2048 lds
v_mfma_f32_16x16x32_bf16 a[132:135], a[100:103], a[64:67], a[132:135]
ds_read_b64_tr_b16 a[32:33], v18 offset:0
v_mfma_f32_16x16x32_bf16 a[148:151], a[64:67], a[116:119], a[148:151]
v_mfma_f32_16x16x32_bf16 a[136:139], a[104:107], a[64:67], a[136:139]
ds_read_b64_tr_b16 a[34:35], v19 offset:0
v_mfma_f32_16x16x32_bf16 a[152:155], a[64:67], a[120:123], a[152:155]
buffer_load_dwordx4 v7, s[44:47], s64 offen offset:3072 lds
v_mfma_f32_16x16x32_bf16 a[140:143], a[108:111], a[64:67], a[140:143]
ds_read_b64_tr_b16 a[48:49], v18 offset:16384
v_mul_u32_u24 v31, 43, v2
v_mfma_f32_16x16x32_bf16 a[156:159], a[64:67], a[124:127], a[156:159]
v_lshrrev_b32 v31, 10, v31
v_mul_u32_u24 v30, 24, v31
v_mfma_f32_16x16x32_bf16 v[32:35], a[96:99], a[68:71], v[32:35]
s_add_u32 m0, s66, 4096
ds_read_b64_tr_b16 a[50:51], v19 offset:16384
v_sub_u32 v30, v2, v30
v_mfma_f32_16x16x32_bf16 v[48:51], a[68:71], a[112:115], v[48:51]
buffer_load_dwordx4 v8, s[44:47], s64 offen offset:0 lds
v_mfma_f32_16x16x32_bf16 v[36:39], a[100:103], a[68:71], v[36:39]
ds_read_b64_tr_b16 a[36:37], v20 offset:0
v_lshl_add_u32 v30, v30, 3, v31
v_mfma_f32_16x16x32_bf16 v[52:55], a[68:71], a[116:119], v[52:55]
v_lshlrev_b32 v30, 7, v30
v_mfma_f32_16x16x32_bf16 v[40:43], a[104:107], a[68:71], v[40:43]
ds_read_b64_tr_b16 a[38:39], v21 offset:0
v_mfma_f32_16x16x32_bf16 v[56:59], a[68:71], a[120:123], v[56:59]
buffer_load_dwordx4 v9, s[44:47], s64 offen offset:1024 lds
v_mfma_f32_16x16x32_bf16 v[44:47], a[108:111], a[68:71], v[44:47]
ds_read_b64_tr_b16 a[52:53], v20 offset:16384
v_mfma_f32_16x16x32_bf16 v[60:63], a[68:71], a[124:127], v[60:63]
v_mfma_f32_16x16x32_bf16 v[64:67], a[96:99], a[72:75], v[64:67]
ds_read_b64_tr_b16 a[54:55], v21 offset:16384
v_mfma_f32_16x16x32_bf16 v[80:83], a[72:75], a[112:115], v[80:83]
buffer_load_dwordx4 v10, s[44:47], s64 offen offset:2048 lds
v_mfma_f32_16x16x32_bf16 v[68:71], a[100:103], a[72:75], v[68:71]
ds_read_b64_tr_b16 a[40:41], v18 offset:512
v_mfma_f32_16x16x32_bf16 v[84:87], a[72:75], a[116:119], v[84:87]
v_mfma_f32_16x16x32_bf16 v[72:75], a[104:107], a[72:75], v[72:75]
ds_read_b64_tr_b16 a[42:43], v19 offset:512
v_mfma_f32_16x16x32_bf16 v[88:91], a[72:75], a[120:123], v[88:91]
buffer_load_dwordx4 v11, s[44:47], s64 offen offset:3072 lds
v_mfma_f32_16x16x32_bf16 v[76:79], a[108:111], a[72:75], v[76:79]
ds_read_b64_tr_b16 a[56:57], v18 offset:16896
v_mfma_f32_16x16x32_bf16 v[92:95], a[72:75], a[124:127], v[92:95]
v_mfma_f32_16x16x32_bf16 v[96:99], a[96:99], a[76:79], v[96:99]
s_add_u32 m0, s67, 0
ds_read_b64_tr_b16 a[58:59], v19 offset:16896
v_mfma_f32_16x16x32_bf16 v[112:115], a[76:79], a[112:115], v[112:115]
buffer_load_dwordx4 v12, s[48:51], s65 offen offset:0 lds
v_mfma_f32_16x16x32_bf16 v[100:103], a[100:103], a[76:79], v[100:103]
ds_read_b64_tr_b16 a[44:45], v20 offset:512
v_mfma_f32_16x16x32_bf16 v[116:119], a[76:79], a[116:119], v[116:119]
v_mfma_f32_16x16x32_bf16 v[104:107], a[104:107], a[76:79], v[104:107]
ds_read_b64_tr_b16 a[46:47], v21 offset:512
v_mfma_f32_16x16x32_bf16 v[120:123], a[76:79], a[120:123], v[120:123]
buffer_load_dwordx4 v13, s[48:51], s65 offen offset:1024 lds
v_mfma_f32_16x16x32_bf16 v[108:111], a[108:111], a[76:79], v[108:111]
ds_read_b64_tr_b16 a[60:61], v20 offset:16896
v_mfma_f32_16x16x32_bf16 v[124:127], a[76:79], a[124:127], v[124:127]
v_mfma_f32_16x16x32_bf16 v[128:131], a[96:99], a[80:83], v[128:131]
ds_read_b64_tr_b16 a[62:63], v21 offset:16896
v_mfma_f32_16x16x32_bf16 v[144:147], a[80:83], a[112:115], v[144:147]
buffer_load_dwordx4 v14, s[48:51], s65 offen offset:2048 lds
v_mfma_f32_16x16x32_bf16 v[132:135], a[100:103], a[80:83], v[132:135]
ds_read_b128 a[4:7], v16 offset:2048
v_mfma_f32_16x16x32_bf16 v[148:151], a[80:83], a[116:119], v[148:151]
v_mfma_f32_16x16x32_bf16 v[136:139], a[104:107], a[80:83], v[136:139]
ds_read_b128 a[8:11], v16 offset:4096
v_mfma_f32_16x16x32_bf16 v[152:155], a[80:83], a[120:123], v[152:155]
buffer_load_dwordx4 v15, s[48:51], s65 offen offset:3072 lds
v_mfma_f32_16x16x32_bf16 v[140:143], a[108:111], a[80:83], v[140:143]
ds_read_b128 a[12:15], v16 offset:6144
v_mfma_f32_16x16x32_bf16 v[156:159], a[80:83], a[124:127], v[156:159]
v_mfma_f32_16x16x32_bf16 v[160:163], a[96:99], a[84:87], v[160:163]
s_add_u32 m0, s67, 16384
ds_read_b128 a[16:19], v16 offset:8192
v_mfma_f32_16x16x32_bf16 v[176:179], a[84:87], a[112:115], v[176:179]
buffer_load_dwordx4 v12, s[52:55], s65 offen offset:0 lds
v_mfma_f32_16x16x32_bf16 v[164:167], a[100:103], a[84:87], v[164:167]
)ASM"
R"ASM(ds_read_b128 a[20:23], v16 offset:10240
v_mfma_f32_16x16x32_bf16 v[180:183], a[84:87], a[116:119], v[180:183]
v_mfma_f32_16x16x32_bf16 v[168:171], a[104:107], a[84:87], v[168:171]
ds_read_b128 a[24:27], v16 offset:12288
v_mfma_f32_16x16x32_bf16 v[184:187], a[84:87], a[120:123], v[184:187]
buffer_load_dwordx4 v13, s[52:55], s65 offen offset:1024 lds
v_mfma_f32_16x16x32_bf16 v[172:175], a[108:111], a[84:87], v[172:175]
ds_read_b128 a[28:31], v16 offset:14336
v_mfma_f32_16x16x32_bf16 v[188:191], a[84:87], a[124:127], v[188:191]
v_mfma_f32_16x16x32_bf16 v[192:195], a[96:99], a[88:91], v[192:195]
v_mfma_f32_16x16x32_bf16 v[208:211], a[88:91], a[112:115], v[208:211]
buffer_load_dwordx4 v14, s[52:55], s65 offen offset:2048 lds
v_mfma_f32_16x16x32_bf16 v[196:199], a[100:103], a[88:91], v[196:199]
v_mfma_f32_16x16x32_bf16 v[212:215], a[88:91], a[116:119], v[212:215]
v_mfma_f32_16x16x32_bf16 v[200:203], a[104:107], a[88:91], v[200:203]
v_mfma_f32_16x16x32_bf16 v[216:219], a[88:91], a[120:123], v[216:219]
buffer_load_dwordx4 v15, s[52:55], s65 offen offset:3072 lds
v_mfma_f32_16x16x32_bf16 v[204:207], a[108:111], a[88:91], v[204:207]
s_add_u32 s64, s64, 128
s_add_u32 s65, s65, 524288
v_mfma_f32_16x16x32_bf16 v[220:223], a[88:91], a[124:127], v[220:223]
s_and_b32 s64, s64, 0x3ff
s_and_b32 s65, s65, 0x3fffff
v_mfma_f32_16x16x32_bf16 v[224:227], a[96:99], a[92:95], v[224:227]
s_xor_b32 s66, s66, 0x10000
s_xor_b32 s67, s67, 0x10000
v_mfma_f32_16x16x32_bf16 v[240:243], a[92:95], a[112:115], v[240:243]
v_mfma_f32_16x16x32_bf16 v[228:231], a[100:103], a[92:95], v[228:231]
v_mfma_f32_16x16x32_bf16 v[244:247], a[92:95], a[116:119], v[244:247]
v_mfma_f32_16x16x32_bf16 v[232:235], a[104:107], a[92:95], v[232:235]
v_xor_b32 v16, 0x10000, v16
v_mfma_f32_16x16x32_bf16 v[248:251], a[92:95], a[120:123], v[248:251]
v_mfma_f32_16x16x32_bf16 v[236:239], a[108:111], a[92:95], v[236:239]
v_mfma_f32_16x16x32_bf16 v[252:255], a[92:95], a[124:127], v[252:255]
s_waitcnt lgkmcnt(15)
v_mfma_f32_16x16x32_bf16 a[128:131], a[32:35], a[0:3], a[128:131]
ds_read_b128 a[64:67], v17 offset:0
v_mfma_f32_16x16x32_bf16 a[144:147], a[0:3], a[48:51], a[144:147]
s_lshl_b32 s70, s39, 12
v_mfma_f32_16x16x32_bf16 a[132:135], a[36:39], a[0:3], a[132:135]
ds_read_b64_tr_b16 a[96:97], v18 offset:8192
v_mfma_f32_16x16x32_bf16 a[148:151], a[0:3], a[52:55], a[148:151]
s_mov_b32 s71, 0
s_waitcnt lgkmcnt(15)
v_mfma_f32_16x16x32_bf16 a[136:139], a[40:43], a[0:3], a[136:139]
s_add_u32 m0, s70, 131072
ds_read_b64_tr_b16 a[98:99], v19 offset:8192
s_waitcnt lgkmcnt(14)
v_mfma_f32_16x16x32_bf16 a[152:155], a[0:3], a[56:59], a[152:155]
buffer_load_dwordx4 v22, s[56:59], s71 offen lds
s_waitcnt lgkmcnt(12)
v_mfma_f32_16x16x32_bf16 a[140:143], a[44:47], a[0:3], a[140:143]
ds_read_b64_tr_b16 a[112:113], v18 offset:24576
s_waitcnt lgkmcnt(11)
v_mfma_f32_16x16x32_bf16 a[156:159], a[0:3], a[60:63], a[156:159]
s_waitcnt lgkmcnt(10)
v_mfma_f32_16x16x32_bf16 v[32:35], a[32:35], a[4:7], v[32:35]
ds_read_b64_tr_b16 a[114:115], v19 offset:24576
v_mfma_f32_16x16x32_bf16 v[48:51], a[4:7], a[48:51], v[48:51]
v_mfma_f32_16x16x32_bf16 v[36:39], a[36:39], a[4:7], v[36:39]
ds_read_b64_tr_b16 a[100:101], v20 offset:8192
v_mfma_f32_16x16x32_bf16 v[52:55], a[4:7], a[52:55], v[52:55]
v_mfma_f32_16x16x32_bf16 v[40:43], a[40:43], a[4:7], v[40:43]
ds_read_b64_tr_b16 a[102:103], v21 offset:8192
v_mfma_f32_16x16x32_bf16 v[56:59], a[4:7], a[56:59], v[56:59]
v_mfma_f32_16x16x32_bf16 v[44:47], a[44:47], a[4:7], v[44:47]
ds_read_b64_tr_b16 a[116:117], v20 offset:24576
v_mfma_f32_16x16x32_bf16 v[60:63], a[4:7], a[60:63], v[60:63]
s_mov_b32 s71, 8192
s_waitcnt lgkmcnt(13)
v_mfma_f32_16x16x32_bf16 v[64:67], a[32:35], a[8:11], v[64:67]
s_add_u32 m0, s70, 132096
ds_read_b64_tr_b16 a[118:119], v21 offset:24576
v_mfma_f32_16x16x32_bf16 v[80:83], a[8:11], a[48:51], v[80:83]
buffer_load_dwordx4 v22, s[56:59], s71 offen lds
v_mfma_f32_16x16x32_bf16 v[68:71], a[36:39], a[8:11], v[68:71]
ds_read_b64_tr_b16 a[104:105], v18 offset:8704
v_mfma_f32_16x16x32_bf16 v[84:87], a[8:11], a[52:55], v[84:87]
v_mfma_f32_16x16x32_bf16 v[72:75], a[40:43], a[8:11], v[72:75]
ds_read_b64_tr_b16 a[106:107], v19 offset:8704
v_mfma_f32_16x16x32_bf16 v[88:91], a[8:11], a[56:59], v[88:91]
v_mfma_f32_16x16x32_bf16 v[76:79], a[44:47], a[8:11], v[76:79]
ds_read_b64_tr_b16 a[120:121], v18 offset:25088
v_mfma_f32_16x16x32_bf16 v[92:95], a[8:11], a[60:63], v[92:95]
s_waitcnt lgkmcnt(15)
v_mfma_f32_16x16x32_bf16 v[96:99], a[32:35], a[12:15], v[96:99]
ds_read_b64_tr_b16 a[122:123], v19 offset:25088
v_mfma_f32_16x16x32_bf16 v[112:115], a[12:15], a[48:51], v[112:115]
v_mfma_f32_16x16x32_bf16 v[100:103], a[36:39], a[12:15], v[100:103]
ds_read_b64_tr_b16 a[108:109], v20 offset:8704
v_mfma_f32_16x16x32_bf16 v[116:119], a[12:15], a[52:55], v[116:119]
s_mov_b32 s71, 16384
v_mfma_f32_16x16x32_bf16 v[104:107], a[40:43], a[12:15], v[104:107]
s_add_u32 m0, s70, 133120
ds_read_b64_tr_b16 a[110:111], v21 offset:8704
v_mfma_f32_16x16x32_bf16 v[120:123], a[12:15], a[56:59], v[120:123]
buffer_load_dwordx4 v22, s[56:59], s71 offen lds
v_mfma_f32_16x16x32_bf16 v[108:111], a[44:47], a[12:15], v[108:111]
ds_read_b64_tr_b16 a[124:125], v20 offset:25088
v_mfma_f32_16x16x32_bf16 v[124:127], a[12:15], a[60:63], v[124:127]
v_mfma_f32_16x16x32_bf16 v[128:131], a[32:35], a[16:19], v[128:131]
ds_read_b64_tr_b16 a[126:127], v21 offset:25088
v_mfma_f32_16x16x32_bf16 v[144:147], a[16:19], a[48:51], v[144:147]
v_mfma_f32_16x16x32_bf16 v[132:135], a[36:39], a[16:19], v[132:135]
ds_read_b128 a[68:71], v17 offset:2048
v_mfma_f32_16x16x32_bf16 v[148:151], a[16:19], a[52:55], v[148:151]
v_mfma_f32_16x16x32_bf16 v[136:139], a[40:43], a[16:19], v[136:139]
ds_read_b128 a[72:75], v17 offset:4096
v_mfma_f32_16x16x32_bf16 v[152:155], a[16:19], a[56:59], v[152:155]
v_mfma_f32_16x16x32_bf16 v[140:143], a[44:47], a[16:19], v[140:143]
ds_read_b128 a[76:79], v17 offset:6144
v_mfma_f32_16x16x32_bf16 v[156:159], a[16:19], a[60:63], v[156:159]
s_mov_b32 s71, 24576
s_waitcnt lgkmcnt(15)
v_mfma_f32_16x16x32_bf16 v[160:163], a[32:35], a[20:23], v[160:163]
s_add_u32 m0, s70, 134144
ds_read_b128 a[80:83], v17 offset:8192
v_mfma_f32_16x16x32_bf16 v[176:179], a[20:23], a[48:51], v[176:179]
buffer_load_dwordx4 v22, s[56:59], s71 offen lds
v_mfma_f32_16x16x32_bf16 v[164:167], a[36:39], a[20:23], v[164:167]
ds_read_b128 a[84:87], v17 offset:10240
v_mfma_f32_16x16x32_bf16 v[180:183], a[20:23], a[52:55], v[180:183]
v_mfma_f32_16x16x32_bf16 v[168:171], a[40:43], a[20:23], v[168:171]
ds_read_b128 a[88:91], v17 offset:12288
v_mfma_f32_16x16x32_bf16 v[184:187], a[20:23], a[56:59], v[184:187]
v_mfma_f32_16x16x32_bf16 v[172:175], a[44:47], a[20:23], v[172:175]
ds_read_b128 a[92:95], v17 offset:14336
v_mfma_f32_16x16x32_bf16 v[188:191], a[20:23], a[60:63], v[188:191]
v_mfma_f32_16x16x32_bf16 v[192:195], a[32:35], a[24:27], v[192:195]
v_mfma_f32_16x16x32_bf16 v[208:211], a[24:27], a[48:51], v[208:211]
v_mfma_f32_16x16x32_bf16 v[196:199], a[36:39], a[24:27], v[196:199]
v_mfma_f32_16x16x32_bf16 v[212:215], a[24:27], a[52:55], v[212:215]
v_mfma_f32_16x16x32_bf16 v[200:203], a[40:43], a[24:27], v[200:203]
v_mfma_f32_16x16x32_bf16 v[216:219], a[24:27], a[56:59], v[216:219]
v_mfma_f32_16x16x32_bf16 v[204:207], a[44:47], a[24:27], v[204:207]
v_mfma_f32_16x16x32_bf16 v[220:223], a[24:27], a[60:63], v[220:223]
s_waitcnt vmcnt(4) lgkmcnt(0)
s_barrier
v_mfma_f32_16x16x32_bf16 v[224:227], a[32:35], a[28:31], v[224:227]
s_add_u32 m0, s66, 0
v_xor_b32 v18, 0x10000, v18
v_mfma_f32_16x16x32_bf16 v[240:243], a[28:31], a[48:51], v[240:243]
buffer_load_dwordx4 v4, s[44:47], s64 offen offset:0 lds
v_xor_b32 v19, 0x10000, v19
v_mfma_f32_16x16x32_bf16 v[228:231], a[36:39], a[28:31], v[228:231]
v_xor_b32 v20, 0x10000, v20
v_mfma_f32_16x16x32_bf16 v[244:247], a[28:31], a[52:55], v[244:247]
s_mov_b32 s79, 0
buffer_load_dword v31, v30, s[40:43], s79 offen sc1
v_xor_b32 v21, 0x10000, v21
v_mfma_f32_16x16x32_bf16 v[232:235], a[40:43], a[28:31], v[232:235]
s_mov_b32 s72, s18
v_xor_b32 v17, 0x10000, v17
v_mfma_f32_16x16x32_bf16 v[248:251], a[28:31], a[56:59], v[248:251]
s_mov_b32 s73, s19
buffer_load_dwordx4 v5, s[44:47], s64 offen offset:1024 lds
v_mfma_f32_16x16x32_bf16 v[236:239], a[44:47], a[28:31], v[236:239]
s_and_b32 s73, s73, 0xffff
v_mfma_f32_16x16x32_bf16 v[252:255], a[28:31], a[60:63], v[252:255]
s_mov_b32 s74, 4194304
v_mfma_f32_16x16x32_bf16 a[128:131], a[96:99], a[64:67], a[128:131]
s_mov_b32 s75, 0x20000
ds_read_b128 a[0:3], v16 offset:0
v_mfma_f32_16x16x32_bf16 a[144:147], a[64:67], a[112:115], a[144:147]
buffer_load_dwordx4 v6, s[44:47], s64 offen offset:2048 lds
v_mfma_f32_16x16x32_bf16 a[132:135], a[100:103], a[64:67], a[132:135]
s_add_u32 s56, s30, 75497472
ds_read_b64_tr_b16 a[32:33], v18 offset:0
v_mfma_f32_16x16x32_bf16 a[148:151], a[64:67], a[116:119], a[148:151]
s_addc_u32 s57, s31, 0
v_mfma_f32_16x16x32_bf16 a[136:139], a[104:107], a[64:67], a[136:139]
s_and_b32 s57, s57, 0xffff
ds_read_b64_tr_b16 a[34:35], v19 offset:0
v_mfma_f32_16x16x32_bf16 a[152:155], a[64:67], a[120:123], a[152:155]
s_mov_b32 s58, 6029312
buffer_load_dwordx4 v7, s[44:47], s64 offen offset:3072 lds
v_mfma_f32_16x16x32_bf16 a[140:143], a[108:111], a[64:67], a[140:143]
s_mov_b32 s59, 0x20000
ds_read_b64_tr_b16 a[48:49], v18 offset:16384
v_mfma_f32_16x16x32_bf16 a[156:159], a[64:67], a[124:127], a[156:159]
v_mfma_f32_16x16x32_bf16 v[32:35], a[96:99], a[68:71], v[32:35]
s_add_u32 m0, s66, 4096
ds_read_b64_tr_b16 a[50:51], v19 offset:16384
v_mfma_f32_16x16x32_bf16 v[48:51], a[68:71], a[112:115], v[48:51]
buffer_load_dwordx4 v8, s[44:47], s64 offen offset:0 lds
v_mfma_f32_16x16x32_bf16 v[36:39], a[100:103], a[68:71], v[36:39]
ds_read_b64_tr_b16 a[36:37], v20 offset:0
v_mfma_f32_16x16x32_bf16 v[52:55], a[68:71], a[116:119], v[52:55]
v_mfma_f32_16x16x32_bf16 v[40:43], a[104:107], a[68:71], v[40:43]
ds_read_b64_tr_b16 a[38:39], v21 offset:0
v_mfma_f32_16x16x32_bf16 v[56:59], a[68:71], a[120:123], v[56:59]
buffer_load_dwordx4 v9, s[44:47], s64 offen offset:1024 lds
v_mfma_f32_16x16x32_bf16 v[44:47], a[108:111], a[68:71], v[44:47]
ds_read_b64_tr_b16 a[52:53], v20 offset:16384
v_mfma_f32_16x16x32_bf16 v[60:63], a[68:71], a[124:127], v[60:63]
v_mfma_f32_16x16x32_bf16 v[64:67], a[96:99], a[72:75], v[64:67]
ds_read_b64_tr_b16 a[54:55], v21 offset:16384
v_mfma_f32_16x16x32_bf16 v[80:83], a[72:75], a[112:115], v[80:83]
buffer_load_dwordx4 v10, s[44:47], s64 offen offset:2048 lds
v_mfma_f32_16x16x32_bf16 v[68:71], a[100:103], a[72:75], v[68:71]
ds_read_b64_tr_b16 a[40:41], v18 offset:512
v_mfma_f32_16x16x32_bf16 v[84:87], a[72:75], a[116:119], v[84:87]
v_mfma_f32_16x16x32_bf16 v[72:75], a[104:107], a[72:75], v[72:75]
ds_read_b64_tr_b16 a[42:43], v19 offset:512
v_mfma_f32_16x16x32_bf16 v[88:91], a[72:75], a[120:123], v[88:91]
buffer_load_dwordx4 v11, s[44:47], s64 offen offset:3072 lds
v_mfma_f32_16x16x32_bf16 v[76:79], a[108:111], a[72:75], v[76:79]
ds_read_b64_tr_b16 a[56:57], v18 offset:16896
v_mfma_f32_16x16x32_bf16 v[92:95], a[72:75], a[124:127], v[92:95]
v_mfma_f32_16x16x32_bf16 v[96:99], a[96:99], a[76:79], v[96:99]
s_add_u32 m0, s67, 0
ds_read_b64_tr_b16 a[58:59], v19 offset:16896
v_mfma_f32_16x16x32_bf16 v[112:115], a[76:79], a[112:115], v[112:115]
buffer_load_dwordx4 v12, s[48:51], s65 offen offset:0 lds
v_mfma_f32_16x16x32_bf16 v[100:103], a[100:103], a[76:79], v[100:103]
ds_read_b64_tr_b16 a[44:45], v20 offset:512
v_mfma_f32_16x16x32_bf16 v[116:119], a[76:79], a[116:119], v[116:119]
v_mfma_f32_16x16x32_bf16 v[104:107], a[104:107], a[76:79], v[104:107]
ds_read_b64_tr_b16 a[46:47], v21 offset:512
v_mfma_f32_16x16x32_bf16 v[120:123], a[76:79], a[120:123], v[120:123]
buffer_load_dwordx4 v13, s[48:51], s65 offen offset:1024 lds
v_mfma_f32_16x16x32_bf16 v[108:111], a[108:111], a[76:79], v[108:111]
ds_read_b64_tr_b16 a[60:61], v20 offset:16896
v_mfma_f32_16x16x32_bf16 v[124:127], a[76:79], a[124:127], v[124:127]
v_mfma_f32_16x16x32_bf16 v[128:131], a[96:99], a[80:83], v[128:131]
ds_read_b64_tr_b16 a[62:63], v21 offset:16896
v_mfma_f32_16x16x32_bf16 v[144:147], a[80:83], a[112:115], v[144:147]
buffer_load_dwordx4 v14, s[48:51], s65 offen offset:2048 lds
v_mfma_f32_16x16x32_bf16 v[132:135], a[100:103], a[80:83], v[132:135]
ds_read_b128 a[4:7], v16 offset:2048
v_mfma_f32_16x16x32_bf16 v[148:151], a[80:83], a[116:119], v[148:151]
v_mfma_f32_16x16x32_bf16 v[136:139], a[104:107], a[80:83], v[136:139]
ds_read_b128 a[8:11], v16 offset:4096
v_mfma_f32_16x16x32_bf16 v[152:155], a[80:83], a[120:123], v[152:155]
buffer_load_dwordx4 v15, s[48:51], s65 offen offset:3072 lds
v_mfma_f32_16x16x32_bf16 v[140:143], a[108:111], a[80:83], v[140:143]
ds_read_b128 a[12:15], v16 offset:6144
v_mfma_f32_16x16x32_bf16 v[156:159], a[80:83], a[124:127], v[156:159]
v_mfma_f32_16x16x32_bf16 v[160:163], a[96:99], a[84:87], v[160:163]
s_add_u32 m0, s67, 16384
ds_read_b128 a[16:19], v16 offset:8192
v_mfma_f32_16x16x32_bf16 v[176:179], a[84:87], a[112:115], v[176:179]
buffer_load_dwordx4 v12, s[52:55], s65 offen offset:0 lds
v_mfma_f32_16x16x32_bf16 v[164:167], a[100:103], a[84:87], v[164:167]
ds_read_b128 a[20:23], v16 offset:10240
v_mfma_f32_16x16x32_bf16 v[180:183], a[84:87], a[116:119], v[180:183]
v_mfma_f32_16x16x32_bf16 v[168:171], a[104:107], a[84:87], v[168:171]
ds_read_b128 a[24:27], v16 offset:12288
v_mfma_f32_16x16x32_bf16 v[184:187], a[84:87], a[120:123], v[184:187]
buffer_load_dwordx4 v13, s[52:55], s65 offen offset:1024 lds
v_mfma_f32_16x16x32_bf16 v[172:175], a[108:111], a[84:87], v[172:175]
ds_read_b128 a[28:31], v16 offset:14336
v_mfma_f32_16x16x32_bf16 v[188:191], a[84:87], a[124:127], v[188:191]
v_mfma_f32_16x16x32_bf16 v[192:195], a[96:99], a[88:91], v[192:195]
v_mfma_f32_16x16x32_bf16 v[208:211], a[88:91], a[112:115], v[208:211]
buffer_load_dwordx4 v14, s[52:55], s65 offen offset:2048 lds
v_mfma_f32_16x16x32_bf16 v[196:199], a[100:103], a[88:91], v[196:199]
v_mfma_f32_16x16x32_bf16 v[212:215], a[88:91], a[116:119], v[212:215]
v_mfma_f32_16x16x32_bf16 v[200:203], a[104:107], a[88:91], v[200:203]
v_mfma_f32_16x16x32_bf16 v[216:219], a[88:91], a[120:123], v[216:219]
buffer_load_dwordx4 v15, s[52:55], s65 offen offset:3072 lds
v_mfma_f32_16x16x32_bf16 v[204:207], a[108:111], a[88:91], v[204:207]
s_add_u32 s64, s64, 128
s_add_u32 s65, s65, 524288
v_mfma_f32_16x16x32_bf16 v[220:223], a[88:91], a[124:127], v[220:223]
s_and_b32 s64, s64, 0x3ff
s_and_b32 s65, s65, 0x3fffff
v_mfma_f32_16x16x32_bf16 v[224:227], a[96:99], a[92:95], v[224:227]
s_xor_b32 s66, s66, 0x10000
s_xor_b32 s67, s67, 0x10000
v_mfma_f32_16x16x32_bf16 v[240:243], a[92:95], a[112:115], v[240:243]
v_mfma_f32_16x16x32_bf16 v[228:231], a[100:103], a[92:95], v[228:231]
v_mfma_f32_16x16x32_bf16 v[244:247], a[92:95], a[116:119], v[244:247]
v_mfma_f32_16x16x32_bf16 v[232:235], a[104:107], a[92:95], v[232:235]
v_xor_b32 v16, 0x10000, v16
v_mfma_f32_16x16x32_bf16 v[248:251], a[92:95], a[120:123], v[248:251]
v_mfma_f32_16x16x32_bf16 v[236:239], a[108:111], a[92:95], v[236:239]
v_mfma_f32_16x16x32_bf16 v[252:255], a[92:95], a[124:127], v[252:255]
s_waitcnt lgkmcnt(15)
v_mfma_f32_16x16x32_bf16 a[128:131], a[32:35], a[0:3], a[128:131]
s_lshl_b32 s70, s39, 12
ds_read_b128 a[64:67], v17 offset:0
v_mfma_f32_16x16x32_bf16 a[144:147], a[0:3], a[48:51], a[144:147]
s_mov_b32 s71, 0
v_mfma_f32_16x16x32_bf16 a[132:135], a[36:39], a[0:3], a[132:135]
s_add_u32 m0, s70, 147456
ds_read_b64_tr_b16 a[96:97], v18 offset:8192
v_mfma_f32_16x16x32_bf16 a[148:151], a[0:3], a[52:55], a[148:151]
buffer_load_dwordx4 v3, s[72:75], s71 offen lds
s_waitcnt lgkmcnt(15)
v_mfma_f32_16x16x32_bf16 a[136:139], a[40:43], a[0:3], a[136:139]
ds_read_b64_tr_b16 a[98:99], v19 offset:8192
s_waitcnt lgkmcnt(14)
v_mfma_f32_16x16x32_bf16 a[152:155], a[0:3], a[56:59], a[152:155]
s_waitcnt lgkmcnt(12)
v_mfma_f32_16x16x32_bf16 a[140:143], a[44:47], a[0:3], a[140:143]
ds_read_b64_tr_b16 a[112:113], v18 offset:24576
s_waitcnt lgkmcnt(11)
v_mfma_f32_16x16x32_bf16 a[156:159], a[0:3], a[60:63], a[156:159]
s_waitcnt lgkmcnt(10)
)ASM"
R"ASM(v_mfma_f32_16x16x32_bf16 v[32:35], a[32:35], a[4:7], v[32:35]
ds_read_b64_tr_b16 a[114:115], v19 offset:24576
v_mfma_f32_16x16x32_bf16 v[48:51], a[4:7], a[48:51], v[48:51]
v_mfma_f32_16x16x32_bf16 v[36:39], a[36:39], a[4:7], v[36:39]
ds_read_b64_tr_b16 a[100:101], v20 offset:8192
v_mfma_f32_16x16x32_bf16 v[52:55], a[4:7], a[52:55], v[52:55]
s_mov_b32 s71, 8192
v_mfma_f32_16x16x32_bf16 v[40:43], a[40:43], a[4:7], v[40:43]
s_add_u32 m0, s70, 148480
ds_read_b64_tr_b16 a[102:103], v21 offset:8192
v_mfma_f32_16x16x32_bf16 v[56:59], a[4:7], a[56:59], v[56:59]
buffer_load_dwordx4 v3, s[72:75], s71 offen lds
v_mfma_f32_16x16x32_bf16 v[44:47], a[44:47], a[4:7], v[44:47]
ds_read_b64_tr_b16 a[116:117], v20 offset:24576
v_mfma_f32_16x16x32_bf16 v[60:63], a[4:7], a[60:63], v[60:63]
s_waitcnt lgkmcnt(13)
v_mfma_f32_16x16x32_bf16 v[64:67], a[32:35], a[8:11], v[64:67]
ds_read_b64_tr_b16 a[118:119], v21 offset:24576
v_mfma_f32_16x16x32_bf16 v[80:83], a[8:11], a[48:51], v[80:83]
v_mfma_f32_16x16x32_bf16 v[68:71], a[36:39], a[8:11], v[68:71]
ds_read_b64_tr_b16 a[104:105], v18 offset:8704
v_mfma_f32_16x16x32_bf16 v[84:87], a[8:11], a[52:55], v[84:87]
v_mfma_f32_16x16x32_bf16 v[72:75], a[40:43], a[8:11], v[72:75]
ds_read_b64_tr_b16 a[106:107], v19 offset:8704
v_mfma_f32_16x16x32_bf16 v[88:91], a[8:11], a[56:59], v[88:91]
s_mov_b32 s71, 16384
v_mfma_f32_16x16x32_bf16 v[76:79], a[44:47], a[8:11], v[76:79]
s_add_u32 m0, s70, 149504
ds_read_b64_tr_b16 a[120:121], v18 offset:25088
v_mfma_f32_16x16x32_bf16 v[92:95], a[8:11], a[60:63], v[92:95]
buffer_load_dwordx4 v3, s[72:75], s71 offen lds
s_waitcnt lgkmcnt(15)
v_mfma_f32_16x16x32_bf16 v[96:99], a[32:35], a[12:15], v[96:99]
ds_read_b64_tr_b16 a[122:123], v19 offset:25088
v_mfma_f32_16x16x32_bf16 v[112:115], a[12:15], a[48:51], v[112:115]
v_mfma_f32_16x16x32_bf16 v[100:103], a[36:39], a[12:15], v[100:103]
ds_read_b64_tr_b16 a[108:109], v20 offset:8704
v_mfma_f32_16x16x32_bf16 v[116:119], a[12:15], a[52:55], v[116:119]
v_mfma_f32_16x16x32_bf16 v[104:107], a[40:43], a[12:15], v[104:107]
ds_read_b64_tr_b16 a[110:111], v21 offset:8704
v_mfma_f32_16x16x32_bf16 v[120:123], a[12:15], a[56:59], v[120:123]
v_mfma_f32_16x16x32_bf16 v[108:111], a[44:47], a[12:15], v[108:111]
ds_read_b64_tr_b16 a[124:125], v20 offset:25088
v_mfma_f32_16x16x32_bf16 v[124:127], a[12:15], a[60:63], v[124:127]
s_mov_b32 s71, 24576
v_mfma_f32_16x16x32_bf16 v[128:131], a[32:35], a[16:19], v[128:131]
s_add_u32 m0, s70, 150528
ds_read_b64_tr_b16 a[126:127], v21 offset:25088
v_mfma_f32_16x16x32_bf16 v[144:147], a[16:19], a[48:51], v[144:147]
buffer_load_dwordx4 v3, s[72:75], s71 offen lds
v_mfma_f32_16x16x32_bf16 v[132:135], a[36:39], a[16:19], v[132:135]
ds_read_b128 a[68:71], v17 offset:2048
v_mfma_f32_16x16x32_bf16 v[148:151], a[16:19], a[52:55], v[148:151]
v_mfma_f32_16x16x32_bf16 v[136:139], a[40:43], a[16:19], v[136:139]
ds_read_b128 a[72:75], v17 offset:4096
v_mfma_f32_16x16x32_bf16 v[152:155], a[16:19], a[56:59], v[152:155]
v_mfma_f32_16x16x32_bf16 v[140:143], a[44:47], a[16:19], v[140:143]
ds_read_b128 a[76:79], v17 offset:6144
v_mfma_f32_16x16x32_bf16 v[156:159], a[16:19], a[60:63], v[156:159]
s_waitcnt lgkmcnt(15)
v_mfma_f32_16x16x32_bf16 v[160:163], a[32:35], a[20:23], v[160:163]
s_add_u32 s72, s30, 52428800
ds_read_b128 a[80:83], v17 offset:8192
v_mfma_f32_16x16x32_bf16 v[176:179], a[20:23], a[48:51], v[176:179]
s_addc_u32 s73, s31, 0
v_mfma_f32_16x16x32_bf16 v[164:167], a[36:39], a[20:23], v[164:167]
s_and_b32 s73, s73, 0xffff
ds_read_b128 a[84:87], v17 offset:10240
v_mfma_f32_16x16x32_bf16 v[180:183], a[20:23], a[52:55], v[180:183]
s_mov_b32 s74, 12288
v_mfma_f32_16x16x32_bf16 v[168:171], a[40:43], a[20:23], v[168:171]
s_mov_b32 s75, 0x20000
ds_read_b128 a[88:91], v17 offset:12288
v_lshlrev_b32 v22, 1, v1
v_mfma_f32_16x16x32_bf16 v[184:187], a[20:23], a[56:59], v[184:187]
v_mfma_f32_16x16x32_bf16 v[172:175], a[44:47], a[20:23], v[172:175]
ds_read_b128 a[92:95], v17 offset:14336
v_mfma_f32_16x16x32_bf16 v[188:191], a[20:23], a[60:63], v[188:191]
v_mfma_f32_16x16x32_bf16 v[192:195], a[32:35], a[24:27], v[192:195]
v_mfma_f32_16x16x32_bf16 v[208:211], a[24:27], a[48:51], v[208:211]
v_mfma_f32_16x16x32_bf16 v[196:199], a[36:39], a[24:27], v[196:199]
v_mfma_f32_16x16x32_bf16 v[212:215], a[24:27], a[52:55], v[212:215]
v_mfma_f32_16x16x32_bf16 v[200:203], a[40:43], a[24:27], v[200:203]
v_mfma_f32_16x16x32_bf16 v[216:219], a[24:27], a[56:59], v[216:219]
v_mfma_f32_16x16x32_bf16 v[204:207], a[44:47], a[24:27], v[204:207]
v_mfma_f32_16x16x32_bf16 v[220:223], a[24:27], a[60:63], v[220:223]
s_waitcnt vmcnt(4) lgkmcnt(0)
s_barrier
v_mfma_f32_16x16x32_bf16 v[224:227], a[32:35], a[28:31], v[224:227]
s_add_u32 m0, s66, 0
v_xor_b32 v18, 0x10000, v18
v_cmp_ne_u32 vcc, s33, v31
v_mfma_f32_16x16x32_bf16 v[240:243], a[28:31], a[48:51], v[240:243]
buffer_load_dwordx4 v4, s[44:47], s64 offen offset:0 lds
v_xor_b32 v19, 0x10000, v19
v_mfma_f32_16x16x32_bf16 v[228:231], a[36:39], a[28:31], v[228:231]
v_xor_b32 v20, 0x10000, v20
v_mfma_f32_16x16x32_bf16 v[244:247], a[28:31], a[52:55], v[244:247]
v_xor_b32 v21, 0x10000, v21
s_nop 1
s_cbranch_vccnz L_slow0%=
L_back0%=:
v_mfma_f32_16x16x32_bf16 v[232:235], a[40:43], a[28:31], v[232:235]
v_xor_b32 v17, 0x10000, v17
v_mfma_f32_16x16x32_bf16 v[248:251], a[28:31], a[56:59], v[248:251]
buffer_load_dwordx4 v5, s[44:47], s64 offen offset:1024 lds
v_mfma_f32_16x16x32_bf16 v[236:239], a[44:47], a[28:31], v[236:239]
v_mfma_f32_16x16x32_bf16 v[252:255], a[28:31], a[60:63], v[252:255]
buffer_load_dwordx4 a[176:179], v22, s[72:75], 0 offen offset:0 sc1
v_mfma_f32_16x16x32_bf16 a[128:131], a[96:99], a[64:67], a[128:131]
ds_read_b128 a[0:3], v16 offset:0
v_mfma_f32_16x16x32_bf16 a[144:147], a[64:67], a[112:115], a[144:147]
buffer_load_dwordx4 v6, s[44:47], s64 offen offset:2048 lds
v_mfma_f32_16x16x32_bf16 a[132:135], a[100:103], a[64:67], a[132:135]
ds_read_b64_tr_b16 a[32:33], v18 offset:0
ds_read_b64_tr_b16 a[224:225], v23 offset:0
v_mfma_f32_16x16x32_bf16 a[148:151], a[64:67], a[116:119], a[148:151]
buffer_load_dwordx4 a[180:183], v22, s[72:75], 0 offen offset:16 sc1
v_mfma_f32_16x16x32_bf16 a[136:139], a[104:107], a[64:67], a[136:139]
ds_read_b64_tr_b16 a[34:35], v19 offset:0
ds_read_b64_tr_b16 a[226:227], v23 offset:2048
v_mfma_f32_16x16x32_bf16 a[152:155], a[64:67], a[120:123], a[152:155]
buffer_load_dwordx4 v7, s[44:47], s64 offen offset:3072 lds
v_mfma_f32_16x16x32_bf16 a[140:143], a[108:111], a[64:67], a[140:143]
ds_read_b64_tr_b16 a[48:49], v18 offset:16384
ds_read_b64_tr_b16 a[228:229], v24 offset:0
v_mfma_f32_16x16x32_bf16 a[156:159], a[64:67], a[124:127], a[156:159]
buffer_load_dwordx4 a[184:187], v22, s[72:75], 0 offen offset:2048 sc1
v_mfma_f32_16x16x32_bf16 v[32:35], a[96:99], a[68:71], v[32:35]
s_add_u32 m0, s66, 4096
ds_read_b64_tr_b16 a[50:51], v19 offset:16384
ds_read_b64_tr_b16 a[230:231], v24 offset:2048
v_mfma_f32_16x16x32_bf16 v[48:51], a[68:71], a[112:115], v[48:51]
buffer_load_dwordx4 v8, s[44:47], s64 offen offset:0 lds
v_mfma_f32_16x16x32_bf16 v[36:39], a[100:103], a[68:71], v[36:39]
ds_read_b64_tr_b16 a[36:37], v20 offset:0
ds_read_b64_tr_b16 a[232:233], v25 offset:0
v_mfma_f32_16x16x32_bf16 v[52:55], a[68:71], a[116:119], v[52:55]
buffer_load_dwordx4 a[188:191], v22, s[72:75], 0 offen offset:2064 sc1
v_mfma_f32_16x16x32_bf16 v[40:43], a[104:107], a[68:71], v[40:43]
ds_read_b64_tr_b16 a[38:39], v21 offset:0
ds_read_b64_tr_b16 a[234:235], v25 offset:2048
v_mfma_f32_16x16x32_bf16 v[56:59], a[68:71], a[120:123], v[56:59]
buffer_load_dwordx4 v9, s[44:47], s64 offen offset:1024 lds
v_mfma_f32_16x16x32_bf16 v[44:47], a[108:111], a[68:71], v[44:47]
ds_read_b64_tr_b16 a[52:53], v20 offset:16384
ds_read_b64_tr_b16 a[236:237], v26 offset:0
v_mfma_f32_16x16x32_bf16 v[60:63], a[68:71], a[124:127], v[60:63]
v_mfma_f32_16x16x32_bf16 v[64:67], a[96:99], a[72:75], v[64:67]
ds_read_b64_tr_b16 a[54:55], v21 offset:16384
ds_read_b64_tr_b16 a[238:239], v26 offset:2048
v_mfma_f32_16x16x32_bf16 v[80:83], a[72:75], a[112:115], v[80:83]
buffer_load_dwordx4 v10, s[44:47], s64 offen offset:2048 lds
v_mfma_f32_16x16x32_bf16 v[68:71], a[100:103], a[72:75], v[68:71]
ds_read_b64_tr_b16 a[40:41], v18 offset:512
v_mfma_f32_16x16x32_bf16 v[84:87], a[72:75], a[116:119], v[84:87]
v_mfma_f32_16x16x32_bf16 v[72:75], a[104:107], a[72:75], v[72:75]
ds_read_b64_tr_b16 a[42:43], v19 offset:512
v_mfma_f32_16x16x32_bf16 v[88:91], a[72:75], a[120:123], v[88:91]
buffer_load_dwordx4 v11, s[44:47], s64 offen offset:3072 lds
v_mfma_f32_16x16x32_bf16 v[76:79], a[108:111], a[72:75], v[76:79]
ds_read_b64_tr_b16 a[56:57], v18 offset:16896
v_mfma_f32_16x16x32_bf16 v[92:95], a[72:75], a[124:127], v[92:95]
v_mfma_f32_16x16x32_bf16 v[96:99], a[96:99], a[76:79], v[96:99]
s_add_u32 m0, s67, 0
ds_read_b64_tr_b16 a[58:59], v19 offset:16896
v_mfma_f32_16x16x32_bf16 v[112:115], a[76:79], a[112:115], v[112:115]
buffer_load_dwordx4 v12, s[48:51], s65 offen offset:0 lds
v_mfma_f32_16x16x32_bf16 v[100:103], a[100:103], a[76:79], v[100:103]
ds_read_b64_tr_b16 a[44:45], v20 offset:512
v_mfma_f32_16x16x32_bf16 v[116:119], a[76:79], a[116:119], v[116:119]
v_mfma_f32_16x16x32_bf16 v[104:107], a[104:107], a[76:79], v[104:107]
ds_read_b64_tr_b16 a[46:47], v21 offset:512
v_mfma_f32_16x16x32_bf16 v[120:123], a[76:79], a[120:123], v[120:123]
buffer_load_dwordx4 v13, s[48:51], s65 offen offset:1024 lds
v_mfma_f32_16x16x32_bf16 v[108:111], a[108:111], a[76:79], v[108:111]
ds_read_b64_tr_b16 a[60:61], v20 offset:16896
v_mfma_f32_16x16x32_bf16 v[124:127], a[76:79], a[124:127], v[124:127]
v_mfma_f32_16x16x32_bf16 v[128:131], a[96:99], a[80:83], v[128:131]
ds_read_b64_tr_b16 a[62:63], v21 offset:16896
v_mfma_f32_16x16x32_bf16 v[144:147], a[80:83], a[112:115], v[144:147]
buffer_load_dwordx4 v14, s[48:51], s65 offen offset:2048 lds
v_mfma_f32_16x16x32_bf16 v[132:135], a[100:103], a[80:83], v[132:135]
ds_read_b128 a[4:7], v16 offset:2048
v_mfma_f32_16x16x32_bf16 v[148:151], a[80:83], a[116:119], v[148:151]
v_mfma_f32_16x16x32_bf16 v[136:139], a[104:107], a[80:83], v[136:139]
ds_read_b128 a[8:11], v16 offset:4096
v_mfma_f32_16x16x32_bf16 v[152:155], a[80:83], a[120:123], v[152:155]
buffer_load_dwordx4 v15, s[48:51], s65 offen offset:3072 lds
v_mfma_f32_16x16x32_bf16 v[140:143], a[108:111], a[80:83], v[140:143]
ds_read_b128 a[12:15], v16 offset:6144
v_mfma_f32_16x16x32_bf16 v[156:159], a[80:83], a[124:127], v[156:159]
v_mfma_f32_16x16x32_bf16 v[160:163], a[96:99], a[84:87], v[160:163]
s_add_u32 m0, s67, 16384
ds_read_b128 a[16:19], v16 offset:8192
v_mfma_f32_16x16x32_bf16 v[176:179], a[84:87], a[112:115], v[176:179]
buffer_load_dwordx4 v12, s[52:55], s65 offen offset:0 lds
v_mfma_f32_16x16x32_bf16 v[164:167], a[100:103], a[84:87], v[164:167]
ds_read_b128 a[20:23], v16 offset:10240
v_mfma_f32_16x16x32_bf16 v[180:183], a[84:87], a[116:119], v[180:183]
v_mfma_f32_16x16x32_bf16 v[168:171], a[104:107], a[84:87], v[168:171]
ds_read_b128 a[24:27], v16 offset:12288
v_mfma_f32_16x16x32_bf16 v[184:187], a[84:87], a[120:123], v[184:187]
buffer_load_dwordx4 v13, s[52:55], s65 offen offset:1024 lds
v_mfma_f32_16x16x32_bf16 v[172:175], a[108:111], a[84:87], v[172:175]
ds_read_b128 a[28:31], v16 offset:14336
v_mfma_f32_16x16x32_bf16 v[188:191], a[84:87], a[124:127], v[188:191]
v_mfma_f32_16x16x32_bf16 v[192:195], a[96:99], a[88:91], v[192:195]
v_mfma_f32_16x16x32_bf16 v[208:211], a[88:91], a[112:115], v[208:211]
buffer_load_dwordx4 v14, s[52:55], s65 offen offset:2048 lds
v_mfma_f32_16x16x32_bf16 v[196:199], a[100:103], a[88:91], v[196:199]
v_mfma_f32_16x16x32_bf16 v[212:215], a[88:91], a[116:119], v[212:215]
v_mfma_f32_16x16x32_bf16 v[200:203], a[104:107], a[88:91], v[200:203]
v_mfma_f32_16x16x32_bf16 v[216:219], a[88:91], a[120:123], v[216:219]
buffer_load_dwordx4 v15, s[52:55], s65 offen offset:3072 lds
v_mfma_f32_16x16x32_bf16 v[204:207], a[108:111], a[88:91], v[204:207]
s_add_u32 s64, s64, 128
s_add_u32 s65, s65, 524288
v_mfma_f32_16x16x32_bf16 v[220:223], a[88:91], a[124:127], v[220:223]
s_and_b32 s64, s64, 0x3ff
s_and_b32 s65, s65, 0x3fffff
v_mfma_f32_16x16x32_bf16 v[224:227], a[96:99], a[92:95], v[224:227]
s_xor_b32 s66, s66, 0x10000
s_xor_b32 s67, s67, 0x10000
v_mfma_f32_16x16x32_bf16 v[240:243], a[92:95], a[112:115], v[240:243]
v_mfma_f32_16x16x32_bf16 v[228:231], a[100:103], a[92:95], v[228:231]
v_mfma_f32_16x16x32_bf16 v[244:247], a[92:95], a[116:119], v[244:247]
v_mfma_f32_16x16x32_bf16 v[232:235], a[104:107], a[92:95], v[232:235]
v_xor_b32 v16, 0x10000, v16
v_mfma_f32_16x16x32_bf16 v[248:251], a[92:95], a[120:123], v[248:251]
v_mfma_f32_16x16x32_bf16 v[236:239], a[108:111], a[92:95], v[236:239]
v_mfma_f32_16x16x32_bf16 v[252:255], a[92:95], a[124:127], v[252:255]
s_waitcnt lgkmcnt(15)
v_mfma_f32_16x16x32_bf16 a[128:131], a[32:35], a[0:3], a[128:131]
ds_read_b128 a[64:67], v17 offset:0
v_mfma_f32_16x16x32_bf16 a[144:147], a[0:3], a[48:51], a[144:147]
v_mfma_f32_16x16x32_bf16 a[132:135], a[36:39], a[0:3], a[132:135]
ds_read_b64_tr_b16 a[96:97], v18 offset:8192
v_mfma_f32_16x16x32_bf16 a[148:151], a[0:3], a[52:55], a[148:151]
s_waitcnt lgkmcnt(15)
v_mfma_f32_16x16x32_bf16 a[136:139], a[40:43], a[0:3], a[136:139]
ds_read_b64_tr_b16 a[98:99], v19 offset:8192
s_waitcnt lgkmcnt(14)
v_mfma_f32_16x16x32_bf16 a[152:155], a[0:3], a[56:59], a[152:155]
s_waitcnt lgkmcnt(12)
v_mfma_f32_16x16x32_bf16 a[140:143], a[44:47], a[0:3], a[140:143]
ds_read_b64_tr_b16 a[112:113], v18 offset:24576
s_waitcnt lgkmcnt(11)
v_mfma_f32_16x16x32_bf16 a[156:159], a[0:3], a[60:63], a[156:159]
s_waitcnt lgkmcnt(10)
v_mfma_f32_16x16x32_bf16 v[32:35], a[32:35], a[4:7], v[32:35]
ds_read_b64_tr_b16 a[114:115], v19 offset:24576
v_mfma_f32_16x16x32_bf16 v[48:51], a[4:7], a[48:51], v[48:51]
v_mfma_f32_16x16x32_bf16 v[36:39], a[36:39], a[4:7], v[36:39]
ds_read_b64_tr_b16 a[100:101], v20 offset:8192
v_mfma_f32_16x16x32_bf16 v[52:55], a[4:7], a[52:55], v[52:55]
buffer_store_dwordx4 a[224:227], v27, s[56:59], s77 offen offset:0 sc1
v_mfma_f32_16x16x32_bf16 v[40:43], a[40:43], a[4:7], v[40:43]
ds_read_b64_tr_b16 a[102:103], v21 offset:8192
v_mfma_f32_16x16x32_bf16 v[56:59], a[4:7], a[56:59], v[56:59]
v_mfma_f32_16x16x32_bf16 v[44:47], a[44:47], a[4:7], v[44:47]
ds_read_b64_tr_b16 a[116:117], v20 offset:24576
v_mfma_f32_16x16x32_bf16 v[60:63], a[4:7], a[60:63], v[60:63]
s_waitcnt lgkmcnt(13)
v_mfma_f32_16x16x32_bf16 v[64:67], a[32:35], a[8:11], v[64:67]
ds_read_b64_tr_b16 a[118:119], v21 offset:24576
v_mfma_f32_16x16x32_bf16 v[80:83], a[8:11], a[48:51], v[80:83]
v_mfma_f32_16x16x32_bf16 v[68:71], a[36:39], a[8:11], v[68:71]
ds_read_b64_tr_b16 a[104:105], v18 offset:8704
v_mfma_f32_16x16x32_bf16 v[84:87], a[8:11], a[52:55], v[84:87]
v_mfma_f32_16x16x32_bf16 v[72:75], a[40:43], a[8:11], v[72:75]
ds_read_b64_tr_b16 a[106:107], v19 offset:8704
v_mfma_f32_16x16x32_bf16 v[88:91], a[8:11], a[56:59], v[88:91]
v_mfma_f32_16x16x32_bf16 v[76:79], a[44:47], a[8:11], v[76:79]
ds_read_b64_tr_b16 a[120:121], v18 offset:25088
v_mfma_f32_16x16x32_bf16 v[92:95], a[8:11], a[60:63], v[92:95]
s_waitcnt lgkmcnt(15)
v_mfma_f32_16x16x32_bf16 v[96:99], a[32:35], a[12:15], v[96:99]
ds_read_b64_tr_b16 a[122:123], v19 offset:25088
v_mfma_f32_16x16x32_bf16 v[112:115], a[12:15], a[48:51], v[112:115]
v_mfma_f32_16x16x32_bf16 v[100:103], a[36:39], a[12:15], v[100:103]
ds_read_b64_tr_b16 a[108:109], v20 offset:8704
v_mfma_f32_16x16x32_bf16 v[116:119], a[12:15], a[52:55], v[116:119]
v_mfma_f32_16x16x32_bf16 v[104:107], a[40:43], a[12:15], v[104:107]
ds_read_b64_tr_b16 a[110:111], v21 offset:8704
v_mfma_f32_16x16x32_bf16 v[120:123], a[12:15], a[56:59], v[120:123]
v_mfma_f32_16x16x32_bf16 v[108:111], a[44:47], a[12:15], v[108:111]
ds_read_b64_tr_b16 a[124:125], v20 offset:25088
v_mfma_f32_16x16x32_bf16 v[124:127], a[12:15], a[60:63], v[124:127]
v_mfma_f32_16x16x32_bf16 v[128:131], a[32:35], a[16:19], v[128:131]
ds_read_b64_tr_b16 a[126:127], v21 offset:25088
v_mfma_f32_16x16x32_bf16 v[144:147], a[16:19], a[48:51], v[144:147]
buffer_store_dwordx4 a[228:231], v27, s[56:59], s77 offen offset:1024 sc1
v_mfma_f32_16x16x32_bf16 v[132:135], a[36:39], a[16:19], v[132:135]
ds_read_b128 a[68:71], v17 offset:2048
v_mfma_f32_16x16x32_bf16 v[148:151], a[16:19], a[52:55], v[148:151]
v_mfma_f32_16x16x32_bf16 v[136:139], a[40:43], a[16:19], v[136:139]
ds_read_b128 a[72:75], v17 offset:4096
v_mfma_f32_16x16x32_bf16 v[152:155], a[16:19], a[56:59], v[152:155]
v_mfma_f32_16x16x32_bf16 v[140:143], a[44:47], a[16:19], v[140:143]
)ASM"
R"ASM(ds_read_b128 a[76:79], v17 offset:6144
v_mfma_f32_16x16x32_bf16 v[156:159], a[16:19], a[60:63], v[156:159]
s_waitcnt lgkmcnt(15)
v_mfma_f32_16x16x32_bf16 v[160:163], a[32:35], a[20:23], v[160:163]
ds_read_b128 a[80:83], v17 offset:8192
v_mfma_f32_16x16x32_bf16 v[176:179], a[20:23], a[48:51], v[176:179]
v_mfma_f32_16x16x32_bf16 v[164:167], a[36:39], a[20:23], v[164:167]
ds_read_b128 a[84:87], v17 offset:10240
v_mfma_f32_16x16x32_bf16 v[180:183], a[20:23], a[52:55], v[180:183]
v_mfma_f32_16x16x32_bf16 v[168:171], a[40:43], a[20:23], v[168:171]
ds_read_b128 a[88:91], v17 offset:12288
v_mfma_f32_16x16x32_bf16 v[184:187], a[20:23], a[56:59], v[184:187]
v_mfma_f32_16x16x32_bf16 v[172:175], a[44:47], a[20:23], v[172:175]
ds_read_b128 a[92:95], v17 offset:14336
v_mfma_f32_16x16x32_bf16 v[188:191], a[20:23], a[60:63], v[188:191]
v_mfma_f32_16x16x32_bf16 v[192:195], a[32:35], a[24:27], v[192:195]
v_mfma_f32_16x16x32_bf16 v[208:211], a[24:27], a[48:51], v[208:211]
v_mfma_f32_16x16x32_bf16 v[196:199], a[36:39], a[24:27], v[196:199]
v_mfma_f32_16x16x32_bf16 v[212:215], a[24:27], a[52:55], v[212:215]
v_mfma_f32_16x16x32_bf16 v[200:203], a[40:43], a[24:27], v[200:203]
v_mfma_f32_16x16x32_bf16 v[216:219], a[24:27], a[56:59], v[216:219]
v_mfma_f32_16x16x32_bf16 v[204:207], a[44:47], a[24:27], v[204:207]
v_mfma_f32_16x16x32_bf16 v[220:223], a[24:27], a[60:63], v[220:223]
s_waitcnt vmcnt(2) lgkmcnt(0)
s_barrier
v_mfma_f32_16x16x32_bf16 v[224:227], a[32:35], a[28:31], v[224:227]
s_add_u32 m0, s66, 0
v_xor_b32 v18, 0x10000, v18
v_mfma_f32_16x16x32_bf16 v[240:243], a[28:31], a[48:51], v[240:243]
buffer_load_dwordx4 v4, s[44:47], s64 offen offset:0 lds
v_xor_b32 v19, 0x10000, v19
v_mfma_f32_16x16x32_bf16 v[228:231], a[36:39], a[28:31], v[228:231]
v_xor_b32 v20, 0x10000, v20
v_accvgpr_read_b32 v22, a184
v_mfma_f32_16x16x32_bf16 v[244:247], a[28:31], a[52:55], v[244:247]
v_xor_b32 v21, 0x10000, v21
v_accvgpr_read_b32 v0, a176
v_mfma_f32_16x16x32_bf16 v[232:235], a[40:43], a[28:31], v[232:235]
v_xor_b32 v17, 0x10000, v17
v_accvgpr_read_b32 v28, a160
v_mfma_f32_16x16x32_bf16 v[248:251], a[28:31], a[56:59], v[248:251]
buffer_load_dwordx4 v5, s[44:47], s64 offen offset:1024 lds
v_mfma_f32_16x16x32_bf16 v[236:239], a[44:47], a[28:31], v[236:239]
v_accvgpr_read_b32 v29, a168
v_fma_f32 v28, v28, v22, v0
v_mfma_f32_16x16x32_bf16 v[252:255], a[28:31], a[60:63], v[252:255]
v_fma_f32 v29, v29, v22, v0
v_accvgpr_read_b32 v22, a185
v_mfma_f32_16x16x32_bf16 a[128:131], a[96:99], a[64:67], a[128:131]
ds_read_b128 a[0:3], v16 offset:0
v_accvgpr_read_b32 v0, a177
v_mfma_f32_16x16x32_bf16 a[144:147], a[64:67], a[112:115], a[144:147]
buffer_load_dwordx4 v6, s[44:47], s64 offen offset:2048 lds
v_mfma_f32_16x16x32_bf16 a[132:135], a[100:103], a[64:67], a[132:135]
ds_read_b64_tr_b16 a[32:33], v18 offset:0
ds_read_b64_tr_b16 a[208:209], v23 offset:16384
v_mfma_f32_16x16x32_bf16 a[148:151], a[64:67], a[116:119], a[148:151]
v_accvgpr_read_b32 v30, a161
v_accvgpr_read_b32 v31, a169
v_mfma_f32_16x16x32_bf16 a[136:139], a[104:107], a[64:67], a[136:139]
ds_read_b64_tr_b16 a[34:35], v19 offset:0
ds_read_b64_tr_b16 a[210:211], v23 offset:18432
v_mfma_f32_16x16x32_bf16 a[152:155], a[64:67], a[120:123], a[152:155]
buffer_load_dwordx4 v7, s[44:47], s64 offen offset:3072 lds
v_mfma_f32_16x16x32_bf16 a[140:143], a[108:111], a[64:67], a[140:143]
ds_read_b64_tr_b16 a[48:49], v18 offset:16384
ds_read_b64_tr_b16 a[212:213], v24 offset:16384
v_mfma_f32_16x16x32_bf16 a[156:159], a[64:67], a[124:127], a[156:159]
v_fma_f32 v30, v30, v22, v0
v_fma_f32 v31, v31, v22, v0
v_mfma_f32_16x16x32_bf16 v[32:35], a[96:99], a[68:71], v[32:35]
s_add_u32 m0, s66, 4096
ds_read_b64_tr_b16 a[50:51], v19 offset:16384
ds_read_b64_tr_b16 a[214:215], v24 offset:18432
v_mfma_f32_16x16x32_bf16 v[48:51], a[68:71], a[112:115], v[48:51]
buffer_load_dwordx4 v8, s[44:47], s64 offen offset:0 lds
v_mfma_f32_16x16x32_bf16 v[36:39], a[100:103], a[68:71], v[36:39]
ds_read_b64_tr_b16 a[36:37], v20 offset:0
ds_read_b64_tr_b16 a[216:217], v25 offset:16384
v_mfma_f32_16x16x32_bf16 v[52:55], a[68:71], a[116:119], v[52:55]
v_cvt_pk_bf16_f32 v22, v28, v30
v_cvt_pk_bf16_f32 v0, v29, v31
v_mfma_f32_16x16x32_bf16 v[40:43], a[104:107], a[68:71], v[40:43]
ds_read_b64_tr_b16 a[38:39], v21 offset:0
ds_read_b64_tr_b16 a[218:219], v25 offset:18432
v_mfma_f32_16x16x32_bf16 v[56:59], a[68:71], a[120:123], v[56:59]
buffer_load_dwordx4 v9, s[44:47], s64 offen offset:1024 lds
v_mfma_f32_16x16x32_bf16 v[44:47], a[108:111], a[68:71], v[44:47]
ds_read_b64_tr_b16 a[52:53], v20 offset:16384
ds_read_b64_tr_b16 a[220:221], v26 offset:16384
v_mfma_f32_16x16x32_bf16 v[60:63], a[68:71], a[124:127], v[60:63]
v_accvgpr_write_b32 a192, v22
v_accvgpr_write_b32 a196, v0
v_mfma_f32_16x16x32_bf16 v[64:67], a[96:99], a[72:75], v[64:67]
ds_read_b64_tr_b16 a[54:55], v21 offset:16384
ds_read_b64_tr_b16 a[222:223], v26 offset:18432
v_mfma_f32_16x16x32_bf16 v[80:83], a[72:75], a[112:115], v[80:83]
buffer_load_dwordx4 v10, s[44:47], s64 offen offset:2048 lds
v_mfma_f32_16x16x32_bf16 v[68:71], a[100:103], a[72:75], v[68:71]
ds_read_b64_tr_b16 a[40:41], v18 offset:512
v_accvgpr_read_b32 v22, a186
v_mfma_f32_16x16x32_bf16 v[84:87], a[72:75], a[116:119], v[84:87]
v_accvgpr_read_b32 v0, a178
v_accvgpr_read_b32 v28, a162
v_mfma_f32_16x16x32_bf16 v[72:75], a[104:107], a[72:75], v[72:75]
ds_read_b64_tr_b16 a[42:43], v19 offset:512
v_accvgpr_read_b32 v29, a170
v_mfma_f32_16x16x32_bf16 v[88:91], a[72:75], a[120:123], v[88:91]
buffer_load_dwordx4 v11, s[44:47], s64 offen offset:3072 lds
v_mfma_f32_16x16x32_bf16 v[76:79], a[108:111], a[72:75], v[76:79]
ds_read_b64_tr_b16 a[56:57], v18 offset:16896
v_fma_f32 v28, v28, v22, v0
v_mfma_f32_16x16x32_bf16 v[92:95], a[72:75], a[124:127], v[92:95]
v_fma_f32 v29, v29, v22, v0
v_accvgpr_read_b32 v22, a187
v_mfma_f32_16x16x32_bf16 v[96:99], a[96:99], a[76:79], v[96:99]
s_add_u32 m0, s67, 0
ds_read_b64_tr_b16 a[58:59], v19 offset:16896
v_accvgpr_read_b32 v0, a179
v_mfma_f32_16x16x32_bf16 v[112:115], a[76:79], a[112:115], v[112:115]
buffer_load_dwordx4 v12, s[48:51], s65 offen offset:0 lds
v_mfma_f32_16x16x32_bf16 v[100:103], a[100:103], a[76:79], v[100:103]
ds_read_b64_tr_b16 a[44:45], v20 offset:512
v_accvgpr_read_b32 v30, a163
v_mfma_f32_16x16x32_bf16 v[116:119], a[76:79], a[116:119], v[116:119]
v_accvgpr_read_b32 v31, a171
v_fma_f32 v30, v30, v22, v0
v_mfma_f32_16x16x32_bf16 v[104:107], a[104:107], a[76:79], v[104:107]
ds_read_b64_tr_b16 a[46:47], v21 offset:512
v_fma_f32 v31, v31, v22, v0
v_mfma_f32_16x16x32_bf16 v[120:123], a[76:79], a[120:123], v[120:123]
buffer_load_dwordx4 v13, s[48:51], s65 offen offset:1024 lds
v_mfma_f32_16x16x32_bf16 v[108:111], a[108:111], a[76:79], v[108:111]
ds_read_b64_tr_b16 a[60:61], v20 offset:16896
v_cvt_pk_bf16_f32 v22, v28, v30
v_mfma_f32_16x16x32_bf16 v[124:127], a[76:79], a[124:127], v[124:127]
v_cvt_pk_bf16_f32 v0, v29, v31
v_accvgpr_write_b32 a193, v22
v_mfma_f32_16x16x32_bf16 v[128:131], a[96:99], a[80:83], v[128:131]
ds_read_b64_tr_b16 a[62:63], v21 offset:16896
v_accvgpr_write_b32 a197, v0
v_mfma_f32_16x16x32_bf16 v[144:147], a[80:83], a[112:115], v[144:147]
buffer_load_dwordx4 v14, s[48:51], s65 offen offset:2048 lds
v_mfma_f32_16x16x32_bf16 v[132:135], a[100:103], a[80:83], v[132:135]
ds_read_b128 a[4:7], v16 offset:2048
v_mfma_f32_16x16x32_bf16 v[148:151], a[80:83], a[116:119], v[148:151]
v_mfma_f32_16x16x32_bf16 v[136:139], a[104:107], a[80:83], v[136:139]
ds_read_b128 a[8:11], v16 offset:4096
v_mfma_f32_16x16x32_bf16 v[152:155], a[80:83], a[120:123], v[152:155]
buffer_load_dwordx4 v15, s[48:51], s65 offen offset:3072 lds
v_mfma_f32_16x16x32_bf16 v[140:143], a[108:111], a[80:83], v[140:143]
ds_read_b128 a[12:15], v16 offset:6144
v_mfma_f32_16x16x32_bf16 v[156:159], a[80:83], a[124:127], v[156:159]
v_mfma_f32_16x16x32_bf16 v[160:163], a[96:99], a[84:87], v[160:163]
s_add_u32 m0, s67, 16384
ds_read_b128 a[16:19], v16 offset:8192
v_mfma_f32_16x16x32_bf16 v[176:179], a[84:87], a[112:115], v[176:179]
buffer_load_dwordx4 v12, s[52:55], s65 offen offset:0 lds
v_mfma_f32_16x16x32_bf16 v[164:167], a[100:103], a[84:87], v[164:167]
ds_read_b128 a[20:23], v16 offset:10240
v_mfma_f32_16x16x32_bf16 v[180:183], a[84:87], a[116:119], v[180:183]
v_mfma_f32_16x16x32_bf16 v[168:171], a[104:107], a[84:87], v[168:171]
ds_read_b128 a[24:27], v16 offset:12288
v_mfma_f32_16x16x32_bf16 v[184:187], a[84:87], a[120:123], v[184:187]
buffer_load_dwordx4 v13, s[52:55], s65 offen offset:1024 lds
v_mfma_f32_16x16x32_bf16 v[172:175], a[108:111], a[84:87], v[172:175]
ds_read_b128 a[28:31], v16 offset:14336
v_mfma_f32_16x16x32_bf16 v[188:191], a[84:87], a[124:127], v[188:191]
v_mfma_f32_16x16x32_bf16 v[192:195], a[96:99], a[88:91], v[192:195]
v_mfma_f32_16x16x32_bf16 v[208:211], a[88:91], a[112:115], v[208:211]
buffer_load_dwordx4 v14, s[52:55], s65 offen offset:2048 lds
v_mfma_f32_16x16x32_bf16 v[196:199], a[100:103], a[88:91], v[196:199]
v_mfma_f32_16x16x32_bf16 v[212:215], a[88:91], a[116:119], v[212:215]
v_mfma_f32_16x16x32_bf16 v[200:203], a[104:107], a[88:91], v[200:203]
v_mfma_f32_16x16x32_bf16 v[216:219], a[88:91], a[120:123], v[216:219]
buffer_load_dwordx4 v15, s[52:55], s65 offen offset:3072 lds
v_mfma_f32_16x16x32_bf16 v[204:207], a[108:111], a[88:91], v[204:207]
s_add_u32 s64, s64, 128
s_add_u32 s65, s65, 524288
v_mfma_f32_16x16x32_bf16 v[220:223], a[88:91], a[124:127], v[220:223]
s_and_b32 s64, s64, 0x3ff
s_and_b32 s65, s65, 0x3fffff
v_mfma_f32_16x16x32_bf16 v[224:227], a[96:99], a[92:95], v[224:227]
s_xor_b32 s66, s66, 0x10000
s_xor_b32 s67, s67, 0x10000
v_mfma_f32_16x16x32_bf16 v[240:243], a[92:95], a[112:115], v[240:243]
v_mfma_f32_16x16x32_bf16 v[228:231], a[100:103], a[92:95], v[228:231]
v_mfma_f32_16x16x32_bf16 v[244:247], a[92:95], a[116:119], v[244:247]
v_mfma_f32_16x16x32_bf16 v[232:235], a[104:107], a[92:95], v[232:235]
v_xor_b32 v16, 0x10000, v16
v_mfma_f32_16x16x32_bf16 v[248:251], a[92:95], a[120:123], v[248:251]
v_mfma_f32_16x16x32_bf16 v[236:239], a[108:111], a[92:95], v[236:239]
v_mfma_f32_16x16x32_bf16 v[252:255], a[92:95], a[124:127], v[252:255]
s_waitcnt lgkmcnt(15)
v_mfma_f32_16x16x32_bf16 a[128:131], a[32:35], a[0:3], a[128:131]
s_lshl_b32 s71, s39, 5
s_add_u32 s71, s71, 133120
ds_read_b128 a[64:67], v17 offset:0
v_mfma_f32_16x16x32_bf16 a[144:147], a[0:3], a[48:51], a[144:147]
v_accvgpr_read_b32 v22, a188
v_accvgpr_read_b32 v0, a180
v_mfma_f32_16x16x32_bf16 a[132:135], a[36:39], a[0:3], a[132:135]
ds_read_b64_tr_b16 a[96:97], v18 offset:8192
v_accvgpr_read_b32 v28, a164
v_mfma_f32_16x16x32_bf16 a[148:151], a[0:3], a[52:55], a[148:151]
v_accvgpr_read_b32 v29, a172
v_fma_f32 v28, v28, v22, v0
s_waitcnt lgkmcnt(15)
v_mfma_f32_16x16x32_bf16 a[136:139], a[40:43], a[0:3], a[136:139]
ds_read_b64_tr_b16 a[98:99], v19 offset:8192
v_fma_f32 v29, v29, v22, v0
s_waitcnt lgkmcnt(14)
v_mfma_f32_16x16x32_bf16 a[152:155], a[0:3], a[56:59], a[152:155]
v_accvgpr_read_b32 v22, a189
v_accvgpr_read_b32 v0, a181
s_waitcnt lgkmcnt(12)
v_mfma_f32_16x16x32_bf16 a[140:143], a[44:47], a[0:3], a[140:143]
ds_read_b64_tr_b16 a[112:113], v18 offset:24576
v_accvgpr_read_b32 v30, a165
s_waitcnt lgkmcnt(11)
v_mfma_f32_16x16x32_bf16 a[156:159], a[0:3], a[60:63], a[156:159]
v_accvgpr_read_b32 v31, a173
v_fma_f32 v30, v30, v22, v0
s_waitcnt lgkmcnt(10)
v_mfma_f32_16x16x32_bf16 v[32:35], a[32:35], a[4:7], v[32:35]
ds_read_b64_tr_b16 a[114:115], v19 offset:24576
v_fma_f32 v31, v31, v22, v0
v_mfma_f32_16x16x32_bf16 v[48:51], a[4:7], a[48:51], v[48:51]
v_cvt_pk_bf16_f32 v22, v28, v30
v_cvt_pk_bf16_f32 v0, v29, v31
v_mfma_f32_16x16x32_bf16 v[36:39], a[36:39], a[4:7], v[36:39]
ds_read_b64_tr_b16 a[100:101], v20 offset:8192
v_accvgpr_write_b32 a194, v22
v_mfma_f32_16x16x32_bf16 v[52:55], a[4:7], a[52:55], v[52:55]
buffer_store_dwordx4 a[232:235], v27, s[56:59], s77 offen offset:2048 sc1
v_accvgpr_write_b32 a198, v0
v_mfma_f32_16x16x32_bf16 v[40:43], a[40:43], a[4:7], v[40:43]
ds_read_b64_tr_b16 a[102:103], v21 offset:8192
v_accvgpr_read_b32 v22, a190
v_mfma_f32_16x16x32_bf16 v[56:59], a[4:7], a[56:59], v[56:59]
v_accvgpr_read_b32 v0, a182
v_accvgpr_read_b32 v28, a166
v_mfma_f32_16x16x32_bf16 v[44:47], a[44:47], a[4:7], v[44:47]
ds_read_b64_tr_b16 a[116:117], v20 offset:24576
v_accvgpr_read_b32 v29, a174
v_mfma_f32_16x16x32_bf16 v[60:63], a[4:7], a[60:63], v[60:63]
v_fma_f32 v28, v28, v22, v0
v_fma_f32 v29, v29, v22, v0
s_waitcnt lgkmcnt(13)
v_mfma_f32_16x16x32_bf16 v[64:67], a[32:35], a[8:11], v[64:67]
ds_read_b64_tr_b16 a[118:119], v21 offset:24576
v_accvgpr_read_b32 v22, a191
v_mfma_f32_16x16x32_bf16 v[80:83], a[8:11], a[48:51], v[80:83]
v_accvgpr_read_b32 v0, a183
v_accvgpr_read_b32 v30, a167
v_mfma_f32_16x16x32_bf16 v[68:71], a[36:39], a[8:11], v[68:71]
ds_read_b64_tr_b16 a[104:105], v18 offset:8704
v_accvgpr_read_b32 v31, a175
v_mfma_f32_16x16x32_bf16 v[84:87], a[8:11], a[52:55], v[84:87]
v_fma_f32 v30, v30, v22, v0
v_fma_f32 v31, v31, v22, v0
v_mfma_f32_16x16x32_bf16 v[72:75], a[40:43], a[8:11], v[72:75]
ds_read_b64_tr_b16 a[106:107], v19 offset:8704
v_cvt_pk_bf16_f32 v22, v28, v30
v_mfma_f32_16x16x32_bf16 v[88:91], a[8:11], a[56:59], v[88:91]
v_cvt_pk_bf16_f32 v0, v29, v31
v_accvgpr_write_b32 a195, v22
v_mfma_f32_16x16x32_bf16 v[76:79], a[44:47], a[8:11], v[76:79]
ds_read_b64_tr_b16 a[120:121], v18 offset:25088
v_accvgpr_write_b32 a199, v0
v_mfma_f32_16x16x32_bf16 v[92:95], a[8:11], a[60:63], v[92:95]
v_lshlrev_b32 v23, 3, v1
v_add_u32 v23, s71, v23
s_waitcnt lgkmcnt(15)
v_mfma_f32_16x16x32_bf16 v[96:99], a[32:35], a[12:15], v[96:99]
ds_read_b64_tr_b16 a[122:123], v19 offset:25088
v_mfma_f32_16x16x32_bf16 v[112:115], a[12:15], a[48:51], v[112:115]
ds_write_b128 v23, a[192:195]
v_mfma_f32_16x16x32_bf16 v[100:103], a[36:39], a[12:15], v[100:103]
ds_read_b64_tr_b16 a[108:109], v20 offset:8704
v_mfma_f32_16x16x32_bf16 v[116:119], a[12:15], a[52:55], v[116:119]
ds_write_b128 v23, a[196:199] offset:16
v_mfma_f32_16x16x32_bf16 v[104:107], a[40:43], a[12:15], v[104:107]
ds_read_b64_tr_b16 a[110:111], v21 offset:8704
v_mfma_f32_16x16x32_bf16 v[120:123], a[12:15], a[56:59], v[120:123]
v_mfma_f32_16x16x32_bf16 v[108:111], a[44:47], a[12:15], v[108:111]
s_lshl_b32 s70, s39, 10
s_add_u32 s70, s70, 133120
ds_read_b64_tr_b16 a[124:125], v20 offset:25088
v_mfma_f32_16x16x32_bf16 v[124:127], a[12:15], a[60:63], v[124:127]
v_mfma_f32_16x16x32_bf16 v[128:131], a[32:35], a[16:19], v[128:131]
ds_read_b64_tr_b16 a[126:127], v21 offset:25088
v_mfma_f32_16x16x32_bf16 v[144:147], a[16:19], a[48:51], v[144:147]
buffer_store_dwordx4 a[236:239], v27, s[56:59], s77 offen offset:3072 sc1
v_add_u32 v25, s70, v1
v_mfma_f32_16x16x32_bf16 v[132:135], a[36:39], a[16:19], v[132:135]
ds_read_b128 a[68:71], v17 offset:2048
v_lshrrev_b32 v26, 5, v2
v_mfma_f32_16x16x32_bf16 v[148:151], a[16:19], a[52:55], v[148:151]
v_lshlrev_b32 v26, 10, v26
)ASM"
R"ASM(v_bfe_u32 v24, v2, 3, 2
v_mfma_f32_16x16x32_bf16 v[136:139], a[40:43], a[16:19], v[136:139]
ds_read_b128 a[72:75], v17 offset:4096
v_lshl_add_u32 v26, v24, 8, v26
v_mfma_f32_16x16x32_bf16 v[152:155], a[16:19], a[56:59], v[152:155]
v_and_b32 v24, 7, v2
v_lshl_add_u32 v26, v24, 4, v26
v_mfma_f32_16x16x32_bf16 v[140:143], a[44:47], a[16:19], v[140:143]
ds_read_b128 a[76:79], v17 offset:6144
v_mfma_f32_16x16x32_bf16 v[156:159], a[16:19], a[60:63], v[156:159]
s_waitcnt lgkmcnt(15)
v_mfma_f32_16x16x32_bf16 v[160:163], a[32:35], a[20:23], v[160:163]
s_add_u32 s72, s30, 33554432
ds_read_b128 a[80:83], v17 offset:8192
v_mfma_f32_16x16x32_bf16 v[176:179], a[20:23], a[48:51], v[176:179]
s_addc_u32 s73, s31, 0
v_mfma_f32_16x16x32_bf16 v[164:167], a[36:39], a[20:23], v[164:167]
s_and_b32 s73, s73, 0xffff
ds_read_b128 a[84:87], v17 offset:10240
v_mfma_f32_16x16x32_bf16 v[180:183], a[20:23], a[52:55], v[180:183]
s_mov_b32 s74, 2097152
v_mfma_f32_16x16x32_bf16 v[168:171], a[40:43], a[20:23], v[168:171]
s_mov_b32 s75, 0x20000
ds_read_b128 a[88:91], v17 offset:12288
v_mfma_f32_16x16x32_bf16 v[184:187], a[20:23], a[56:59], v[184:187]
v_mfma_f32_16x16x32_bf16 v[172:175], a[44:47], a[20:23], v[172:175]
s_lshr_b32 s70, s38, 1
s_lshl_b32 s70, s70, 14
ds_read_b128 a[92:95], v17 offset:14336
v_mfma_f32_16x16x32_bf16 v[188:191], a[20:23], a[60:63], v[188:191]
s_and_b32 s71, s38, 1
s_lshl_b32 s71, s71, 7
v_mfma_f32_16x16x32_bf16 v[192:195], a[32:35], a[24:27], v[192:195]
s_add_u32 s70, s70, s71
s_lshl_b32 s71, s39, 11
v_mfma_f32_16x16x32_bf16 v[208:211], a[24:27], a[48:51], v[208:211]
s_add_u32 s70, s70, s71
s_add_u32 s71, s70, 8192
v_mfma_f32_16x16x32_bf16 v[196:199], a[36:39], a[24:27], v[196:199]
v_mfma_f32_16x16x32_bf16 v[212:215], a[24:27], a[52:55], v[212:215]
v_mfma_f32_16x16x32_bf16 v[200:203], a[40:43], a[24:27], v[200:203]
v_mfma_f32_16x16x32_bf16 v[216:219], a[24:27], a[56:59], v[216:219]
v_mfma_f32_16x16x32_bf16 v[204:207], a[44:47], a[24:27], v[204:207]
v_mfma_f32_16x16x32_bf16 v[220:223], a[24:27], a[60:63], v[220:223]
s_waitcnt vmcnt(2) lgkmcnt(0)
s_barrier
v_mfma_f32_16x16x32_bf16 v[224:227], a[32:35], a[28:31], v[224:227]
s_add_u32 m0, s66, 0
v_xor_b32 v18, 0x10000, v18
v_mfma_f32_16x16x32_bf16 v[240:243], a[28:31], a[48:51], v[240:243]
buffer_load_dwordx4 v4, s[44:47], s64 offen offset:0 lds
v_xor_b32 v19, 0x10000, v19
v_mfma_f32_16x16x32_bf16 v[228:231], a[36:39], a[28:31], v[228:231]
v_xor_b32 v20, 0x10000, v20
v_mfma_f32_16x16x32_bf16 v[244:247], a[28:31], a[52:55], v[244:247]
ds_read_b128 a[192:195], v25
v_xor_b32 v21, 0x10000, v21
v_mfma_f32_16x16x32_bf16 v[232:235], a[40:43], a[28:31], v[232:235]
v_xor_b32 v17, 0x10000, v17
v_mfma_f32_16x16x32_bf16 v[248:251], a[28:31], a[56:59], v[248:251]
buffer_load_dwordx4 v5, s[44:47], s64 offen offset:1024 lds
v_mfma_f32_16x16x32_bf16 v[236:239], a[44:47], a[28:31], v[236:239]
v_mfma_f32_16x16x32_bf16 v[252:255], a[28:31], a[60:63], v[252:255]
ds_read_b128 a[196:199], v25 offset:4096
v_mfma_f32_16x16x32_bf16 a[128:131], a[96:99], a[64:67], a[128:131]
ds_read_b128 a[0:3], v16 offset:0
v_mfma_f32_16x16x32_bf16 a[144:147], a[64:67], a[112:115], a[144:147]
buffer_load_dwordx4 v6, s[44:47], s64 offen offset:2048 lds
v_mfma_f32_16x16x32_bf16 a[132:135], a[100:103], a[64:67], a[132:135]
ds_read_b64_tr_b16 a[32:33], v18 offset:0
v_mfma_f32_16x16x32_bf16 a[148:151], a[64:67], a[116:119], a[148:151]
v_mfma_f32_16x16x32_bf16 a[136:139], a[104:107], a[64:67], a[136:139]
ds_read_b64_tr_b16 a[34:35], v19 offset:0
v_mfma_f32_16x16x32_bf16 a[152:155], a[64:67], a[120:123], a[152:155]
buffer_load_dwordx4 v7, s[44:47], s64 offen offset:3072 lds
v_mfma_f32_16x16x32_bf16 a[140:143], a[108:111], a[64:67], a[140:143]
ds_read_b64_tr_b16 a[48:49], v18 offset:16384
v_mfma_f32_16x16x32_bf16 a[156:159], a[64:67], a[124:127], a[156:159]
v_mfma_f32_16x16x32_bf16 v[32:35], a[96:99], a[68:71], v[32:35]
s_add_u32 m0, s66, 4096
ds_read_b64_tr_b16 a[50:51], v19 offset:16384
v_mfma_f32_16x16x32_bf16 v[48:51], a[68:71], a[112:115], v[48:51]
buffer_load_dwordx4 v8, s[44:47], s64 offen offset:0 lds
v_mfma_f32_16x16x32_bf16 v[36:39], a[100:103], a[68:71], v[36:39]
ds_read_b64_tr_b16 a[36:37], v20 offset:0
v_mfma_f32_16x16x32_bf16 v[52:55], a[68:71], a[116:119], v[52:55]
v_mfma_f32_16x16x32_bf16 v[40:43], a[104:107], a[68:71], v[40:43]
ds_read_b64_tr_b16 a[38:39], v21 offset:0
v_mfma_f32_16x16x32_bf16 v[56:59], a[68:71], a[120:123], v[56:59]
buffer_load_dwordx4 v9, s[44:47], s64 offen offset:1024 lds
v_mfma_f32_16x16x32_bf16 v[44:47], a[108:111], a[68:71], v[44:47]
ds_read_b64_tr_b16 a[52:53], v20 offset:16384
v_mfma_f32_16x16x32_bf16 v[60:63], a[68:71], a[124:127], v[60:63]
s_waitcnt lgkmcnt(8)
buffer_store_dwordx4 a[192:195], v26, s[72:75], s70 offen sc1
v_mfma_f32_16x16x32_bf16 v[64:67], a[96:99], a[72:75], v[64:67]
ds_read_b64_tr_b16 a[54:55], v21 offset:16384
v_mfma_f32_16x16x32_bf16 v[80:83], a[72:75], a[112:115], v[80:83]
buffer_load_dwordx4 v10, s[44:47], s64 offen offset:2048 lds
v_mfma_f32_16x16x32_bf16 v[68:71], a[100:103], a[72:75], v[68:71]
ds_read_b64_tr_b16 a[40:41], v18 offset:512
v_mfma_f32_16x16x32_bf16 v[84:87], a[72:75], a[116:119], v[84:87]
buffer_store_dwordx4 a[196:199], v26, s[72:75], s71 offen sc1
v_mfma_f32_16x16x32_bf16 v[72:75], a[104:107], a[72:75], v[72:75]
ds_read_b64_tr_b16 a[42:43], v19 offset:512
v_mfma_f32_16x16x32_bf16 v[88:91], a[72:75], a[120:123], v[88:91]
buffer_load_dwordx4 v11, s[44:47], s64 offen offset:3072 lds
v_mfma_f32_16x16x32_bf16 v[76:79], a[108:111], a[72:75], v[76:79]
ds_read_b64_tr_b16 a[56:57], v18 offset:16896
v_mfma_f32_16x16x32_bf16 v[92:95], a[72:75], a[124:127], v[92:95]
v_mfma_f32_16x16x32_bf16 v[96:99], a[96:99], a[76:79], v[96:99]
s_add_u32 m0, s67, 0
ds_read_b64_tr_b16 a[58:59], v19 offset:16896
v_mfma_f32_16x16x32_bf16 v[112:115], a[76:79], a[112:115], v[112:115]
buffer_load_dwordx4 v12, s[48:51], s65 offen offset:0 lds
v_mfma_f32_16x16x32_bf16 v[100:103], a[100:103], a[76:79], v[100:103]
ds_read_b64_tr_b16 a[44:45], v20 offset:512
v_mfma_f32_16x16x32_bf16 v[116:119], a[76:79], a[116:119], v[116:119]
v_mfma_f32_16x16x32_bf16 v[104:107], a[104:107], a[76:79], v[104:107]
ds_read_b64_tr_b16 a[46:47], v21 offset:512
v_mfma_f32_16x16x32_bf16 v[120:123], a[76:79], a[120:123], v[120:123]
buffer_load_dwordx4 v13, s[48:51], s65 offen offset:1024 lds
v_mfma_f32_16x16x32_bf16 v[108:111], a[108:111], a[76:79], v[108:111]
ds_read_b64_tr_b16 a[60:61], v20 offset:16896
v_mfma_f32_16x16x32_bf16 v[124:127], a[76:79], a[124:127], v[124:127]
v_mfma_f32_16x16x32_bf16 v[128:131], a[96:99], a[80:83], v[128:131]
ds_read_b64_tr_b16 a[62:63], v21 offset:16896
v_mfma_f32_16x16x32_bf16 v[144:147], a[80:83], a[112:115], v[144:147]
buffer_load_dwordx4 v14, s[48:51], s65 offen offset:2048 lds
v_mfma_f32_16x16x32_bf16 v[132:135], a[100:103], a[80:83], v[132:135]
ds_read_b128 a[4:7], v16 offset:2048
v_mfma_f32_16x16x32_bf16 v[148:151], a[80:83], a[116:119], v[148:151]
v_mfma_f32_16x16x32_bf16 v[136:139], a[104:107], a[80:83], v[136:139]
ds_read_b128 a[8:11], v16 offset:4096
v_mfma_f32_16x16x32_bf16 v[152:155], a[80:83], a[120:123], v[152:155]
buffer_load_dwordx4 v15, s[48:51], s65 offen offset:3072 lds
v_mfma_f32_16x16x32_bf16 v[140:143], a[108:111], a[80:83], v[140:143]
ds_read_b128 a[12:15], v16 offset:6144
v_mfma_f32_16x16x32_bf16 v[156:159], a[80:83], a[124:127], v[156:159]
v_mfma_f32_16x16x32_bf16 v[160:163], a[96:99], a[84:87], v[160:163]
s_add_u32 m0, s67, 16384
ds_read_b128 a[16:19], v16 offset:8192
v_mfma_f32_16x16x32_bf16 v[176:179], a[84:87], a[112:115], v[176:179]
buffer_load_dwordx4 v12, s[52:55], s65 offen offset:0 lds
v_mfma_f32_16x16x32_bf16 v[164:167], a[100:103], a[84:87], v[164:167]
ds_read_b128 a[20:23], v16 offset:10240
v_mfma_f32_16x16x32_bf16 v[180:183], a[84:87], a[116:119], v[180:183]
v_mfma_f32_16x16x32_bf16 v[168:171], a[104:107], a[84:87], v[168:171]
ds_read_b128 a[24:27], v16 offset:12288
v_mfma_f32_16x16x32_bf16 v[184:187], a[84:87], a[120:123], v[184:187]
buffer_load_dwordx4 v13, s[52:55], s65 offen offset:1024 lds
v_mfma_f32_16x16x32_bf16 v[172:175], a[108:111], a[84:87], v[172:175]
ds_read_b128 a[28:31], v16 offset:14336
v_mfma_f32_16x16x32_bf16 v[188:191], a[84:87], a[124:127], v[188:191]
v_mfma_f32_16x16x32_bf16 v[192:195], a[96:99], a[88:91], v[192:195]
v_mfma_f32_16x16x32_bf16 v[208:211], a[88:91], a[112:115], v[208:211]
buffer_load_dwordx4 v14, s[52:55], s65 offen offset:2048 lds
v_mfma_f32_16x16x32_bf16 v[196:199], a[100:103], a[88:91], v[196:199]
v_mfma_f32_16x16x32_bf16 v[212:215], a[88:91], a[116:119], v[212:215]
v_mfma_f32_16x16x32_bf16 v[200:203], a[104:107], a[88:91], v[200:203]
v_mfma_f32_16x16x32_bf16 v[216:219], a[88:91], a[120:123], v[216:219]
buffer_load_dwordx4 v15, s[52:55], s65 offen offset:3072 lds
v_mfma_f32_16x16x32_bf16 v[204:207], a[108:111], a[88:91], v[204:207]
s_add_u32 s64, s64, 128
s_add_u32 s65, s65, 524288
v_mfma_f32_16x16x32_bf16 v[220:223], a[88:91], a[124:127], v[220:223]
s_and_b32 s64, s64, 0x3ff
s_and_b32 s65, s65, 0x3fffff
v_mfma_f32_16x16x32_bf16 v[224:227], a[96:99], a[92:95], v[224:227]
s_xor_b32 s66, s66, 0x10000
s_xor_b32 s67, s67, 0x10000
v_mfma_f32_16x16x32_bf16 v[240:243], a[92:95], a[112:115], v[240:243]
v_mfma_f32_16x16x32_bf16 v[228:231], a[100:103], a[92:95], v[228:231]
v_mfma_f32_16x16x32_bf16 v[244:247], a[92:95], a[116:119], v[244:247]
v_mfma_f32_16x16x32_bf16 v[232:235], a[104:107], a[92:95], v[232:235]
v_xor_b32 v16, 0x10000, v16
v_mfma_f32_16x16x32_bf16 v[248:251], a[92:95], a[120:123], v[248:251]
v_mfma_f32_16x16x32_bf16 v[236:239], a[108:111], a[92:95], v[236:239]
v_mfma_f32_16x16x32_bf16 v[252:255], a[92:95], a[124:127], v[252:255]
s_waitcnt lgkmcnt(15)
v_mfma_f32_16x16x32_bf16 a[128:131], a[32:35], a[0:3], a[128:131]
ds_read_b128 a[64:67], v17 offset:0
v_mfma_f32_16x16x32_bf16 a[144:147], a[0:3], a[48:51], a[144:147]
v_mfma_f32_16x16x32_bf16 a[132:135], a[36:39], a[0:3], a[132:135]
ds_read_b64_tr_b16 a[96:97], v18 offset:8192
v_mfma_f32_16x16x32_bf16 a[148:151], a[0:3], a[52:55], a[148:151]
s_waitcnt lgkmcnt(15)
v_mfma_f32_16x16x32_bf16 a[136:139], a[40:43], a[0:3], a[136:139]
ds_read_b64_tr_b16 a[98:99], v19 offset:8192
s_waitcnt lgkmcnt(14)
v_mfma_f32_16x16x32_bf16 a[152:155], a[0:3], a[56:59], a[152:155]
s_waitcnt lgkmcnt(12)
v_mfma_f32_16x16x32_bf16 a[140:143], a[44:47], a[0:3], a[140:143]
ds_read_b64_tr_b16 a[112:113], v18 offset:24576
s_waitcnt lgkmcnt(11)
v_mfma_f32_16x16x32_bf16 a[156:159], a[0:3], a[60:63], a[156:159]
s_waitcnt lgkmcnt(10)
v_mfma_f32_16x16x32_bf16 v[32:35], a[32:35], a[4:7], v[32:35]
ds_read_b64_tr_b16 a[114:115], v19 offset:24576
v_mfma_f32_16x16x32_bf16 v[48:51], a[4:7], a[48:51], v[48:51]
v_mfma_f32_16x16x32_bf16 v[36:39], a[36:39], a[4:7], v[36:39]
ds_read_b64_tr_b16 a[100:101], v20 offset:8192
v_mfma_f32_16x16x32_bf16 v[52:55], a[4:7], a[52:55], v[52:55]
buffer_store_dwordx4 a[208:211], v1, s[56:59], s76 offen offset:0 sc1
v_mfma_f32_16x16x32_bf16 v[40:43], a[40:43], a[4:7], v[40:43]
ds_read_b64_tr_b16 a[102:103], v21 offset:8192
v_mfma_f32_16x16x32_bf16 v[56:59], a[4:7], a[56:59], v[56:59]
v_mfma_f32_16x16x32_bf16 v[44:47], a[44:47], a[4:7], v[44:47]
ds_read_b64_tr_b16 a[116:117], v20 offset:24576
v_mfma_f32_16x16x32_bf16 v[60:63], a[4:7], a[60:63], v[60:63]
s_waitcnt lgkmcnt(13)
v_mfma_f32_16x16x32_bf16 v[64:67], a[32:35], a[8:11], v[64:67]
ds_read_b64_tr_b16 a[118:119], v21 offset:24576
v_mfma_f32_16x16x32_bf16 v[80:83], a[8:11], a[48:51], v[80:83]
v_mfma_f32_16x16x32_bf16 v[68:71], a[36:39], a[8:11], v[68:71]
ds_read_b64_tr_b16 a[104:105], v18 offset:8704
v_mfma_f32_16x16x32_bf16 v[84:87], a[8:11], a[52:55], v[84:87]
v_mfma_f32_16x16x32_bf16 v[72:75], a[40:43], a[8:11], v[72:75]
ds_read_b64_tr_b16 a[106:107], v19 offset:8704
v_mfma_f32_16x16x32_bf16 v[88:91], a[8:11], a[56:59], v[88:91]
v_mfma_f32_16x16x32_bf16 v[76:79], a[44:47], a[8:11], v[76:79]
ds_read_b64_tr_b16 a[120:121], v18 offset:25088
v_mfma_f32_16x16x32_bf16 v[92:95], a[8:11], a[60:63], v[92:95]
s_waitcnt lgkmcnt(15)
v_mfma_f32_16x16x32_bf16 v[96:99], a[32:35], a[12:15], v[96:99]
ds_read_b64_tr_b16 a[122:123], v19 offset:25088
v_mfma_f32_16x16x32_bf16 v[112:115], a[12:15], a[48:51], v[112:115]
v_mfma_f32_16x16x32_bf16 v[100:103], a[36:39], a[12:15], v[100:103]
ds_read_b64_tr_b16 a[108:109], v20 offset:8704
v_mfma_f32_16x16x32_bf16 v[116:119], a[12:15], a[52:55], v[116:119]
v_mfma_f32_16x16x32_bf16 v[104:107], a[40:43], a[12:15], v[104:107]
ds_read_b64_tr_b16 a[110:111], v21 offset:8704
v_mfma_f32_16x16x32_bf16 v[120:123], a[12:15], a[56:59], v[120:123]
v_mfma_f32_16x16x32_bf16 v[108:111], a[44:47], a[12:15], v[108:111]
ds_read_b64_tr_b16 a[124:125], v20 offset:25088
v_mfma_f32_16x16x32_bf16 v[124:127], a[12:15], a[60:63], v[124:127]
v_mfma_f32_16x16x32_bf16 v[128:131], a[32:35], a[16:19], v[128:131]
ds_read_b64_tr_b16 a[126:127], v21 offset:25088
v_mfma_f32_16x16x32_bf16 v[144:147], a[16:19], a[48:51], v[144:147]
buffer_store_dwordx4 a[212:215], v1, s[56:59], s76 offen offset:1024 sc1
v_mfma_f32_16x16x32_bf16 v[132:135], a[36:39], a[16:19], v[132:135]
ds_read_b128 a[68:71], v17 offset:2048
v_mfma_f32_16x16x32_bf16 v[148:151], a[16:19], a[52:55], v[148:151]
v_mfma_f32_16x16x32_bf16 v[136:139], a[40:43], a[16:19], v[136:139]
ds_read_b128 a[72:75], v17 offset:4096
v_mfma_f32_16x16x32_bf16 v[152:155], a[16:19], a[56:59], v[152:155]
v_mfma_f32_16x16x32_bf16 v[140:143], a[44:47], a[16:19], v[140:143]
ds_read_b128 a[76:79], v17 offset:6144
v_mfma_f32_16x16x32_bf16 v[156:159], a[16:19], a[60:63], v[156:159]
s_waitcnt lgkmcnt(15)
v_mfma_f32_16x16x32_bf16 v[160:163], a[32:35], a[20:23], v[160:163]
ds_read_b128 a[80:83], v17 offset:8192
v_mfma_f32_16x16x32_bf16 v[176:179], a[20:23], a[48:51], v[176:179]
v_mfma_f32_16x16x32_bf16 v[164:167], a[36:39], a[20:23], v[164:167]
ds_read_b128 a[84:87], v17 offset:10240
v_mfma_f32_16x16x32_bf16 v[180:183], a[20:23], a[52:55], v[180:183]
v_mfma_f32_16x16x32_bf16 v[168:171], a[40:43], a[20:23], v[168:171]
ds_read_b128 a[88:91], v17 offset:12288
v_mov_b32 v28, s33
v_mfma_f32_16x16x32_bf16 v[184:187], a[20:23], a[56:59], v[184:187]
v_mov_b32 v29, 0
v_mfma_f32_16x16x32_bf16 v[172:175], a[44:47], a[20:23], v[172:175]
ds_read_b128 a[92:95], v17 offset:14336
v_mfma_f32_16x16x32_bf16 v[188:191], a[20:23], a[60:63], v[188:191]
v_mfma_f32_16x16x32_bf16 v[192:195], a[32:35], a[24:27], v[192:195]
v_mfma_f32_16x16x32_bf16 v[208:211], a[24:27], a[48:51], v[208:211]
v_mfma_f32_16x16x32_bf16 v[196:199], a[36:39], a[24:27], v[196:199]
v_mfma_f32_16x16x32_bf16 v[212:215], a[24:27], a[52:55], v[212:215]
v_mfma_f32_16x16x32_bf16 v[200:203], a[40:43], a[24:27], v[200:203]
v_mfma_f32_16x16x32_bf16 v[216:219], a[24:27], a[56:59], v[216:219]
v_mfma_f32_16x16x32_bf16 v[204:207], a[44:47], a[24:27], v[204:207]
v_mfma_f32_16x16x32_bf16 v[220:223], a[24:27], a[60:63], v[220:223]
s_waitcnt vmcnt(2) lgkmcnt(0)
s_barrier
v_mfma_f32_16x16x32_bf16 v[224:227], a[32:35], a[28:31], v[224:227]
s_add_u32 m0, s66, 0
v_xor_b32 v18, 0x10000, v18
v_mfma_f32_16x16x32_bf16 v[240:243], a[28:31], a[48:51], v[240:243]
buffer_load_dwordx4 v4, s[44:47], s64 offen offset:0 lds
v_xor_b32 v19, 0x10000, v19
v_mfma_f32_16x16x32_bf16 v[228:231], a[36:39], a[28:31], v[228:231]
v_xor_b32 v20, 0x10000, v20
s_lshl_b32 s79, s38, 7
s_add_u32 s79, s79, 32768
s_mov_b64 exec, s[34:35]
buffer_store_dword v28, v29, s[40:43], s79 offen sc1
s_mov_b64 exec, -1
v_mfma_f32_16x16x32_bf16 v[244:247], a[28:31], a[52:55], v[244:247]
v_xor_b32 v21, 0x10000, v21
v_mfma_f32_16x16x32_bf16 v[232:235], a[40:43], a[28:31], v[232:235]
s_mov_b32 s72, s12
s_lshl_b32 s70, s61, 4
s_sub_u32 s71, 224, s70
v_xor_b32 v17, 0x10000, v17
v_mfma_f32_16x16x32_bf16 v[248:251], a[28:31], a[56:59], v[248:251]
s_mov_b32 s73, s13
buffer_load_dwordx4 v5, s[44:47], s64 offen offset:1024 lds
v_mfma_f32_16x16x32_bf16 v[236:239], a[44:47], a[28:31], v[236:239]
s_and_b32 s73, s73, 0xffff
v_lshrrev_b32 v22, 2, v2
)ASM"
R"ASM(v_lshlrev_b32 v22, 13, v22
v_mfma_f32_16x16x32_bf16 v[252:255], a[28:31], a[60:63], v[252:255]
s_mov_b32 s74, 4194304
v_and_b32 v24, 3, v2
v_lshl_add_u32 v22, v24, 6, v22
v_mfma_f32_16x16x32_bf16 a[128:131], a[96:99], a[64:67], a[128:131]
s_mov_b32 s75, 0x20000
s_sub_u32 s71, s71, s70
ds_read_b128 a[0:3], v16 offset:0
v_mfma_f32_16x16x32_bf16 a[144:147], a[64:67], a[112:115], a[144:147]
buffer_load_dwordx4 v6, s[44:47], s64 offen offset:2048 lds
v_mfma_f32_16x16x32_bf16 a[132:135], a[100:103], a[64:67], a[132:135]
s_lshl_b32 s77, s39, 3
s_add_u32 s77, s77, s61
ds_read_b64_tr_b16 a[32:33], v18 offset:0
v_mfma_f32_16x16x32_bf16 a[148:151], a[64:67], a[116:119], a[148:151]
s_lshl_b32 s77, s77, 9
s_add_u32 s77, s77, s60
v_mfma_f32_16x16x32_bf16 a[136:139], a[104:107], a[64:67], a[136:139]
s_lshl_b32 s77, s77, 8
ds_read_b64_tr_b16 a[34:35], v19 offset:0
v_mfma_f32_16x16x32_bf16 a[152:155], a[64:67], a[120:123], a[152:155]
buffer_load_dwordx4 v7, s[44:47], s64 offen offset:3072 lds
v_mfma_f32_16x16x32_bf16 a[140:143], a[108:111], a[64:67], a[140:143]
ds_read_b64_tr_b16 a[48:49], v18 offset:16384
v_and_b32 v30, 31, v2
v_mfma_f32_16x16x32_bf16 a[156:159], a[64:67], a[124:127], a[156:159]
v_lshrrev_b32 v31, 4, v30
v_mul_lo_u32 v31, s71, v31
v_mfma_f32_16x16x32_bf16 v[32:35], a[96:99], a[68:71], v[32:35]
s_add_u32 m0, s66, 4096
ds_read_b64_tr_b16 a[50:51], v19 offset:16384
v_add_u32 v30, v30, v31
v_mfma_f32_16x16x32_bf16 v[48:51], a[68:71], a[112:115], v[48:51]
buffer_load_dwordx4 v8, s[44:47], s64 offen offset:0 lds
v_mfma_f32_16x16x32_bf16 v[36:39], a[100:103], a[68:71], v[36:39]
ds_read_b64_tr_b16 a[36:37], v20 offset:0
v_add_u32 v30, s70, v30
v_mfma_f32_16x16x32_bf16 v[52:55], a[68:71], a[116:119], v[52:55]
v_lshlrev_b32 v30, 7, v30
v_mfma_f32_16x16x32_bf16 v[40:43], a[104:107], a[68:71], v[40:43]
ds_read_b64_tr_b16 a[38:39], v21 offset:0
v_mfma_f32_16x16x32_bf16 v[56:59], a[68:71], a[120:123], v[56:59]
buffer_load_dwordx4 v9, s[44:47], s64 offen offset:1024 lds
v_mfma_f32_16x16x32_bf16 v[44:47], a[108:111], a[68:71], v[44:47]
ds_read_b64_tr_b16 a[52:53], v20 offset:16384
v_mfma_f32_16x16x32_bf16 v[60:63], a[68:71], a[124:127], v[60:63]
v_mfma_f32_16x16x32_bf16 v[64:67], a[96:99], a[72:75], v[64:67]
ds_read_b64_tr_b16 a[54:55], v21 offset:16384
v_mfma_f32_16x16x32_bf16 v[80:83], a[72:75], a[112:115], v[80:83]
buffer_load_dwordx4 v10, s[44:47], s64 offen offset:2048 lds
v_mfma_f32_16x16x32_bf16 v[68:71], a[100:103], a[72:75], v[68:71]
ds_read_b64_tr_b16 a[40:41], v18 offset:512
v_mfma_f32_16x16x32_bf16 v[84:87], a[72:75], a[116:119], v[84:87]
v_mfma_f32_16x16x32_bf16 v[72:75], a[104:107], a[72:75], v[72:75]
ds_read_b64_tr_b16 a[42:43], v19 offset:512
v_mfma_f32_16x16x32_bf16 v[88:91], a[72:75], a[120:123], v[88:91]
buffer_load_dwordx4 v11, s[44:47], s64 offen offset:3072 lds
v_mfma_f32_16x16x32_bf16 v[76:79], a[108:111], a[72:75], v[76:79]
ds_read_b64_tr_b16 a[56:57], v18 offset:16896
v_mfma_f32_16x16x32_bf16 v[92:95], a[72:75], a[124:127], v[92:95]
v_mfma_f32_16x16x32_bf16 v[96:99], a[96:99], a[76:79], v[96:99]
s_add_u32 m0, s67, 0
ds_read_b64_tr_b16 a[58:59], v19 offset:16896
v_mfma_f32_16x16x32_bf16 v[112:115], a[76:79], a[112:115], v[112:115]
buffer_load_dwordx4 v12, s[48:51], s65 offen offset:0 lds
v_mfma_f32_16x16x32_bf16 v[100:103], a[100:103], a[76:79], v[100:103]
ds_read_b64_tr_b16 a[44:45], v20 offset:512
v_mfma_f32_16x16x32_bf16 v[116:119], a[76:79], a[116:119], v[116:119]
v_mfma_f32_16x16x32_bf16 v[104:107], a[104:107], a[76:79], v[104:107]
ds_read_b64_tr_b16 a[46:47], v21 offset:512
v_mfma_f32_16x16x32_bf16 v[120:123], a[76:79], a[120:123], v[120:123]
buffer_load_dwordx4 v13, s[48:51], s65 offen offset:1024 lds
v_mfma_f32_16x16x32_bf16 v[108:111], a[108:111], a[76:79], v[108:111]
ds_read_b64_tr_b16 a[60:61], v20 offset:16896
v_mfma_f32_16x16x32_bf16 v[124:127], a[76:79], a[124:127], v[124:127]
v_mfma_f32_16x16x32_bf16 v[128:131], a[96:99], a[80:83], v[128:131]
ds_read_b64_tr_b16 a[62:63], v21 offset:16896
v_mfma_f32_16x16x32_bf16 v[144:147], a[80:83], a[112:115], v[144:147]
buffer_load_dwordx4 v14, s[48:51], s65 offen offset:2048 lds
v_mfma_f32_16x16x32_bf16 v[132:135], a[100:103], a[80:83], v[132:135]
ds_read_b128 a[4:7], v16 offset:2048
v_mfma_f32_16x16x32_bf16 v[148:151], a[80:83], a[116:119], v[148:151]
v_mfma_f32_16x16x32_bf16 v[136:139], a[104:107], a[80:83], v[136:139]
ds_read_b128 a[8:11], v16 offset:4096
v_mfma_f32_16x16x32_bf16 v[152:155], a[80:83], a[120:123], v[152:155]
buffer_load_dwordx4 v15, s[48:51], s65 offen offset:3072 lds
v_mfma_f32_16x16x32_bf16 v[140:143], a[108:111], a[80:83], v[140:143]
ds_read_b128 a[12:15], v16 offset:6144
v_mfma_f32_16x16x32_bf16 v[156:159], a[80:83], a[124:127], v[156:159]
v_mfma_f32_16x16x32_bf16 v[160:163], a[96:99], a[84:87], v[160:163]
s_add_u32 m0, s67, 16384
ds_read_b128 a[16:19], v16 offset:8192
v_mfma_f32_16x16x32_bf16 v[176:179], a[84:87], a[112:115], v[176:179]
buffer_load_dwordx4 v12, s[52:55], s65 offen offset:0 lds
v_mfma_f32_16x16x32_bf16 v[164:167], a[100:103], a[84:87], v[164:167]
ds_read_b128 a[20:23], v16 offset:10240
v_mfma_f32_16x16x32_bf16 v[180:183], a[84:87], a[116:119], v[180:183]
v_mfma_f32_16x16x32_bf16 v[168:171], a[104:107], a[84:87], v[168:171]
ds_read_b128 a[24:27], v16 offset:12288
v_mfma_f32_16x16x32_bf16 v[184:187], a[84:87], a[120:123], v[184:187]
buffer_load_dwordx4 v13, s[52:55], s65 offen offset:1024 lds
v_mfma_f32_16x16x32_bf16 v[172:175], a[108:111], a[84:87], v[172:175]
ds_read_b128 a[28:31], v16 offset:14336
v_mfma_f32_16x16x32_bf16 v[188:191], a[84:87], a[124:127], v[188:191]
v_mfma_f32_16x16x32_bf16 v[192:195], a[96:99], a[88:91], v[192:195]
v_mfma_f32_16x16x32_bf16 v[208:211], a[88:91], a[112:115], v[208:211]
buffer_load_dwordx4 v14, s[52:55], s65 offen offset:2048 lds
v_mfma_f32_16x16x32_bf16 v[196:199], a[100:103], a[88:91], v[196:199]
v_mfma_f32_16x16x32_bf16 v[212:215], a[88:91], a[116:119], v[212:215]
v_mfma_f32_16x16x32_bf16 v[200:203], a[104:107], a[88:91], v[200:203]
v_mfma_f32_16x16x32_bf16 v[216:219], a[88:91], a[120:123], v[216:219]
buffer_load_dwordx4 v15, s[52:55], s65 offen offset:3072 lds
v_mfma_f32_16x16x32_bf16 v[204:207], a[108:111], a[88:91], v[204:207]
s_add_u32 s64, s64, 128
s_add_u32 s65, s65, 524288
v_mfma_f32_16x16x32_bf16 v[220:223], a[88:91], a[124:127], v[220:223]
s_and_b32 s64, s64, 0x3ff
s_and_b32 s65, s65, 0x3fffff
v_mfma_f32_16x16x32_bf16 v[224:227], a[96:99], a[92:95], v[224:227]
s_xor_b32 s66, s66, 0x10000
s_xor_b32 s67, s67, 0x10000
v_mfma_f32_16x16x32_bf16 v[240:243], a[92:95], a[112:115], v[240:243]
v_mfma_f32_16x16x32_bf16 v[228:231], a[100:103], a[92:95], v[228:231]
v_mfma_f32_16x16x32_bf16 v[244:247], a[92:95], a[116:119], v[244:247]
v_mfma_f32_16x16x32_bf16 v[232:235], a[104:107], a[92:95], v[232:235]
v_xor_b32 v16, 0x10000, v16
v_mfma_f32_16x16x32_bf16 v[248:251], a[92:95], a[120:123], v[248:251]
v_mfma_f32_16x16x32_bf16 v[236:239], a[108:111], a[92:95], v[236:239]
buffer_load_dword a200, v22, s[72:75], s77 offen
v_mfma_f32_16x16x32_bf16 v[252:255], a[92:95], a[124:127], v[252:255]
s_waitcnt lgkmcnt(15)
v_mfma_f32_16x16x32_bf16 a[128:131], a[32:35], a[0:3], a[128:131]
ds_read_b128 a[64:67], v17 offset:0
v_mfma_f32_16x16x32_bf16 a[144:147], a[0:3], a[48:51], a[144:147]
v_mfma_f32_16x16x32_bf16 a[132:135], a[36:39], a[0:3], a[132:135]
s_sub_u32 s48, s12, 4096
ds_read_b64_tr_b16 a[96:97], v18 offset:8192
v_mfma_f32_16x16x32_bf16 a[148:151], a[0:3], a[52:55], a[148:151]
s_subb_u32 s49, s13, 0
s_waitcnt lgkmcnt(15)
v_mfma_f32_16x16x32_bf16 a[136:139], a[40:43], a[0:3], a[136:139]
s_and_b32 s49, s49, 0xffff
ds_read_b64_tr_b16 a[98:99], v19 offset:8192
s_waitcnt lgkmcnt(14)
v_mfma_f32_16x16x32_bf16 a[152:155], a[0:3], a[56:59], a[152:155]
s_mov_b32 s50, 4198400
s_waitcnt lgkmcnt(12)
v_mfma_f32_16x16x32_bf16 a[140:143], a[44:47], a[0:3], a[140:143]
s_mov_b32 s51, 0x20000
ds_read_b64_tr_b16 a[112:113], v18 offset:24576
s_waitcnt lgkmcnt(11)
v_mfma_f32_16x16x32_bf16 a[156:159], a[0:3], a[60:63], a[156:159]
s_waitcnt lgkmcnt(10)
v_mfma_f32_16x16x32_bf16 v[32:35], a[32:35], a[4:7], v[32:35]
s_add_u32 s52, s30, 33554432
ds_read_b64_tr_b16 a[114:115], v19 offset:24576
v_mfma_f32_16x16x32_bf16 v[48:51], a[4:7], a[48:51], v[48:51]
s_addc_u32 s53, s31, 0
v_mfma_f32_16x16x32_bf16 v[36:39], a[36:39], a[4:7], v[36:39]
s_and_b32 s53, s53, 0xffff
ds_read_b64_tr_b16 a[100:101], v20 offset:8192
v_mfma_f32_16x16x32_bf16 v[52:55], a[4:7], a[52:55], v[52:55]
s_mov_b32 s54, 2097152
buffer_store_dwordx4 a[216:219], v1, s[56:59], s76 offen offset:2048 sc1
v_mfma_f32_16x16x32_bf16 v[40:43], a[40:43], a[4:7], v[40:43]
s_mov_b32 s55, 0x20000
ds_read_b64_tr_b16 a[102:103], v21 offset:8192
v_mfma_f32_16x16x32_bf16 v[56:59], a[4:7], a[56:59], v[56:59]
v_mfma_f32_16x16x32_bf16 v[44:47], a[44:47], a[4:7], v[44:47]
s_mov_b32 s72, 0
ds_read_b64_tr_b16 a[116:117], v20 offset:24576
v_mfma_f32_16x16x32_bf16 v[60:63], a[4:7], a[60:63], v[60:63]
s_mov_b32 s73, 4096
s_waitcnt lgkmcnt(13)
v_mfma_f32_16x16x32_bf16 v[64:67], a[32:35], a[8:11], v[64:67]
s_mov_b32 s74, 8192
ds_read_b64_tr_b16 a[118:119], v21 offset:24576
v_mfma_f32_16x16x32_bf16 v[80:83], a[8:11], a[48:51], v[80:83]
s_mov_b32 s75, 12288
v_mfma_f32_16x16x32_bf16 v[68:71], a[36:39], a[8:11], v[68:71]
ds_read_b64_tr_b16 a[104:105], v18 offset:8704
v_mfma_f32_16x16x32_bf16 v[84:87], a[8:11], a[52:55], v[84:87]
s_lshl_b32 s70, s61, 3
v_mfma_f32_16x16x32_bf16 v[72:75], a[40:43], a[8:11], v[72:75]
s_lshl_b32 s71, s39, 1
ds_read_b64_tr_b16 a[106:107], v19 offset:8704
v_mfma_f32_16x16x32_bf16 v[88:91], a[8:11], a[56:59], v[88:91]
s_add_u32 s70, s70, s71
v_mfma_f32_16x16x32_bf16 v[76:79], a[44:47], a[8:11], v[76:79]
s_lshl_b32 s70, s70, 14
ds_read_b64_tr_b16 a[120:121], v18 offset:25088
v_mfma_f32_16x16x32_bf16 v[92:95], a[8:11], a[60:63], v[92:95]
s_waitcnt lgkmcnt(15)
v_mfma_f32_16x16x32_bf16 v[96:99], a[32:35], a[12:15], v[96:99]
ds_read_b64_tr_b16 a[122:123], v19 offset:25088
v_mfma_f32_16x16x32_bf16 v[112:115], a[12:15], a[48:51], v[112:115]
v_add_u32 v4, s70, v1
v_mfma_f32_16x16x32_bf16 v[100:103], a[36:39], a[12:15], v[100:103]
ds_read_b64_tr_b16 a[108:109], v20 offset:8704
v_mfma_f32_16x16x32_bf16 v[116:119], a[12:15], a[52:55], v[116:119]
v_add_u32 v5, 16384, v4
v_mfma_f32_16x16x32_bf16 v[104:107], a[40:43], a[12:15], v[104:107]
ds_read_b64_tr_b16 a[110:111], v21 offset:8704
v_mfma_f32_16x16x32_bf16 v[120:123], a[12:15], a[56:59], v[120:123]
s_sub_u32 s70, 15, s61
v_mfma_f32_16x16x32_bf16 v[108:111], a[44:47], a[12:15], v[108:111]
s_lshl_b32 s70, s70, 3
ds_read_b64_tr_b16 a[124:125], v20 offset:25088
v_mfma_f32_16x16x32_bf16 v[124:127], a[12:15], a[60:63], v[124:127]
s_lshl_b32 s71, s39, 1
v_mfma_f32_16x16x32_bf16 v[128:131], a[32:35], a[16:19], v[128:131]
s_add_u32 s70, s70, s71
ds_read_b64_tr_b16 a[126:127], v21 offset:25088
v_mfma_f32_16x16x32_bf16 v[144:147], a[16:19], a[48:51], v[144:147]
s_lshl_b32 s70, s70, 14
buffer_store_dwordx4 a[220:223], v1, s[56:59], s76 offen offset:3072 sc1
v_mfma_f32_16x16x32_bf16 v[132:135], a[36:39], a[16:19], v[132:135]
ds_read_b128 a[68:71], v17 offset:2048
v_mfma_f32_16x16x32_bf16 v[148:151], a[16:19], a[52:55], v[148:151]
v_add_u32 v6, s70, v1
v_mfma_f32_16x16x32_bf16 v[136:139], a[40:43], a[16:19], v[136:139]
ds_read_b128 a[72:75], v17 offset:4096
v_mfma_f32_16x16x32_bf16 v[152:155], a[16:19], a[56:59], v[152:155]
v_add_u32 v7, 16384, v6
v_mfma_f32_16x16x32_bf16 v[140:143], a[44:47], a[16:19], v[140:143]
ds_read_b128 a[76:79], v17 offset:6144
v_mfma_f32_16x16x32_bf16 v[156:159], a[16:19], a[60:63], v[156:159]
s_waitcnt lgkmcnt(15)
v_mfma_f32_16x16x32_bf16 v[160:163], a[32:35], a[20:23], v[160:163]
ds_read_b128 a[80:83], v17 offset:8192
v_mfma_f32_16x16x32_bf16 v[176:179], a[20:23], a[48:51], v[176:179]
s_mov_b32 s79, 32768
buffer_load_dword v31, v30, s[40:43], s79 offen sc1
v_mfma_f32_16x16x32_bf16 v[164:167], a[36:39], a[20:23], v[164:167]
ds_read_b128 a[84:87], v17 offset:10240
v_mfma_f32_16x16x32_bf16 v[180:183], a[20:23], a[52:55], v[180:183]
v_mfma_f32_16x16x32_bf16 v[168:171], a[40:43], a[20:23], v[168:171]
s_mov_b32 s56, s30
ds_read_b128 a[88:91], v17 offset:12288
v_mfma_f32_16x16x32_bf16 v[184:187], a[20:23], a[56:59], v[184:187]
s_mov_b32 s57, s31
v_mfma_f32_16x16x32_bf16 v[172:175], a[44:47], a[20:23], v[172:175]
s_and_b32 s57, s57, 0xffff
ds_read_b128 a[92:95], v17 offset:14336
v_mfma_f32_16x16x32_bf16 v[188:191], a[20:23], a[60:63], v[188:191]
s_mov_b32 s58, 33554432
v_mfma_f32_16x16x32_bf16 v[192:195], a[32:35], a[24:27], v[192:195]
s_mov_b32 s59, 0x20000
v_mfma_f32_16x16x32_bf16 v[208:211], a[24:27], a[48:51], v[208:211]
v_mfma_f32_16x16x32_bf16 v[196:199], a[36:39], a[24:27], v[196:199]
v_mfma_f32_16x16x32_bf16 v[212:215], a[24:27], a[52:55], v[212:215]
v_mfma_f32_16x16x32_bf16 v[200:203], a[40:43], a[24:27], v[200:203]
v_mfma_f32_16x16x32_bf16 v[216:219], a[24:27], a[56:59], v[216:219]
v_mfma_f32_16x16x32_bf16 v[204:207], a[44:47], a[24:27], v[204:207]
v_mfma_f32_16x16x32_bf16 v[220:223], a[24:27], a[60:63], v[220:223]
s_waitcnt vmcnt(4) lgkmcnt(0)
s_barrier
v_mfma_f32_16x16x32_bf16 v[224:227], a[32:35], a[28:31], v[224:227]
s_lshl_b32 s76, s39, 17
v_xor_b32 v18, 0x10000, v18
v_mfma_f32_16x16x32_bf16 v[240:243], a[28:31], a[48:51], v[240:243]
s_add_u32 s76, s76, 0
v_xor_b32 v19, 0x10000, v19
v_mfma_f32_16x16x32_bf16 v[228:231], a[36:39], a[28:31], v[228:231]
s_lshl_b32 s70, s60, 8
v_xor_b32 v20, 0x10000, v20
v_mfma_f32_16x16x32_bf16 v[244:247], a[28:31], a[52:55], v[244:247]
s_add_u32 s76, s76, s70
v_xor_b32 v21, 0x10000, v21
v_mfma_f32_16x16x32_bf16 v[232:235], a[40:43], a[28:31], v[232:235]
s_lshl_b32 s70, s39, 13
v_xor_b32 v17, 0x10000, v17
v_mfma_f32_16x16x32_bf16 v[248:251], a[28:31], a[56:59], v[248:251]
s_add_u32 m0, s70, 0
v_mfma_f32_16x16x32_bf16 v[236:239], a[44:47], a[28:31], v[236:239]
buffer_load_dwordx4 v12, s[48:51], s76 offen offset:0 lds
v_mfma_f32_16x16x32_bf16 v[252:255], a[28:31], a[60:63], v[252:255]
v_mfma_f32_16x16x32_bf16 a[128:131], a[96:99], a[64:67], a[128:131]
ds_read_b128 a[0:3], v16 offset:0
v_mfma_f32_16x16x32_bf16 a[144:147], a[64:67], a[112:115], a[144:147]
v_mfma_f32_16x16x32_bf16 a[132:135], a[100:103], a[64:67], a[132:135]
ds_read_b64_tr_b16 a[32:33], v18 offset:0
buffer_load_dwordx4 v13, s[48:51], s76 offen offset:1024 lds
v_mfma_f32_16x16x32_bf16 a[148:151], a[64:67], a[116:119], a[148:151]
v_mfma_f32_16x16x32_bf16 a[136:139], a[104:107], a[64:67], a[136:139]
ds_read_b64_tr_b16 a[34:35], v19 offset:0
v_mfma_f32_16x16x32_bf16 a[152:155], a[64:67], a[120:123], a[152:155]
v_mfma_f32_16x16x32_bf16 a[140:143], a[108:111], a[64:67], a[140:143]
ds_read_b64_tr_b16 a[48:49], v18 offset:16384
buffer_load_dwordx4 v14, s[48:51], s76 offen offset:2048 lds
v_mfma_f32_16x16x32_bf16 a[156:159], a[64:67], a[124:127], a[156:159]
v_mfma_f32_16x16x32_bf16 v[32:35], a[96:99], a[68:71], v[32:35]
s_lshl_b32 s77, s39, 17
ds_read_b64_tr_b16 a[50:51], v19 offset:16384
v_mfma_f32_16x16x32_bf16 v[48:51], a[68:71], a[112:115], v[48:51]
s_add_u32 s77, s77, 131072
v_mfma_f32_16x16x32_bf16 v[36:39], a[100:103], a[68:71], v[36:39]
s_lshl_b32 s70, s60, 8
ds_read_b64_tr_b16 a[36:37], v20 offset:0
)ASM"
R"ASM(buffer_load_dwordx4 v15, s[48:51], s76 offen offset:3072 lds
v_mfma_f32_16x16x32_bf16 v[52:55], a[68:71], a[116:119], v[52:55]
s_add_u32 s77, s77, s70
v_mfma_f32_16x16x32_bf16 v[40:43], a[104:107], a[68:71], v[40:43]
s_lshl_b32 s70, s39, 13
ds_read_b64_tr_b16 a[38:39], v21 offset:0
v_mfma_f32_16x16x32_bf16 v[56:59], a[68:71], a[120:123], v[56:59]
s_add_u32 m0, s70, 4096
v_mfma_f32_16x16x32_bf16 v[44:47], a[108:111], a[68:71], v[44:47]
ds_read_b64_tr_b16 a[52:53], v20 offset:16384
buffer_load_dwordx4 v12, s[48:51], s77 offen offset:0 lds
v_mfma_f32_16x16x32_bf16 v[60:63], a[68:71], a[124:127], v[60:63]
v_mfma_f32_16x16x32_bf16 v[64:67], a[96:99], a[72:75], v[64:67]
ds_read_b64_tr_b16 a[54:55], v21 offset:16384
v_mfma_f32_16x16x32_bf16 v[80:83], a[72:75], a[112:115], v[80:83]
v_mfma_f32_16x16x32_bf16 v[68:71], a[100:103], a[72:75], v[68:71]
ds_read_b64_tr_b16 a[40:41], v18 offset:512
buffer_load_dwordx4 v13, s[48:51], s77 offen offset:1024 lds
v_mfma_f32_16x16x32_bf16 v[84:87], a[72:75], a[116:119], v[84:87]
v_mfma_f32_16x16x32_bf16 v[72:75], a[104:107], a[72:75], v[72:75]
ds_read_b64_tr_b16 a[42:43], v19 offset:512
v_mfma_f32_16x16x32_bf16 v[88:91], a[72:75], a[120:123], v[88:91]
v_mfma_f32_16x16x32_bf16 v[76:79], a[108:111], a[72:75], v[76:79]
ds_read_b64_tr_b16 a[56:57], v18 offset:16896
buffer_load_dwordx4 v14, s[48:51], s77 offen offset:2048 lds
v_mfma_f32_16x16x32_bf16 v[92:95], a[72:75], a[124:127], v[92:95]
v_mfma_f32_16x16x32_bf16 v[96:99], a[96:99], a[76:79], v[96:99]
s_lshl_b32 s76, s39, 17
ds_read_b64_tr_b16 a[58:59], v19 offset:16896
v_mfma_f32_16x16x32_bf16 v[112:115], a[76:79], a[112:115], v[112:115]
s_add_u32 s76, s76, 1048576
v_mfma_f32_16x16x32_bf16 v[100:103], a[100:103], a[76:79], v[100:103]
s_lshl_b32 s70, s60, 8
ds_read_b64_tr_b16 a[44:45], v20 offset:512
buffer_load_dwordx4 v15, s[48:51], s77 offen offset:3072 lds
v_mfma_f32_16x16x32_bf16 v[116:119], a[76:79], a[116:119], v[116:119]
s_add_u32 s76, s76, s70
v_mfma_f32_16x16x32_bf16 v[104:107], a[104:107], a[76:79], v[104:107]
s_lshl_b32 s70, s39, 13
ds_read_b64_tr_b16 a[46:47], v21 offset:512
v_mfma_f32_16x16x32_bf16 v[120:123], a[76:79], a[120:123], v[120:123]
s_add_u32 m0, s70, 32768
v_mfma_f32_16x16x32_bf16 v[108:111], a[108:111], a[76:79], v[108:111]
ds_read_b64_tr_b16 a[60:61], v20 offset:16896
buffer_load_dwordx4 v12, s[48:51], s76 offen offset:0 lds
v_mfma_f32_16x16x32_bf16 v[124:127], a[76:79], a[124:127], v[124:127]
v_mfma_f32_16x16x32_bf16 v[128:131], a[96:99], a[80:83], v[128:131]
ds_read_b64_tr_b16 a[62:63], v21 offset:16896
v_mfma_f32_16x16x32_bf16 v[144:147], a[80:83], a[112:115], v[144:147]
v_mfma_f32_16x16x32_bf16 v[132:135], a[100:103], a[80:83], v[132:135]
ds_read_b128 a[4:7], v16 offset:2048
buffer_load_dwordx4 v13, s[48:51], s76 offen offset:1024 lds
v_mfma_f32_16x16x32_bf16 v[148:151], a[80:83], a[116:119], v[148:151]
v_mfma_f32_16x16x32_bf16 v[136:139], a[104:107], a[80:83], v[136:139]
ds_read_b128 a[8:11], v16 offset:4096
v_mfma_f32_16x16x32_bf16 v[152:155], a[80:83], a[120:123], v[152:155]
v_mfma_f32_16x16x32_bf16 v[140:143], a[108:111], a[80:83], v[140:143]
ds_read_b128 a[12:15], v16 offset:6144
buffer_load_dwordx4 v14, s[48:51], s76 offen offset:2048 lds
v_mfma_f32_16x16x32_bf16 v[156:159], a[80:83], a[124:127], v[156:159]
v_mfma_f32_16x16x32_bf16 v[160:163], a[96:99], a[84:87], v[160:163]
s_lshl_b32 s77, s39, 17
ds_read_b128 a[16:19], v16 offset:8192
v_mfma_f32_16x16x32_bf16 v[176:179], a[84:87], a[112:115], v[176:179]
s_add_u32 s77, s77, 1179648
v_mfma_f32_16x16x32_bf16 v[164:167], a[100:103], a[84:87], v[164:167]
s_lshl_b32 s70, s60, 8
ds_read_b128 a[20:23], v16 offset:10240
buffer_load_dwordx4 v15, s[48:51], s76 offen offset:3072 lds
v_mfma_f32_16x16x32_bf16 v[180:183], a[84:87], a[116:119], v[180:183]
s_add_u32 s77, s77, s70
v_mfma_f32_16x16x32_bf16 v[168:171], a[104:107], a[84:87], v[168:171]
s_lshl_b32 s70, s39, 13
ds_read_b128 a[24:27], v16 offset:12288
v_mfma_f32_16x16x32_bf16 v[184:187], a[84:87], a[120:123], v[184:187]
s_add_u32 m0, s70, 36864
v_mfma_f32_16x16x32_bf16 v[172:175], a[108:111], a[84:87], v[172:175]
ds_read_b128 a[28:31], v16 offset:14336
buffer_load_dwordx4 v12, s[48:51], s77 offen offset:0 lds
v_mfma_f32_16x16x32_bf16 v[188:191], a[84:87], a[124:127], v[188:191]
v_mfma_f32_16x16x32_bf16 v[192:195], a[96:99], a[88:91], v[192:195]
v_mfma_f32_16x16x32_bf16 v[208:211], a[88:91], a[112:115], v[208:211]
v_mfma_f32_16x16x32_bf16 v[196:199], a[100:103], a[88:91], v[196:199]
buffer_load_dwordx4 v13, s[48:51], s77 offen offset:1024 lds
v_mfma_f32_16x16x32_bf16 v[212:215], a[88:91], a[116:119], v[212:215]
v_mfma_f32_16x16x32_bf16 v[200:203], a[104:107], a[88:91], v[200:203]
v_mfma_f32_16x16x32_bf16 v[216:219], a[88:91], a[120:123], v[216:219]
v_mfma_f32_16x16x32_bf16 v[204:207], a[108:111], a[88:91], v[204:207]
buffer_load_dwordx4 v14, s[48:51], s77 offen offset:2048 lds
v_mfma_f32_16x16x32_bf16 v[220:223], a[88:91], a[124:127], v[220:223]
v_mfma_f32_16x16x32_bf16 v[224:227], a[96:99], a[92:95], v[224:227]
v_mfma_f32_16x16x32_bf16 v[240:243], a[92:95], a[112:115], v[240:243]
v_mfma_f32_16x16x32_bf16 v[228:231], a[100:103], a[92:95], v[228:231]
buffer_load_dwordx4 v15, s[48:51], s77 offen offset:3072 lds
v_mfma_f32_16x16x32_bf16 v[244:247], a[92:95], a[116:119], v[244:247]
v_mfma_f32_16x16x32_bf16 v[232:235], a[104:107], a[92:95], v[232:235]
v_xor_b32 v16, 0x10000, v16
v_mfma_f32_16x16x32_bf16 v[248:251], a[92:95], a[120:123], v[248:251]
v_mfma_f32_16x16x32_bf16 v[236:239], a[108:111], a[92:95], v[236:239]
v_mfma_f32_16x16x32_bf16 v[252:255], a[92:95], a[124:127], v[252:255]
s_waitcnt vmcnt(16)
v_cmp_ne_u32 vcc, s33, v31
s_waitcnt lgkmcnt(15)
v_mfma_f32_16x16x32_bf16 a[128:131], a[32:35], a[0:3], a[128:131]
s_and_b32 s70, s39, 1
s_lshl_b32 s70, s70, 10
s_add_u32 s70, s70, 32768
ds_read_b128 a[64:67], v17 offset:0
v_mfma_f32_16x16x32_bf16 a[144:147], a[0:3], a[48:51], a[144:147]
v_mfma_f32_16x16x32_bf16 a[132:135], a[36:39], a[0:3], a[132:135]
ds_read_b64_tr_b16 a[96:97], v18 offset:8192
s_nop 1
s_cbranch_vccnz L_slow1%=
L_back1%=:
v_mfma_f32_16x16x32_bf16 a[148:151], a[0:3], a[52:55], a[148:151]
buffer_load_dwordx4 a[160:163], v4, s[52:55], s72 offen offset:0 sc1
v_and_b32 v8, 0xffff, v18
s_waitcnt lgkmcnt(15)
v_mfma_f32_16x16x32_bf16 a[136:139], a[40:43], a[0:3], a[136:139]
ds_read_b64_tr_b16 a[98:99], v19 offset:8192
v_subrev_u32 v8, s70, v8
s_waitcnt lgkmcnt(14)
v_mfma_f32_16x16x32_bf16 a[152:155], a[0:3], a[56:59], a[152:155]
buffer_load_dwordx4 a[164:167], v5, s[52:55], s72 offen offset:0 sc1
v_and_b32 v9, 0xffff, v19
s_waitcnt lgkmcnt(12)
v_mfma_f32_16x16x32_bf16 a[140:143], a[44:47], a[0:3], a[140:143]
ds_read_b64_tr_b16 a[112:113], v18 offset:24576
v_subrev_u32 v9, s70, v9
s_waitcnt lgkmcnt(11)
v_mfma_f32_16x16x32_bf16 a[156:159], a[0:3], a[60:63], a[156:159]
buffer_load_dwordx4 a[168:171], v6, s[52:55], s72 offen offset:0 sc1
v_and_b32 v10, 0xffff, v20
s_waitcnt lgkmcnt(10)
v_mfma_f32_16x16x32_bf16 v[32:35], a[32:35], a[4:7], v[32:35]
ds_read_b64_tr_b16 a[114:115], v19 offset:24576
v_subrev_u32 v10, s70, v10
v_mfma_f32_16x16x32_bf16 v[48:51], a[4:7], a[48:51], v[48:51]
buffer_load_dwordx4 a[172:175], v7, s[52:55], s72 offen offset:0 sc1
v_and_b32 v11, 0xffff, v21
v_mfma_f32_16x16x32_bf16 v[36:39], a[36:39], a[4:7], v[36:39]
ds_read_b64_tr_b16 a[100:101], v20 offset:8192
v_subrev_u32 v11, s70, v11
v_mfma_f32_16x16x32_bf16 v[52:55], a[4:7], a[52:55], v[52:55]
buffer_load_dwordx4 a[176:179], v4, s[52:55], s72 offen offset:1024 sc1
v_mfma_f32_16x16x32_bf16 v[40:43], a[40:43], a[4:7], v[40:43]
ds_read_b64_tr_b16 a[102:103], v21 offset:8192
v_mfma_f32_16x16x32_bf16 v[56:59], a[4:7], a[56:59], v[56:59]
buffer_load_dwordx4 a[180:183], v5, s[52:55], s72 offen offset:1024 sc1
v_mfma_f32_16x16x32_bf16 v[44:47], a[44:47], a[4:7], v[44:47]
ds_read_b64_tr_b16 a[116:117], v20 offset:24576
v_mfma_f32_16x16x32_bf16 v[60:63], a[4:7], a[60:63], v[60:63]
buffer_load_dwordx4 a[184:187], v6, s[52:55], s72 offen offset:1024 sc1
s_waitcnt lgkmcnt(13)
v_mfma_f32_16x16x32_bf16 v[64:67], a[32:35], a[8:11], v[64:67]
ds_read_b64_tr_b16 a[118:119], v21 offset:24576
v_mfma_f32_16x16x32_bf16 v[80:83], a[8:11], a[48:51], v[80:83]
buffer_load_dwordx4 a[188:191], v7, s[52:55], s72 offen offset:1024 sc1
v_mfma_f32_16x16x32_bf16 v[68:71], a[36:39], a[8:11], v[68:71]
ds_read_b64_tr_b16 a[104:105], v18 offset:8704
v_mfma_f32_16x16x32_bf16 v[84:87], a[8:11], a[52:55], v[84:87]
buffer_load_dwordx4 a[192:195], v4, s[52:55], s72 offen offset:2048 sc1
v_mfma_f32_16x16x32_bf16 v[72:75], a[40:43], a[8:11], v[72:75]
ds_read_b64_tr_b16 a[106:107], v19 offset:8704
v_mfma_f32_16x16x32_bf16 v[88:91], a[8:11], a[56:59], v[88:91]
buffer_load_dwordx4 a[196:199], v5, s[52:55], s72 offen offset:2048 sc1
v_mfma_f32_16x16x32_bf16 v[76:79], a[44:47], a[8:11], v[76:79]
ds_read_b64_tr_b16 a[120:121], v18 offset:25088
v_mfma_f32_16x16x32_bf16 v[92:95], a[8:11], a[60:63], v[92:95]
buffer_load_dwordx4 a[200:203], v6, s[52:55], s72 offen offset:2048 sc1
s_waitcnt lgkmcnt(15)
v_mfma_f32_16x16x32_bf16 v[96:99], a[32:35], a[12:15], v[96:99]
ds_read_b64_tr_b16 a[122:123], v19 offset:25088
v_mfma_f32_16x16x32_bf16 v[112:115], a[12:15], a[48:51], v[112:115]
buffer_load_dwordx4 a[204:207], v7, s[52:55], s72 offen offset:2048 sc1
v_mfma_f32_16x16x32_bf16 v[100:103], a[36:39], a[12:15], v[100:103]
ds_read_b64_tr_b16 a[108:109], v20 offset:8704
v_mfma_f32_16x16x32_bf16 v[116:119], a[12:15], a[52:55], v[116:119]
buffer_load_dwordx4 a[208:211], v4, s[52:55], s72 offen offset:3072 sc1
v_mfma_f32_16x16x32_bf16 v[104:107], a[40:43], a[12:15], v[104:107]
ds_read_b64_tr_b16 a[110:111], v21 offset:8704
v_mfma_f32_16x16x32_bf16 v[120:123], a[12:15], a[56:59], v[120:123]
buffer_load_dwordx4 a[212:215], v5, s[52:55], s72 offen offset:3072 sc1
v_mfma_f32_16x16x32_bf16 v[108:111], a[44:47], a[12:15], v[108:111]
ds_read_b64_tr_b16 a[124:125], v20 offset:25088
v_mfma_f32_16x16x32_bf16 v[124:127], a[12:15], a[60:63], v[124:127]
buffer_load_dwordx4 a[216:219], v6, s[52:55], s72 offen offset:3072 sc1
v_mfma_f32_16x16x32_bf16 v[128:131], a[32:35], a[16:19], v[128:131]
ds_read_b64_tr_b16 a[126:127], v21 offset:25088
v_mfma_f32_16x16x32_bf16 v[144:147], a[16:19], a[48:51], v[144:147]
buffer_load_dwordx4 a[220:223], v7, s[52:55], s72 offen offset:3072 sc1
v_mfma_f32_16x16x32_bf16 v[132:135], a[36:39], a[16:19], v[132:135]
ds_read_b128 a[68:71], v17 offset:2048
v_mfma_f32_16x16x32_bf16 v[148:151], a[16:19], a[52:55], v[148:151]
buffer_load_dwordx4 a[224:227], v4, s[52:55], s73 offen offset:0 sc1
v_mfma_f32_16x16x32_bf16 v[136:139], a[40:43], a[16:19], v[136:139]
ds_read_b128 a[72:75], v17 offset:4096
v_mfma_f32_16x16x32_bf16 v[152:155], a[16:19], a[56:59], v[152:155]
buffer_load_dwordx4 a[228:231], v5, s[52:55], s73 offen offset:0 sc1
v_mfma_f32_16x16x32_bf16 v[140:143], a[44:47], a[16:19], v[140:143]
ds_read_b128 a[76:79], v17 offset:6144
v_mfma_f32_16x16x32_bf16 v[156:159], a[16:19], a[60:63], v[156:159]
buffer_load_dwordx4 a[232:235], v6, s[52:55], s73 offen offset:0 sc1
s_waitcnt lgkmcnt(15)
v_mfma_f32_16x16x32_bf16 v[160:163], a[32:35], a[20:23], v[160:163]
ds_read_b128 a[80:83], v17 offset:8192
v_mfma_f32_16x16x32_bf16 v[176:179], a[20:23], a[48:51], v[176:179]
buffer_load_dwordx4 a[236:239], v7, s[52:55], s73 offen offset:0 sc1
v_mfma_f32_16x16x32_bf16 v[164:167], a[36:39], a[20:23], v[164:167]
ds_read_b128 a[84:87], v17 offset:10240
v_mfma_f32_16x16x32_bf16 v[180:183], a[20:23], a[52:55], v[180:183]
buffer_load_dwordx4 a[240:243], v4, s[52:55], s73 offen offset:1024 sc1
v_mfma_f32_16x16x32_bf16 v[168:171], a[40:43], a[20:23], v[168:171]
ds_read_b128 a[88:91], v17 offset:12288
v_mfma_f32_16x16x32_bf16 v[184:187], a[20:23], a[56:59], v[184:187]
buffer_load_dwordx4 a[244:247], v5, s[52:55], s73 offen offset:1024 sc1
v_mfma_f32_16x16x32_bf16 v[172:175], a[44:47], a[20:23], v[172:175]
ds_read_b128 a[92:95], v17 offset:14336
v_mfma_f32_16x16x32_bf16 v[188:191], a[20:23], a[60:63], v[188:191]
buffer_load_dwordx4 a[248:251], v6, s[52:55], s73 offen offset:1024 sc1
v_mfma_f32_16x16x32_bf16 v[192:195], a[32:35], a[24:27], v[192:195]
v_mfma_f32_16x16x32_bf16 v[208:211], a[24:27], a[48:51], v[208:211]
buffer_load_dwordx4 a[252:255], v7, s[52:55], s73 offen offset:1024 sc1
v_mfma_f32_16x16x32_bf16 v[196:199], a[36:39], a[24:27], v[196:199]
v_mfma_f32_16x16x32_bf16 v[212:215], a[24:27], a[52:55], v[212:215]
v_mfma_f32_16x16x32_bf16 v[200:203], a[40:43], a[24:27], v[200:203]
v_mfma_f32_16x16x32_bf16 v[216:219], a[24:27], a[56:59], v[216:219]
v_mfma_f32_16x16x32_bf16 v[204:207], a[44:47], a[24:27], v[204:207]
v_mfma_f32_16x16x32_bf16 v[220:223], a[24:27], a[60:63], v[220:223]
s_waitcnt lgkmcnt(0)
s_barrier
v_mfma_f32_16x16x32_bf16 v[224:227], a[32:35], a[28:31], v[224:227]
v_mfma_f32_16x16x32_bf16 v[240:243], a[28:31], a[48:51], v[240:243]
v_mfma_f32_16x16x32_bf16 v[228:231], a[36:39], a[28:31], v[228:231]
v_mfma_f32_16x16x32_bf16 v[244:247], a[28:31], a[52:55], v[244:247]
v_mfma_f32_16x16x32_bf16 v[232:235], a[40:43], a[28:31], v[232:235]
v_mfma_f32_16x16x32_bf16 v[248:251], a[28:31], a[56:59], v[248:251]
v_mfma_f32_16x16x32_bf16 v[236:239], a[44:47], a[28:31], v[236:239]
v_mfma_f32_16x16x32_bf16 v[252:255], a[28:31], a[60:63], v[252:255]
s_nop 0
v_mfma_f32_16x16x32_bf16 a[128:131], a[96:99], a[64:67], a[128:131]
v_mfma_f32_16x16x32_bf16 a[144:147], a[64:67], a[112:115], a[144:147]
v_mfma_f32_16x16x32_bf16 a[132:135], a[100:103], a[64:67], a[132:135]
v_mfma_f32_16x16x32_bf16 a[148:151], a[64:67], a[116:119], a[148:151]
v_mfma_f32_16x16x32_bf16 a[136:139], a[104:107], a[64:67], a[136:139]
v_mfma_f32_16x16x32_bf16 a[152:155], a[64:67], a[120:123], a[152:155]
v_mfma_f32_16x16x32_bf16 a[140:143], a[108:111], a[64:67], a[140:143]
v_mfma_f32_16x16x32_bf16 a[156:159], a[64:67], a[124:127], a[156:159]
s_nop 0
v_accvgpr_read_b32 v24, a128
v_accvgpr_read_b32 v25, a129
v_mfma_f32_16x16x32_bf16 v[32:35], a[96:99], a[68:71], v[32:35]
v_accvgpr_read_b32 v26, a130
v_accvgpr_read_b32 v27, a131
v_mfma_f32_16x16x32_bf16 v[48:51], a[68:71], a[112:115], v[48:51]
v_accvgpr_read_b32 v28, a132
v_accvgpr_read_b32 v29, a133
v_mfma_f32_16x16x32_bf16 v[36:39], a[100:103], a[68:71], v[36:39]
v_accvgpr_read_b32 v30, a134
v_accvgpr_read_b32 v31, a135
v_mfma_f32_16x16x32_bf16 v[52:55], a[68:71], a[116:119], v[52:55]
v_cvt_pk_bf16_f32 v24, v24, v25
v_cvt_pk_bf16_f32 v25, v26, v27
v_mfma_f32_16x16x32_bf16 v[40:43], a[104:107], a[68:71], v[40:43]
v_cvt_pk_bf16_f32 v26, v28, v29
v_cvt_pk_bf16_f32 v27, v30, v31
v_mfma_f32_16x16x32_bf16 v[56:59], a[68:71], a[120:123], v[56:59]
s_add_u32 s62, s68, 0
buffer_store_dwordx4 v[24:27], v1, s[56:59], s62 offen offset:0 sc1
v_mfma_f32_16x16x32_bf16 v[44:47], a[108:111], a[68:71], v[44:47]
s_nop 1
v_accvgpr_read_b32 v24, a136
v_accvgpr_read_b32 v25, a137
v_mfma_f32_16x16x32_bf16 v[60:63], a[68:71], a[124:127], v[60:63]
v_accvgpr_read_b32 v26, a138
v_accvgpr_read_b32 v27, a139
v_mfma_f32_16x16x32_bf16 v[64:67], a[96:99], a[72:75], v[64:67]
v_accvgpr_read_b32 v28, a140
v_accvgpr_read_b32 v29, a141
v_mfma_f32_16x16x32_bf16 v[80:83], a[72:75], a[112:115], v[80:83]
v_accvgpr_read_b32 v30, a142
v_accvgpr_read_b32 v31, a143
v_mfma_f32_16x16x32_bf16 v[68:71], a[100:103], a[72:75], v[68:71]
v_cvt_pk_bf16_f32 v24, v24, v25
v_cvt_pk_bf16_f32 v25, v26, v27
v_mfma_f32_16x16x32_bf16 v[84:87], a[72:75], a[116:119], v[84:87]
v_cvt_pk_bf16_f32 v26, v28, v29
v_cvt_pk_bf16_f32 v27, v30, v31
v_mfma_f32_16x16x32_bf16 v[72:75], a[104:107], a[72:75], v[72:75]
s_add_u32 s62, s68, 0
buffer_store_dwordx4 v[24:27], v1, s[56:59], s62 offen offset:1024 sc1
v_mfma_f32_16x16x32_bf16 v[88:91], a[72:75], a[120:123], v[88:91]
v_cvt_pk_bf16_f32 v32, v32, v33
v_cvt_pk_bf16_f32 v33, v34, v35
v_mfma_f32_16x16x32_bf16 v[76:79], a[108:111], a[72:75], v[76:79]
v_cvt_pk_bf16_f32 v34, v36, v37
v_cvt_pk_bf16_f32 v35, v38, v39
v_mfma_f32_16x16x32_bf16 v[92:95], a[72:75], a[124:127], v[92:95]
s_add_u32 s62, s68, 4096
buffer_store_dwordx4 v[32:35], v1, s[56:59], s62 offen offset:0 sc1
)ASM"
R"ASM(v_mfma_f32_16x16x32_bf16 v[96:99], a[96:99], a[76:79], v[96:99]
v_cvt_pk_bf16_f32 v40, v40, v41
v_cvt_pk_bf16_f32 v41, v42, v43
v_mfma_f32_16x16x32_bf16 v[112:115], a[76:79], a[112:115], v[112:115]
v_cvt_pk_bf16_f32 v42, v44, v45
v_cvt_pk_bf16_f32 v43, v46, v47
v_mfma_f32_16x16x32_bf16 v[100:103], a[100:103], a[76:79], v[100:103]
s_add_u32 s62, s68, 4096
buffer_store_dwordx4 v[40:43], v1, s[56:59], s62 offen offset:1024 sc1
v_mfma_f32_16x16x32_bf16 v[116:119], a[76:79], a[116:119], v[116:119]
v_accvgpr_read_b32 v24, a144
v_accvgpr_read_b32 v25, a145
v_mfma_f32_16x16x32_bf16 v[104:107], a[104:107], a[76:79], v[104:107]
v_accvgpr_read_b32 v26, a146
v_accvgpr_read_b32 v27, a147
s_waitcnt vmcnt(28)
s_barrier
v_mfma_f32_16x16x32_bf16 v[120:123], a[76:79], a[120:123], v[120:123]
v_cvt_pk_bf16_f32 v24, v24, v25
v_cvt_pk_bf16_f32 v25, v26, v27
v_mfma_f32_16x16x32_bf16 v[108:111], a[108:111], a[76:79], v[108:111]
ds_read_b64_tr_b16 a[0:1], v8 offset:0
v_cvt_pk_bf16_f32 v26, v48, v49
v_cvt_pk_bf16_f32 v27, v50, v51
v_mfma_f32_16x16x32_bf16 v[124:127], a[76:79], a[124:127], v[124:127]
s_add_u32 s62, s69, 16384
buffer_store_dwordx4 v[24:27], v1, s[56:59], s62 offen offset:0 sc1
v_mfma_f32_16x16x32_bf16 v[128:131], a[96:99], a[80:83], v[128:131]
ds_read_b64_tr_b16 a[2:3], v9 offset:0
v_accvgpr_read_b32 v24, a148
v_accvgpr_read_b32 v25, a149
v_mfma_f32_16x16x32_bf16 v[144:147], a[80:83], a[112:115], v[144:147]
v_accvgpr_read_b32 v26, a150
v_accvgpr_read_b32 v27, a151
v_mfma_f32_16x16x32_bf16 v[132:135], a[100:103], a[80:83], v[132:135]
ds_read_b64_tr_b16 a[4:5], v10 offset:0
v_cvt_pk_bf16_f32 v24, v24, v25
v_cvt_pk_bf16_f32 v25, v26, v27
v_mfma_f32_16x16x32_bf16 v[148:151], a[80:83], a[116:119], v[148:151]
v_cvt_pk_bf16_f32 v26, v52, v53
v_cvt_pk_bf16_f32 v27, v54, v55
v_mfma_f32_16x16x32_bf16 v[136:139], a[104:107], a[80:83], v[136:139]
ds_read_b64_tr_b16 a[6:7], v11 offset:0
s_add_u32 s62, s69, 16384
buffer_store_dwordx4 v[24:27], v1, s[56:59], s62 offen offset:1024 sc1
v_mfma_f32_16x16x32_bf16 v[152:155], a[80:83], a[120:123], v[152:155]
s_nop 1
v_accvgpr_read_b32 v24, a152
v_accvgpr_read_b32 v25, a153
v_mfma_f32_16x16x32_bf16 v[140:143], a[108:111], a[80:83], v[140:143]
ds_read_b64_tr_b16 a[8:9], v8 offset:512
v_accvgpr_read_b32 v26, a154
v_accvgpr_read_b32 v27, a155
v_mfma_f32_16x16x32_bf16 v[156:159], a[80:83], a[124:127], v[156:159]
v_cvt_pk_bf16_f32 v24, v24, v25
v_cvt_pk_bf16_f32 v25, v26, v27
v_mfma_f32_16x16x32_bf16 v[160:163], a[96:99], a[84:87], v[160:163]
ds_read_b64_tr_b16 a[10:11], v9 offset:512
v_cvt_pk_bf16_f32 v26, v56, v57
v_cvt_pk_bf16_f32 v27, v58, v59
v_mfma_f32_16x16x32_bf16 v[176:179], a[84:87], a[112:115], v[176:179]
s_add_u32 s62, s69, 16384
buffer_store_dwordx4 v[24:27], v1, s[56:59], s62 offen offset:2048 sc1
v_mfma_f32_16x16x32_bf16 v[164:167], a[100:103], a[84:87], v[164:167]
ds_read_b64_tr_b16 a[12:13], v10 offset:512
v_accvgpr_read_b32 v24, a156
v_accvgpr_read_b32 v25, a157
v_mfma_f32_16x16x32_bf16 v[180:183], a[84:87], a[116:119], v[180:183]
v_accvgpr_read_b32 v26, a158
v_accvgpr_read_b32 v27, a159
v_mfma_f32_16x16x32_bf16 v[168:171], a[104:107], a[84:87], v[168:171]
ds_read_b64_tr_b16 a[14:15], v11 offset:512
v_cvt_pk_bf16_f32 v24, v24, v25
v_cvt_pk_bf16_f32 v25, v26, v27
v_mfma_f32_16x16x32_bf16 v[184:187], a[84:87], a[120:123], v[184:187]
v_cvt_pk_bf16_f32 v26, v60, v61
v_cvt_pk_bf16_f32 v27, v62, v63
v_mfma_f32_16x16x32_bf16 v[172:175], a[108:111], a[84:87], v[172:175]
ds_read_b64_tr_b16 a[16:17], v8 offset:1024
s_add_u32 s62, s69, 16384
buffer_store_dwordx4 v[24:27], v1, s[56:59], s62 offen offset:3072 sc1
v_mfma_f32_16x16x32_bf16 v[188:191], a[84:87], a[124:127], v[188:191]
v_cvt_pk_bf16_f32 v64, v64, v65
v_cvt_pk_bf16_f32 v65, v66, v67
v_mfma_f32_16x16x32_bf16 v[192:195], a[96:99], a[88:91], v[192:195]
ds_read_b64_tr_b16 a[18:19], v9 offset:1024
v_cvt_pk_bf16_f32 v66, v68, v69
v_cvt_pk_bf16_f32 v67, v70, v71
v_mfma_f32_16x16x32_bf16 v[208:211], a[88:91], a[112:115], v[208:211]
v_cvt_pk_bf16_f32 v72, v72, v73
v_cvt_pk_bf16_f32 v73, v74, v75
v_mfma_f32_16x16x32_bf16 v[196:199], a[100:103], a[88:91], v[196:199]
ds_read_b64_tr_b16 a[20:21], v10 offset:1024
v_cvt_pk_bf16_f32 v74, v76, v77
v_cvt_pk_bf16_f32 v75, v78, v79
v_mfma_f32_16x16x32_bf16 v[212:215], a[88:91], a[116:119], v[212:215]
v_cvt_pk_bf16_f32 v96, v96, v97
v_cvt_pk_bf16_f32 v97, v98, v99
v_mfma_f32_16x16x32_bf16 v[200:203], a[104:107], a[88:91], v[200:203]
ds_read_b64_tr_b16 a[22:23], v11 offset:1024
v_cvt_pk_bf16_f32 v98, v100, v101
v_cvt_pk_bf16_f32 v99, v102, v103
v_mfma_f32_16x16x32_bf16 v[216:219], a[88:91], a[120:123], v[216:219]
v_cvt_pk_bf16_f32 v104, v104, v105
v_cvt_pk_bf16_f32 v105, v106, v107
v_mfma_f32_16x16x32_bf16 v[204:207], a[108:111], a[88:91], v[204:207]
ds_read_b64_tr_b16 a[24:25], v8 offset:1536
v_cvt_pk_bf16_f32 v106, v108, v109
v_cvt_pk_bf16_f32 v107, v110, v111
v_mfma_f32_16x16x32_bf16 v[220:223], a[88:91], a[124:127], v[220:223]
v_cvt_pk_bf16_f32 v80, v80, v81
v_cvt_pk_bf16_f32 v81, v82, v83
v_mfma_f32_16x16x32_bf16 v[224:227], a[96:99], a[92:95], v[224:227]
ds_read_b64_tr_b16 a[26:27], v9 offset:1536
v_cvt_pk_bf16_f32 v82, v112, v113
v_cvt_pk_bf16_f32 v83, v114, v115
v_mfma_f32_16x16x32_bf16 v[240:243], a[92:95], a[112:115], v[240:243]
v_cvt_pk_bf16_f32 v84, v84, v85
v_cvt_pk_bf16_f32 v85, v86, v87
v_mfma_f32_16x16x32_bf16 v[228:231], a[100:103], a[92:95], v[228:231]
ds_read_b64_tr_b16 a[28:29], v10 offset:1536
v_cvt_pk_bf16_f32 v86, v116, v117
v_cvt_pk_bf16_f32 v87, v118, v119
v_mfma_f32_16x16x32_bf16 v[244:247], a[92:95], a[116:119], v[244:247]
v_cvt_pk_bf16_f32 v88, v88, v89
v_cvt_pk_bf16_f32 v89, v90, v91
v_mfma_f32_16x16x32_bf16 v[232:235], a[104:107], a[92:95], v[232:235]
ds_read_b64_tr_b16 a[30:31], v11 offset:1536
v_cvt_pk_bf16_f32 v90, v120, v121
v_cvt_pk_bf16_f32 v91, v122, v123
v_mfma_f32_16x16x32_bf16 v[248:251], a[92:95], a[120:123], v[248:251]
v_cvt_pk_bf16_f32 v92, v92, v93
v_cvt_pk_bf16_f32 v93, v94, v95
v_mfma_f32_16x16x32_bf16 v[236:239], a[108:111], a[92:95], v[236:239]
v_cvt_pk_bf16_f32 v94, v124, v125
v_cvt_pk_bf16_f32 v95, v126, v127
v_mfma_f32_16x16x32_bf16 v[252:255], a[92:95], a[124:127], v[252:255]
v_cvt_pk_bf16_f32 v36, v128, v129
v_cvt_pk_bf16_f32 v37, v130, v131
v_cvt_pk_bf16_f32 v38, v132, v133
v_cvt_pk_bf16_f32 v39, v134, v135
v_cvt_pk_bf16_f32 v32, v136, v137
v_cvt_pk_bf16_f32 v33, v138, v139
v_cvt_pk_bf16_f32 v34, v140, v141
v_cvt_pk_bf16_f32 v35, v142, v143
v_cvt_pk_bf16_f32 v44, v160, v161
v_cvt_pk_bf16_f32 v45, v162, v163
v_cvt_pk_bf16_f32 v46, v164, v165
v_cvt_pk_bf16_f32 v47, v166, v167
v_cvt_pk_bf16_f32 v40, v168, v169
v_cvt_pk_bf16_f32 v41, v170, v171
v_cvt_pk_bf16_f32 v42, v172, v173
v_cvt_pk_bf16_f32 v43, v174, v175
v_cvt_pk_bf16_f32 v48, v144, v145
v_cvt_pk_bf16_f32 v49, v146, v147
v_cvt_pk_bf16_f32 v50, v176, v177
v_cvt_pk_bf16_f32 v51, v178, v179
v_cvt_pk_bf16_f32 v52, v148, v149
v_cvt_pk_bf16_f32 v53, v150, v151
v_cvt_pk_bf16_f32 v54, v180, v181
v_cvt_pk_bf16_f32 v55, v182, v183
v_cvt_pk_bf16_f32 v56, v152, v153
v_cvt_pk_bf16_f32 v57, v154, v155
v_cvt_pk_bf16_f32 v58, v184, v185
v_cvt_pk_bf16_f32 v59, v186, v187
v_cvt_pk_bf16_f32 v60, v156, v157
v_cvt_pk_bf16_f32 v61, v158, v159
v_cvt_pk_bf16_f32 v62, v188, v189
v_cvt_pk_bf16_f32 v63, v190, v191
v_cvt_pk_bf16_f32 v68, v192, v193
v_cvt_pk_bf16_f32 v69, v194, v195
v_cvt_pk_bf16_f32 v70, v196, v197
v_cvt_pk_bf16_f32 v71, v198, v199
v_cvt_pk_bf16_f32 v76, v200, v201
v_cvt_pk_bf16_f32 v77, v202, v203
v_cvt_pk_bf16_f32 v78, v204, v205
v_cvt_pk_bf16_f32 v79, v206, v207
v_cvt_pk_bf16_f32 v100, v224, v225
v_cvt_pk_bf16_f32 v101, v226, v227
v_cvt_pk_bf16_f32 v102, v228, v229
v_cvt_pk_bf16_f32 v103, v230, v231
v_cvt_pk_bf16_f32 v108, v232, v233
v_cvt_pk_bf16_f32 v109, v234, v235
v_cvt_pk_bf16_f32 v110, v236, v237
v_cvt_pk_bf16_f32 v111, v238, v239
v_cvt_pk_bf16_f32 v112, v208, v209
v_cvt_pk_bf16_f32 v113, v210, v211
v_cvt_pk_bf16_f32 v114, v240, v241
v_cvt_pk_bf16_f32 v115, v242, v243
v_cvt_pk_bf16_f32 v116, v212, v213
v_cvt_pk_bf16_f32 v117, v214, v215
v_cvt_pk_bf16_f32 v118, v244, v245
v_cvt_pk_bf16_f32 v119, v246, v247
v_cvt_pk_bf16_f32 v120, v216, v217
v_cvt_pk_bf16_f32 v121, v218, v219
v_cvt_pk_bf16_f32 v122, v248, v249
v_cvt_pk_bf16_f32 v123, v250, v251
v_cvt_pk_bf16_f32 v124, v220, v221
v_cvt_pk_bf16_f32 v125, v222, v223
v_cvt_pk_bf16_f32 v126, v252, v253
v_cvt_pk_bf16_f32 v127, v254, v255
s_waitcnt vmcnt(8)
s_waitcnt lgkmcnt(14)
v_mfma_f32_16x16x32_bf16 v[128:131], a[0:3], a[160:163], 0
v_mfma_f32_16x16x32_bf16 v[160:163], a[0:3], a[164:167], 0
ds_read_b64_tr_b16 a[32:33], v8 offset:8192
v_mfma_f32_16x16x32_bf16 v[192:195], a[0:3], a[168:171], 0
ds_read_b64_tr_b16 a[34:35], v9 offset:8192
v_mfma_f32_16x16x32_bf16 v[224:227], a[0:3], a[172:175], 0
buffer_load_dwordx4 a[128:131], v4, s[52:55], s73 offen offset:2048 sc1
s_waitcnt lgkmcnt(14)
v_mfma_f32_16x16x32_bf16 v[132:135], a[4:7], a[160:163], 0
ds_read_b64_tr_b16 a[36:37], v10 offset:8192
v_mfma_f32_16x16x32_bf16 v[164:167], a[4:7], a[164:167], 0
ds_read_b64_tr_b16 a[38:39], v11 offset:8192
buffer_load_dwordx4 a[132:135], v5, s[52:55], s73 offen offset:2048 sc1
v_mfma_f32_16x16x32_bf16 v[196:199], a[4:7], a[168:171], 0
v_mfma_f32_16x16x32_bf16 v[228:231], a[4:7], a[172:175], 0
ds_read_b64_tr_b16 a[40:41], v8 offset:8704
buffer_load_dwordx4 a[136:139], v6, s[52:55], s73 offen offset:2048 sc1
s_waitcnt lgkmcnt(15)
v_mfma_f32_16x16x32_bf16 v[136:139], a[8:11], a[160:163], 0
v_mfma_f32_16x16x32_bf16 v[168:171], a[8:11], a[164:167], 0
ds_read_b64_tr_b16 a[42:43], v9 offset:8704
buffer_load_dwordx4 a[140:143], v7, s[52:55], s73 offen offset:2048 sc1
v_mfma_f32_16x16x32_bf16 v[200:203], a[8:11], a[168:171], 0
ds_read_b64_tr_b16 a[44:45], v10 offset:8704
v_mfma_f32_16x16x32_bf16 v[232:235], a[8:11], a[172:175], 0
buffer_load_dwordx4 a[144:147], v4, s[52:55], s73 offen offset:3072 sc1
s_waitcnt lgkmcnt(15)
v_mfma_f32_16x16x32_bf16 v[140:143], a[12:15], a[160:163], 0
ds_read_b64_tr_b16 a[46:47], v11 offset:8704
v_mfma_f32_16x16x32_bf16 v[172:175], a[12:15], a[164:167], 0
buffer_load_dwordx4 a[148:151], v5, s[52:55], s73 offen offset:3072 sc1
s_lshl_b32 s76, s39, 17
v_mfma_f32_16x16x32_bf16 v[204:207], a[12:15], a[168:171], 0
ds_read_b64_tr_b16 a[48:49], v8 offset:9216
s_add_u32 s76, s76, 2097152
v_mfma_f32_16x16x32_bf16 v[236:239], a[12:15], a[172:175], 0
ds_read_b64_tr_b16 a[50:51], v9 offset:9216
buffer_load_dwordx4 a[152:155], v6, s[52:55], s73 offen offset:3072 sc1
s_lshl_b32 s70, s60, 8
s_add_u32 s78, s69, 57344
s_waitcnt lgkmcnt(15)
v_mfma_f32_16x16x32_bf16 v[144:147], a[16:19], a[160:163], 0
s_add_u32 s76, s76, s70
buffer_store_dwordx4 v[112:115], v1, s[56:59], s78 offen offset:0 sc1
v_mfma_f32_16x16x32_bf16 v[176:179], a[16:19], a[164:167], 0
ds_read_b64_tr_b16 a[52:53], v10 offset:9216
buffer_load_dwordx4 a[156:159], v7, s[52:55], s73 offen offset:3072 sc1
s_lshl_b32 s70, s39, 13
v_mfma_f32_16x16x32_bf16 v[208:211], a[16:19], a[168:171], 0
ds_read_b64_tr_b16 a[54:55], v11 offset:9216
s_add_u32 m0, s70, 65536
v_mfma_f32_16x16x32_bf16 v[240:243], a[16:19], a[172:175], 0
buffer_load_dwordx4 v12, s[48:51], s76 offen offset:0 lds
s_waitcnt lgkmcnt(15)
v_mfma_f32_16x16x32_bf16 v[148:151], a[20:23], a[160:163], 0
ds_read_b64_tr_b16 a[56:57], v8 offset:9728
s_add_u32 s78, s69, 57344
v_mfma_f32_16x16x32_bf16 v[180:183], a[20:23], a[164:167], 0
buffer_store_dwordx4 v[116:119], v1, s[56:59], s78 offen offset:1024 sc1
v_mfma_f32_16x16x32_bf16 v[212:215], a[20:23], a[168:171], 0
ds_read_b64_tr_b16 a[58:59], v9 offset:9728
buffer_load_dwordx4 v13, s[48:51], s76 offen offset:1024 lds
v_mfma_f32_16x16x32_bf16 v[244:247], a[20:23], a[172:175], 0
ds_read_b64_tr_b16 a[60:61], v10 offset:9728
s_waitcnt lgkmcnt(15)
v_mfma_f32_16x16x32_bf16 v[152:155], a[24:27], a[160:163], 0
v_mfma_f32_16x16x32_bf16 v[184:187], a[24:27], a[164:167], 0
ds_read_b64_tr_b16 a[62:63], v11 offset:9728
buffer_load_dwordx4 v14, s[48:51], s76 offen offset:2048 lds
s_add_u32 s78, s69, 57344
v_mfma_f32_16x16x32_bf16 v[216:219], a[24:27], a[168:171], 0
buffer_store_dwordx4 v[120:123], v1, s[56:59], s78 offen offset:2048 sc1
v_mfma_f32_16x16x32_bf16 v[248:251], a[24:27], a[172:175], 0
v_mfma_f32_16x16x32_bf16 v[156:159], a[28:31], a[160:163], 0
buffer_load_dwordx4 v15, s[48:51], s76 offen offset:3072 lds
v_mfma_f32_16x16x32_bf16 v[188:191], a[28:31], a[164:167], 0
v_mfma_f32_16x16x32_bf16 v[220:223], a[28:31], a[168:171], 0
v_mfma_f32_16x16x32_bf16 v[252:255], a[28:31], a[172:175], 0
s_waitcnt lgkmcnt(14)
v_mfma_f32_16x16x32_bf16 v[128:131], a[32:35], a[176:179], v[128:131]
s_lshl_b32 s77, s39, 17
v_mfma_f32_16x16x32_bf16 v[160:163], a[32:35], a[180:183], v[160:163]
ds_read_b64_tr_b16 a[0:1], v8 offset:16384
s_add_u32 s77, s77, 2228224
v_mfma_f32_16x16x32_bf16 v[192:195], a[32:35], a[184:187], v[192:195]
ds_read_b64_tr_b16 a[2:3], v9 offset:16384
s_lshl_b32 s70, s60, 8
v_mfma_f32_16x16x32_bf16 v[224:227], a[32:35], a[188:191], v[224:227]
s_add_u32 s77, s77, s70
s_waitcnt lgkmcnt(14)
v_mfma_f32_16x16x32_bf16 v[132:135], a[36:39], a[176:179], v[132:135]
ds_read_b64_tr_b16 a[4:5], v10 offset:16384
s_lshl_b32 s70, s39, 13
v_mfma_f32_16x16x32_bf16 v[164:167], a[36:39], a[180:183], v[164:167]
ds_read_b64_tr_b16 a[6:7], v11 offset:16384
s_add_u32 m0, s70, 69632
v_mfma_f32_16x16x32_bf16 v[196:199], a[36:39], a[184:187], v[196:199]
)ASM"
R"ASM(buffer_load_dwordx4 v12, s[48:51], s77 offen offset:0 lds
v_mfma_f32_16x16x32_bf16 v[228:231], a[36:39], a[188:191], v[228:231]
ds_read_b64_tr_b16 a[8:9], v8 offset:16896
s_waitcnt lgkmcnt(15)
v_mfma_f32_16x16x32_bf16 v[136:139], a[40:43], a[176:179], v[136:139]
v_mfma_f32_16x16x32_bf16 v[168:171], a[40:43], a[180:183], v[168:171]
ds_read_b64_tr_b16 a[10:11], v9 offset:16896
buffer_load_dwordx4 v13, s[48:51], s77 offen offset:1024 lds
v_mfma_f32_16x16x32_bf16 v[200:203], a[40:43], a[184:187], v[200:203]
ds_read_b64_tr_b16 a[12:13], v10 offset:16896
s_add_u32 s78, s69, 57344
v_mfma_f32_16x16x32_bf16 v[232:235], a[40:43], a[188:191], v[232:235]
buffer_store_dwordx4 v[124:127], v1, s[56:59], s78 offen offset:3072 sc1
s_waitcnt lgkmcnt(15)
v_mfma_f32_16x16x32_bf16 v[140:143], a[44:47], a[176:179], v[140:143]
ds_read_b64_tr_b16 a[14:15], v11 offset:16896
buffer_load_dwordx4 v14, s[48:51], s77 offen offset:2048 lds
v_mfma_f32_16x16x32_bf16 v[172:175], a[44:47], a[180:183], v[172:175]
s_lshl_b32 s76, s39, 17
v_mfma_f32_16x16x32_bf16 v[204:207], a[44:47], a[184:187], v[204:207]
ds_read_b64_tr_b16 a[16:17], v8 offset:17408
s_add_u32 s76, s76, 3145728
v_mfma_f32_16x16x32_bf16 v[236:239], a[44:47], a[188:191], v[236:239]
ds_read_b64_tr_b16 a[18:19], v9 offset:17408
buffer_load_dwordx4 v15, s[48:51], s77 offen offset:3072 lds
s_lshl_b32 s70, s60, 8
s_add_u32 s78, s68, 12288
s_waitcnt lgkmcnt(15)
v_mfma_f32_16x16x32_bf16 v[144:147], a[48:51], a[176:179], v[144:147]
s_add_u32 s76, s76, s70
buffer_store_dwordx4 v[96:99], v1, s[56:59], s78 offen offset:0 sc1
v_mfma_f32_16x16x32_bf16 v[176:179], a[48:51], a[180:183], v[176:179]
ds_read_b64_tr_b16 a[20:21], v10 offset:17408
s_lshl_b32 s70, s39, 13
buffer_load_dwordx4 a[160:163], v4, s[52:55], s74 offen offset:0 sc1
v_mfma_f32_16x16x32_bf16 v[208:211], a[48:51], a[184:187], v[208:211]
ds_read_b64_tr_b16 a[22:23], v11 offset:17408
s_add_u32 m0, s70, 98304
buffer_load_dwordx4 a[164:167], v5, s[52:55], s74 offen offset:0 sc1
v_mfma_f32_16x16x32_bf16 v[240:243], a[48:51], a[188:191], v[240:243]
buffer_load_dwordx4 v12, s[48:51], s76 offen offset:0 lds
s_waitcnt lgkmcnt(15)
v_mfma_f32_16x16x32_bf16 v[148:151], a[52:55], a[176:179], v[148:151]
ds_read_b64_tr_b16 a[24:25], v8 offset:17920
s_add_u32 s78, s68, 12288
v_mfma_f32_16x16x32_bf16 v[180:183], a[52:55], a[180:183], v[180:183]
buffer_store_dwordx4 v[104:107], v1, s[56:59], s78 offen offset:1024 sc1
v_mfma_f32_16x16x32_bf16 v[212:215], a[52:55], a[184:187], v[212:215]
ds_read_b64_tr_b16 a[26:27], v9 offset:17920
buffer_load_dwordx4 v13, s[48:51], s76 offen offset:1024 lds
v_mfma_f32_16x16x32_bf16 v[244:247], a[52:55], a[188:191], v[244:247]
ds_read_b64_tr_b16 a[28:29], v10 offset:17920
s_waitcnt lgkmcnt(15)
v_mfma_f32_16x16x32_bf16 v[152:155], a[56:59], a[176:179], v[152:155]
v_mfma_f32_16x16x32_bf16 v[184:187], a[56:59], a[180:183], v[184:187]
ds_read_b64_tr_b16 a[30:31], v11 offset:17920
buffer_load_dwordx4 v14, s[48:51], s76 offen offset:2048 lds
v_mfma_f32_16x16x32_bf16 v[216:219], a[56:59], a[184:187], v[216:219]
v_mfma_f32_16x16x32_bf16 v[248:251], a[56:59], a[188:191], v[248:251]
v_mfma_f32_16x16x32_bf16 v[156:159], a[60:63], a[176:179], v[156:159]
buffer_load_dwordx4 v15, s[48:51], s76 offen offset:3072 lds
v_mfma_f32_16x16x32_bf16 v[188:191], a[60:63], a[180:183], v[188:191]
v_mfma_f32_16x16x32_bf16 v[220:223], a[60:63], a[184:187], v[220:223]
buffer_load_dwordx4 a[168:171], v6, s[52:55], s74 offen offset:0 sc1
v_mfma_f32_16x16x32_bf16 v[252:255], a[60:63], a[188:191], v[252:255]
buffer_load_dwordx4 a[172:175], v7, s[52:55], s74 offen offset:0 sc1
s_waitcnt lgkmcnt(14)
v_mfma_f32_16x16x32_bf16 v[128:131], a[0:3], a[192:195], v[128:131]
s_lshl_b32 s77, s39, 17
v_mfma_f32_16x16x32_bf16 v[160:163], a[0:3], a[196:199], v[160:163]
ds_read_b64_tr_b16 a[32:33], v8 offset:24576
s_add_u32 s77, s77, 3276800
v_mfma_f32_16x16x32_bf16 v[192:195], a[0:3], a[200:203], v[192:195]
ds_read_b64_tr_b16 a[34:35], v9 offset:24576
s_lshl_b32 s70, s60, 8
v_mfma_f32_16x16x32_bf16 v[224:227], a[0:3], a[204:207], v[224:227]
s_add_u32 s77, s77, s70
s_waitcnt lgkmcnt(14)
v_mfma_f32_16x16x32_bf16 v[132:135], a[4:7], a[192:195], v[132:135]
ds_read_b64_tr_b16 a[36:37], v10 offset:24576
s_lshl_b32 s70, s39, 13
v_mfma_f32_16x16x32_bf16 v[164:167], a[4:7], a[196:199], v[164:167]
ds_read_b64_tr_b16 a[38:39], v11 offset:24576
s_add_u32 m0, s70, 102400
v_mfma_f32_16x16x32_bf16 v[196:199], a[4:7], a[200:203], v[196:199]
buffer_load_dwordx4 v12, s[48:51], s77 offen offset:0 lds
v_mfma_f32_16x16x32_bf16 v[228:231], a[4:7], a[204:207], v[228:231]
ds_read_b64_tr_b16 a[40:41], v8 offset:25088
s_waitcnt lgkmcnt(15)
v_mfma_f32_16x16x32_bf16 v[136:139], a[8:11], a[192:195], v[136:139]
buffer_load_dwordx4 a[176:179], v4, s[52:55], s74 offen offset:1024 sc1
v_mfma_f32_16x16x32_bf16 v[168:171], a[8:11], a[196:199], v[168:171]
ds_read_b64_tr_b16 a[42:43], v9 offset:25088
v_mfma_f32_16x16x32_bf16 v[200:203], a[8:11], a[200:203], v[200:203]
ds_read_b64_tr_b16 a[44:45], v10 offset:25088
buffer_load_dwordx4 v13, s[48:51], s77 offen offset:1024 lds
s_add_u32 s78, s68, 45056
v_mfma_f32_16x16x32_bf16 v[232:235], a[8:11], a[204:207], v[232:235]
buffer_store_dwordx4 v[100:103], v1, s[56:59], s78 offen offset:0 sc1
s_waitcnt lgkmcnt(15)
v_mfma_f32_16x16x32_bf16 v[140:143], a[12:15], a[192:195], v[140:143]
ds_read_b64_tr_b16 a[46:47], v11 offset:25088
v_mfma_f32_16x16x32_bf16 v[172:175], a[12:15], a[196:199], v[172:175]
v_mfma_f32_16x16x32_bf16 v[204:207], a[12:15], a[200:203], v[204:207]
ds_read_b64_tr_b16 a[48:49], v8 offset:25600
buffer_load_dwordx4 v14, s[48:51], s77 offen offset:2048 lds
v_mfma_f32_16x16x32_bf16 v[236:239], a[12:15], a[204:207], v[236:239]
ds_read_b64_tr_b16 a[50:51], v9 offset:25600
s_waitcnt lgkmcnt(15)
v_mfma_f32_16x16x32_bf16 v[144:147], a[16:19], a[192:195], v[144:147]
buffer_load_dwordx4 a[180:183], v5, s[52:55], s74 offen offset:1024 sc1
v_mfma_f32_16x16x32_bf16 v[176:179], a[16:19], a[196:199], v[176:179]
ds_read_b64_tr_b16 a[52:53], v10 offset:25600
v_mfma_f32_16x16x32_bf16 v[208:211], a[16:19], a[200:203], v[208:211]
ds_read_b64_tr_b16 a[54:55], v11 offset:25600
buffer_load_dwordx4 v15, s[48:51], s77 offen offset:3072 lds
v_mfma_f32_16x16x32_bf16 v[240:243], a[16:19], a[204:207], v[240:243]
s_waitcnt lgkmcnt(15)
v_mfma_f32_16x16x32_bf16 v[148:151], a[20:23], a[192:195], v[148:151]
ds_read_b64_tr_b16 a[56:57], v8 offset:26112
s_add_u32 s78, s68, 45056
v_mfma_f32_16x16x32_bf16 v[180:183], a[20:23], a[196:199], v[180:183]
buffer_store_dwordx4 v[108:111], v1, s[56:59], s78 offen offset:1024 sc1
v_mfma_f32_16x16x32_bf16 v[212:215], a[20:23], a[200:203], v[212:215]
ds_read_b64_tr_b16 a[58:59], v9 offset:26112
v_mfma_f32_16x16x32_bf16 v[244:247], a[20:23], a[204:207], v[244:247]
ds_read_b64_tr_b16 a[60:61], v10 offset:26112
s_waitcnt lgkmcnt(15)
v_mfma_f32_16x16x32_bf16 v[152:155], a[24:27], a[192:195], v[152:155]
buffer_load_dwordx4 a[184:187], v6, s[52:55], s74 offen offset:1024 sc1
v_mfma_f32_16x16x32_bf16 v[184:187], a[24:27], a[196:199], v[184:187]
ds_read_b64_tr_b16 a[62:63], v11 offset:26112
v_mfma_f32_16x16x32_bf16 v[216:219], a[24:27], a[200:203], v[216:219]
v_mfma_f32_16x16x32_bf16 v[248:251], a[24:27], a[204:207], v[248:251]
v_mfma_f32_16x16x32_bf16 v[156:159], a[28:31], a[192:195], v[156:159]
buffer_load_dwordx4 a[188:191], v7, s[52:55], s74 offen offset:1024 sc1
v_mfma_f32_16x16x32_bf16 v[188:191], a[28:31], a[196:199], v[188:191]
v_mfma_f32_16x16x32_bf16 v[220:223], a[28:31], a[200:203], v[220:223]
v_mfma_f32_16x16x32_bf16 v[252:255], a[28:31], a[204:207], v[252:255]
s_waitcnt lgkmcnt(14)
v_mfma_f32_16x16x32_bf16 v[128:131], a[32:35], a[208:211], v[128:131]
v_mfma_f32_16x16x32_bf16 v[160:163], a[32:35], a[212:215], v[160:163]
ds_read_b64_tr_b16 a[0:1], v8 offset:32768
v_mfma_f32_16x16x32_bf16 v[192:195], a[32:35], a[216:219], v[192:195]
ds_read_b64_tr_b16 a[2:3], v9 offset:32768
v_mfma_f32_16x16x32_bf16 v[224:227], a[32:35], a[220:223], v[224:227]
s_waitcnt lgkmcnt(14)
v_mfma_f32_16x16x32_bf16 v[132:135], a[36:39], a[208:211], v[132:135]
ds_read_b64_tr_b16 a[4:5], v10 offset:32768
buffer_load_dwordx4 a[192:195], v4, s[52:55], s74 offen offset:2048 sc1
v_mfma_f32_16x16x32_bf16 v[164:167], a[36:39], a[212:215], v[164:167]
ds_read_b64_tr_b16 a[6:7], v11 offset:32768
s_lshl_b32 s62, s60, 20
v_mfma_f32_16x16x32_bf16 v[196:199], a[36:39], a[216:219], v[196:199]
s_add_u32 s44, s30, s62
v_mfma_f32_16x16x32_bf16 v[228:231], a[36:39], a[220:223], v[228:231]
ds_read_b64_tr_b16 a[8:9], v8 offset:33280
s_addc_u32 s45, s31, 0
s_waitcnt lgkmcnt(15)
v_mfma_f32_16x16x32_bf16 v[136:139], a[40:43], a[208:211], v[136:139]
s_and_b32 s45, s45, 0xffff
v_mfma_f32_16x16x32_bf16 v[168:171], a[40:43], a[212:215], v[168:171]
ds_read_b64_tr_b16 a[10:11], v9 offset:33280
s_lshl_b32 s62, s61, 1
v_mfma_f32_16x16x32_bf16 v[200:203], a[40:43], a[216:219], v[200:203]
ds_read_b64_tr_b16 a[12:13], v10 offset:33280
s_add_u32 s70, s62, 2
v_mfma_f32_16x16x32_bf16 v[232:235], a[40:43], a[220:223], v[232:235]
buffer_load_dwordx4 a[196:199], v5, s[52:55], s74 offen offset:2048 sc1
s_sub_u32 s71, 32, s62
s_waitcnt lgkmcnt(15)
v_mfma_f32_16x16x32_bf16 v[140:143], a[44:47], a[208:211], v[140:143]
ds_read_b64_tr_b16 a[14:15], v11 offset:33280
s_add_u32 s78, s68, 8192
s_lshl_b32 s46, s71, 15
v_mfma_f32_16x16x32_bf16 v[172:175], a[44:47], a[212:215], v[172:175]
buffer_store_dwordx4 v[64:67], v1, s[56:59], s78 offen offset:0 sc1
s_mov_b32 s47, 0x20000
v_mfma_f32_16x16x32_bf16 v[204:207], a[44:47], a[216:219], v[204:207]
ds_read_b64_tr_b16 a[16:17], v8 offset:33792
v_mfma_f32_16x16x32_bf16 v[236:239], a[44:47], a[220:223], v[236:239]
ds_read_b64_tr_b16 a[18:19], v9 offset:33792
s_waitcnt lgkmcnt(15)
v_mfma_f32_16x16x32_bf16 v[144:147], a[48:51], a[208:211], v[144:147]
v_mfma_f32_16x16x32_bf16 v[176:179], a[48:51], a[212:215], v[176:179]
ds_read_b64_tr_b16 a[20:21], v10 offset:33792
v_mfma_f32_16x16x32_bf16 v[208:211], a[48:51], a[216:219], v[208:211]
ds_read_b64_tr_b16 a[22:23], v11 offset:33792
buffer_load_dwordx4 a[200:203], v6, s[52:55], s74 offen offset:2048 sc1
v_mfma_f32_16x16x32_bf16 v[240:243], a[48:51], a[220:223], v[240:243]
s_waitcnt lgkmcnt(15)
v_mfma_f32_16x16x32_bf16 v[148:151], a[52:55], a[208:211], v[148:151]
ds_read_b64_tr_b16 a[24:25], v8 offset:34304
v_mfma_f32_16x16x32_bf16 v[180:183], a[52:55], a[212:215], v[180:183]
v_mfma_f32_16x16x32_bf16 v[212:215], a[52:55], a[216:219], v[212:215]
ds_read_b64_tr_b16 a[26:27], v9 offset:34304
v_mfma_f32_16x16x32_bf16 v[244:247], a[52:55], a[220:223], v[244:247]
ds_read_b64_tr_b16 a[28:29], v10 offset:34304
s_add_u32 s78, s68, 8192
s_waitcnt lgkmcnt(15)
v_mfma_f32_16x16x32_bf16 v[152:155], a[56:59], a[208:211], v[152:155]
buffer_store_dwordx4 v[72:75], v1, s[56:59], s78 offen offset:1024 sc1
v_mfma_f32_16x16x32_bf16 v[184:187], a[56:59], a[212:215], v[184:187]
ds_read_b64_tr_b16 a[30:31], v11 offset:34304
buffer_load_dwordx4 a[204:207], v7, s[52:55], s74 offen offset:2048 sc1
v_mfma_f32_16x16x32_bf16 v[216:219], a[56:59], a[216:219], v[216:219]
v_mfma_f32_16x16x32_bf16 v[248:251], a[56:59], a[220:223], v[248:251]
v_mfma_f32_16x16x32_bf16 v[156:159], a[60:63], a[208:211], v[156:159]
v_mfma_f32_16x16x32_bf16 v[188:191], a[60:63], a[212:215], v[188:191]
v_mfma_f32_16x16x32_bf16 v[220:223], a[60:63], a[216:219], v[220:223]
v_mfma_f32_16x16x32_bf16 v[252:255], a[60:63], a[220:223], v[252:255]
s_waitcnt lgkmcnt(14)
v_mfma_f32_16x16x32_bf16 v[128:131], a[0:3], a[224:227], v[128:131]
s_and_b32 s62, s60, 3
s_lshl_b32 s62, s62, 6
v_mfma_f32_16x16x32_bf16 v[160:163], a[0:3], a[228:231], v[160:163]
ds_read_b64_tr_b16 a[32:33], v8 offset:40960
s_lshr_b32 s63, s60, 2
s_add_u32 s62, s62, s63
v_mfma_f32_16x16x32_bf16 v[192:195], a[0:3], a[232:235], v[192:195]
ds_read_b64_tr_b16 a[34:35], v9 offset:40960
v_mfma_f32_16x16x32_bf16 v[224:227], a[0:3], a[236:239], v[224:227]
s_waitcnt lgkmcnt(14)
v_mfma_f32_16x16x32_bf16 v[132:135], a[4:7], a[224:227], v[132:135]
ds_read_b64_tr_b16 a[36:37], v10 offset:40960
buffer_load_dwordx4 a[208:211], v4, s[52:55], s74 offen offset:3072 sc1
v_mfma_f32_16x16x32_bf16 v[164:167], a[4:7], a[228:231], v[164:167]
ds_read_b64_tr_b16 a[38:39], v11 offset:40960
v_and_b32 v18, 7, v2
v_mfma_f32_16x16x32_bf16 v[196:199], a[4:7], a[232:235], v[196:199]
v_mfma_f32_16x16x32_bf16 v[228:231], a[4:7], a[236:239], v[228:231]
ds_read_b64_tr_b16 a[40:41], v8 offset:41472
v_lshl_add_u32 v18, v18, 3, s62
s_waitcnt lgkmcnt(15)
v_mfma_f32_16x16x32_bf16 v[136:139], a[8:11], a[224:227], v[136:139]
v_mfma_f32_16x16x32_bf16 v[168:171], a[8:11], a[228:231], v[168:171]
ds_read_b64_tr_b16 a[42:43], v9 offset:41472
v_lshlrev_b32 v18, 7, v18
v_mfma_f32_16x16x32_bf16 v[200:203], a[8:11], a[232:235], v[200:203]
ds_read_b64_tr_b16 a[44:45], v10 offset:41472
v_mfma_f32_16x16x32_bf16 v[232:235], a[8:11], a[236:239], v[232:235]
buffer_load_dwordx4 a[212:215], v5, s[52:55], s74 offen offset:3072 sc1
s_waitcnt lgkmcnt(15)
v_mfma_f32_16x16x32_bf16 v[140:143], a[12:15], a[224:227], v[140:143]
ds_read_b64_tr_b16 a[46:47], v11 offset:41472
s_add_u32 s78, s68, 40960
v_mfma_f32_16x16x32_bf16 v[172:175], a[12:15], a[228:231], v[172:175]
buffer_store_dwordx4 v[68:71], v1, s[56:59], s78 offen offset:0 sc1
v_mfma_f32_16x16x32_bf16 v[204:207], a[12:15], a[232:235], v[204:207]
ds_read_b64_tr_b16 a[48:49], v8 offset:41984
v_mfma_f32_16x16x32_bf16 v[236:239], a[12:15], a[236:239], v[236:239]
ds_read_b64_tr_b16 a[50:51], v9 offset:41984
s_waitcnt lgkmcnt(15)
v_mfma_f32_16x16x32_bf16 v[144:147], a[16:19], a[224:227], v[144:147]
v_mfma_f32_16x16x32_bf16 v[176:179], a[16:19], a[228:231], v[176:179]
ds_read_b64_tr_b16 a[52:53], v10 offset:41984
v_mfma_f32_16x16x32_bf16 v[208:211], a[16:19], a[232:235], v[208:211]
ds_read_b64_tr_b16 a[54:55], v11 offset:41984
buffer_load_dwordx4 a[216:219], v6, s[52:55], s74 offen offset:3072 sc1
v_mfma_f32_16x16x32_bf16 v[240:243], a[16:19], a[236:239], v[240:243]
s_waitcnt lgkmcnt(15)
v_mfma_f32_16x16x32_bf16 v[148:151], a[20:23], a[224:227], v[148:151]
ds_read_b64_tr_b16 a[56:57], v8 offset:42496
v_mfma_f32_16x16x32_bf16 v[180:183], a[20:23], a[228:231], v[180:183]
v_mfma_f32_16x16x32_bf16 v[212:215], a[20:23], a[232:235], v[212:215]
ds_read_b64_tr_b16 a[58:59], v9 offset:42496
v_mfma_f32_16x16x32_bf16 v[244:247], a[20:23], a[236:239], v[244:247]
ds_read_b64_tr_b16 a[60:61], v10 offset:42496
s_add_u32 s78, s68, 40960
s_waitcnt lgkmcnt(15)
v_mfma_f32_16x16x32_bf16 v[152:155], a[24:27], a[224:227], v[152:155]
buffer_store_dwordx4 v[76:79], v1, s[56:59], s78 offen offset:1024 sc1
v_mfma_f32_16x16x32_bf16 v[184:187], a[24:27], a[228:231], v[184:187]
ds_read_b64_tr_b16 a[62:63], v11 offset:42496
buffer_load_dwordx4 a[220:223], v7, s[52:55], s74 offen offset:3072 sc1
v_mfma_f32_16x16x32_bf16 v[216:219], a[24:27], a[232:235], v[216:219]
v_mfma_f32_16x16x32_bf16 v[248:251], a[24:27], a[236:239], v[248:251]
v_mfma_f32_16x16x32_bf16 v[156:159], a[28:31], a[224:227], v[156:159]
v_mfma_f32_16x16x32_bf16 v[188:191], a[28:31], a[228:231], v[188:191]
v_mfma_f32_16x16x32_bf16 v[220:223], a[28:31], a[232:235], v[220:223]
v_mfma_f32_16x16x32_bf16 v[252:255], a[28:31], a[236:239], v[252:255]
s_waitcnt lgkmcnt(14)
v_mfma_f32_16x16x32_bf16 v[128:131], a[32:35], a[240:243], v[128:131]
v_mfma_f32_16x16x32_bf16 v[160:163], a[32:35], a[244:247], v[160:163]
ds_read_b64_tr_b16 a[0:1], v8 offset:49152
v_mfma_f32_16x16x32_bf16 v[192:195], a[32:35], a[248:251], v[192:195]
ds_read_b64_tr_b16 a[2:3], v9 offset:49152
v_mfma_f32_16x16x32_bf16 v[224:227], a[32:35], a[252:255], v[224:227]
s_waitcnt lgkmcnt(14)
v_mfma_f32_16x16x32_bf16 v[132:135], a[36:39], a[240:243], v[132:135]
ds_read_b64_tr_b16 a[4:5], v10 offset:49152
buffer_load_dwordx4 a[224:227], v4, s[52:55], s75 offen offset:0 sc1
v_mfma_f32_16x16x32_bf16 v[164:167], a[36:39], a[244:247], v[164:167]
ds_read_b64_tr_b16 a[6:7], v11 offset:49152
v_mfma_f32_16x16x32_bf16 v[196:199], a[36:39], a[248:251], v[196:199]
v_mfma_f32_16x16x32_bf16 v[228:231], a[36:39], a[252:255], v[228:231]
ds_read_b64_tr_b16 a[8:9], v8 offset:49664
s_waitcnt lgkmcnt(15)
v_mfma_f32_16x16x32_bf16 v[136:139], a[40:43], a[240:243], v[136:139]
v_mfma_f32_16x16x32_bf16 v[168:171], a[40:43], a[244:247], v[168:171]
)ASM"
R"ASM(ds_read_b64_tr_b16 a[10:11], v9 offset:49664
v_mfma_f32_16x16x32_bf16 v[200:203], a[40:43], a[248:251], v[200:203]
ds_read_b64_tr_b16 a[12:13], v10 offset:49664
v_mfma_f32_16x16x32_bf16 v[232:235], a[40:43], a[252:255], v[232:235]
buffer_load_dwordx4 a[228:231], v5, s[52:55], s75 offen offset:0 sc1
s_waitcnt lgkmcnt(15)
v_mfma_f32_16x16x32_bf16 v[140:143], a[44:47], a[240:243], v[140:143]
ds_read_b64_tr_b16 a[14:15], v11 offset:49664
s_add_u32 s78, s69, 49152
v_mfma_f32_16x16x32_bf16 v[172:175], a[44:47], a[244:247], v[172:175]
s_waitcnt vmcnt(48)
buffer_store_dwordx4 v[48:51], v1, s[56:59], s78 offen offset:0 sc1
v_mfma_f32_16x16x32_bf16 v[204:207], a[44:47], a[248:251], v[204:207]
ds_read_b64_tr_b16 a[16:17], v8 offset:50176
v_mfma_f32_16x16x32_bf16 v[236:239], a[44:47], a[252:255], v[236:239]
ds_read_b64_tr_b16 a[18:19], v9 offset:50176
s_waitcnt lgkmcnt(15)
v_mfma_f32_16x16x32_bf16 v[144:147], a[48:51], a[240:243], v[144:147]
v_mfma_f32_16x16x32_bf16 v[176:179], a[48:51], a[244:247], v[176:179]
ds_read_b64_tr_b16 a[20:21], v10 offset:50176
v_mfma_f32_16x16x32_bf16 v[208:211], a[48:51], a[248:251], v[208:211]
ds_read_b64_tr_b16 a[22:23], v11 offset:50176
buffer_load_dwordx4 a[232:235], v6, s[52:55], s75 offen offset:0 sc1
v_mfma_f32_16x16x32_bf16 v[240:243], a[48:51], a[252:255], v[240:243]
s_waitcnt lgkmcnt(15)
v_mfma_f32_16x16x32_bf16 v[148:151], a[52:55], a[240:243], v[148:151]
ds_read_b64_tr_b16 a[24:25], v8 offset:50688
v_mfma_f32_16x16x32_bf16 v[180:183], a[52:55], a[244:247], v[180:183]
v_mfma_f32_16x16x32_bf16 v[212:215], a[52:55], a[248:251], v[212:215]
ds_read_b64_tr_b16 a[26:27], v9 offset:50688
v_mfma_f32_16x16x32_bf16 v[244:247], a[52:55], a[252:255], v[244:247]
ds_read_b64_tr_b16 a[28:29], v10 offset:50688
s_add_u32 s78, s69, 49152
s_waitcnt lgkmcnt(15)
v_mfma_f32_16x16x32_bf16 v[152:155], a[56:59], a[240:243], v[152:155]
buffer_store_dwordx4 v[52:55], v1, s[56:59], s78 offen offset:1024 sc1
v_mfma_f32_16x16x32_bf16 v[184:187], a[56:59], a[244:247], v[184:187]
ds_read_b64_tr_b16 a[30:31], v11 offset:50688
buffer_load_dwordx4 a[236:239], v7, s[52:55], s75 offen offset:0 sc1
v_mfma_f32_16x16x32_bf16 v[216:219], a[56:59], a[248:251], v[216:219]
v_mfma_f32_16x16x32_bf16 v[248:251], a[56:59], a[252:255], v[248:251]
v_mfma_f32_16x16x32_bf16 v[156:159], a[60:63], a[240:243], v[156:159]
v_mfma_f32_16x16x32_bf16 v[188:191], a[60:63], a[244:247], v[188:191]
v_mfma_f32_16x16x32_bf16 v[220:223], a[60:63], a[248:251], v[220:223]
v_mfma_f32_16x16x32_bf16 v[252:255], a[60:63], a[252:255], v[252:255]
s_waitcnt lgkmcnt(14)
v_mfma_f32_16x16x32_bf16 v[128:131], a[0:3], a[128:131], v[128:131]
v_mfma_f32_16x16x32_bf16 v[160:163], a[0:3], a[132:135], v[160:163]
ds_read_b64_tr_b16 a[32:33], v8 offset:57344
v_mfma_f32_16x16x32_bf16 v[192:195], a[0:3], a[136:139], v[192:195]
ds_read_b64_tr_b16 a[34:35], v9 offset:57344
v_mfma_f32_16x16x32_bf16 v[224:227], a[0:3], a[140:143], v[224:227]
s_waitcnt lgkmcnt(14)
v_mfma_f32_16x16x32_bf16 v[132:135], a[4:7], a[128:131], v[132:135]
ds_read_b64_tr_b16 a[36:37], v10 offset:57344
buffer_load_dwordx4 a[240:243], v4, s[52:55], s75 offen offset:1024 sc1
v_mfma_f32_16x16x32_bf16 v[164:167], a[4:7], a[132:135], v[164:167]
ds_read_b64_tr_b16 a[38:39], v11 offset:57344
v_mfma_f32_16x16x32_bf16 v[196:199], a[4:7], a[136:139], v[196:199]
v_mfma_f32_16x16x32_bf16 v[228:231], a[4:7], a[140:143], v[228:231]
ds_read_b64_tr_b16 a[40:41], v8 offset:57856
s_waitcnt lgkmcnt(15)
v_mfma_f32_16x16x32_bf16 v[136:139], a[8:11], a[128:131], v[136:139]
v_mfma_f32_16x16x32_bf16 v[168:171], a[8:11], a[132:135], v[168:171]
ds_read_b64_tr_b16 a[42:43], v9 offset:57856
v_mfma_f32_16x16x32_bf16 v[200:203], a[8:11], a[136:139], v[200:203]
ds_read_b64_tr_b16 a[44:45], v10 offset:57856
v_mfma_f32_16x16x32_bf16 v[232:235], a[8:11], a[140:143], v[232:235]
buffer_load_dwordx4 a[244:247], v5, s[52:55], s75 offen offset:1024 sc1
s_waitcnt lgkmcnt(15)
v_mfma_f32_16x16x32_bf16 v[140:143], a[12:15], a[128:131], v[140:143]
ds_read_b64_tr_b16 a[46:47], v11 offset:57856
s_add_u32 s78, s69, 49152
v_mfma_f32_16x16x32_bf16 v[172:175], a[12:15], a[132:135], v[172:175]
buffer_store_dwordx4 v[56:59], v1, s[56:59], s78 offen offset:2048 sc1
v_mfma_f32_16x16x32_bf16 v[204:207], a[12:15], a[136:139], v[204:207]
ds_read_b64_tr_b16 a[48:49], v8 offset:58368
v_mfma_f32_16x16x32_bf16 v[236:239], a[12:15], a[140:143], v[236:239]
ds_read_b64_tr_b16 a[50:51], v9 offset:58368
s_waitcnt lgkmcnt(15)
v_mfma_f32_16x16x32_bf16 v[144:147], a[16:19], a[128:131], v[144:147]
v_mfma_f32_16x16x32_bf16 v[176:179], a[16:19], a[132:135], v[176:179]
ds_read_b64_tr_b16 a[52:53], v10 offset:58368
v_mfma_f32_16x16x32_bf16 v[208:211], a[16:19], a[136:139], v[208:211]
ds_read_b64_tr_b16 a[54:55], v11 offset:58368
buffer_load_dwordx4 a[248:251], v6, s[52:55], s75 offen offset:1024 sc1
v_mfma_f32_16x16x32_bf16 v[240:243], a[16:19], a[140:143], v[240:243]
s_waitcnt lgkmcnt(15)
v_mfma_f32_16x16x32_bf16 v[148:151], a[20:23], a[128:131], v[148:151]
ds_read_b64_tr_b16 a[56:57], v8 offset:58880
v_mfma_f32_16x16x32_bf16 v[180:183], a[20:23], a[132:135], v[180:183]
v_mfma_f32_16x16x32_bf16 v[212:215], a[20:23], a[136:139], v[212:215]
ds_read_b64_tr_b16 a[58:59], v9 offset:58880
v_mfma_f32_16x16x32_bf16 v[244:247], a[20:23], a[140:143], v[244:247]
ds_read_b64_tr_b16 a[60:61], v10 offset:58880
s_add_u32 s78, s69, 49152
s_waitcnt lgkmcnt(15)
v_mfma_f32_16x16x32_bf16 v[152:155], a[24:27], a[128:131], v[152:155]
buffer_store_dwordx4 v[60:63], v1, s[56:59], s78 offen offset:3072 sc1
v_mfma_f32_16x16x32_bf16 v[184:187], a[24:27], a[132:135], v[184:187]
ds_read_b64_tr_b16 a[62:63], v11 offset:58880
buffer_load_dwordx4 a[252:255], v7, s[52:55], s75 offen offset:1024 sc1
v_mfma_f32_16x16x32_bf16 v[216:219], a[24:27], a[136:139], v[216:219]
v_mfma_f32_16x16x32_bf16 v[248:251], a[24:27], a[140:143], v[248:251]
v_mfma_f32_16x16x32_bf16 v[156:159], a[28:31], a[128:131], v[156:159]
v_mfma_f32_16x16x32_bf16 v[188:191], a[28:31], a[132:135], v[188:191]
v_mfma_f32_16x16x32_bf16 v[220:223], a[28:31], a[136:139], v[220:223]
v_mfma_f32_16x16x32_bf16 v[252:255], a[28:31], a[140:143], v[252:255]
s_waitcnt lgkmcnt(14)
v_mfma_f32_16x16x32_bf16 v[128:131], a[32:35], a[144:147], v[128:131]
s_waitcnt vmcnt(27)
s_barrier
v_add_u32 v8, 0x10000, v8
v_add_u32 v9, 0x10000, v9
v_add_u32 v10, 0x10000, v10
v_add_u32 v11, 0x10000, v11
v_mfma_f32_16x16x32_bf16 v[160:163], a[32:35], a[148:151], v[160:163]
ds_read_b64_tr_b16 a[0:1], v8 offset:0
v_mfma_f32_16x16x32_bf16 v[192:195], a[32:35], a[152:155], v[192:195]
ds_read_b64_tr_b16 a[2:3], v9 offset:0
v_mfma_f32_16x16x32_bf16 v[224:227], a[32:35], a[156:159], v[224:227]
s_waitcnt lgkmcnt(14)
v_mfma_f32_16x16x32_bf16 v[132:135], a[36:39], a[144:147], v[132:135]
ds_read_b64_tr_b16 a[4:5], v10 offset:0
buffer_load_dwordx4 a[128:131], v4, s[52:55], s75 offen offset:2048 sc1
v_mfma_f32_16x16x32_bf16 v[164:167], a[36:39], a[148:151], v[164:167]
ds_read_b64_tr_b16 a[6:7], v11 offset:0
v_mfma_f32_16x16x32_bf16 v[196:199], a[36:39], a[152:155], v[196:199]
v_mfma_f32_16x16x32_bf16 v[228:231], a[36:39], a[156:159], v[228:231]
ds_read_b64_tr_b16 a[8:9], v8 offset:512
s_waitcnt lgkmcnt(15)
v_mfma_f32_16x16x32_bf16 v[136:139], a[40:43], a[144:147], v[136:139]
v_mfma_f32_16x16x32_bf16 v[168:171], a[40:43], a[148:151], v[168:171]
ds_read_b64_tr_b16 a[10:11], v9 offset:512
v_mfma_f32_16x16x32_bf16 v[200:203], a[40:43], a[152:155], v[200:203]
ds_read_b64_tr_b16 a[12:13], v10 offset:512
v_mfma_f32_16x16x32_bf16 v[232:235], a[40:43], a[156:159], v[232:235]
buffer_load_dwordx4 a[132:135], v5, s[52:55], s75 offen offset:2048 sc1
s_waitcnt lgkmcnt(15)
v_mfma_f32_16x16x32_bf16 v[140:143], a[44:47], a[144:147], v[140:143]
ds_read_b64_tr_b16 a[14:15], v11 offset:512
s_add_u32 s78, s69, 24576
v_mfma_f32_16x16x32_bf16 v[172:175], a[44:47], a[148:151], v[172:175]
buffer_store_dwordx4 v[80:83], v1, s[56:59], s78 offen offset:0 sc1
v_mfma_f32_16x16x32_bf16 v[204:207], a[44:47], a[152:155], v[204:207]
ds_read_b64_tr_b16 a[16:17], v8 offset:1024
v_mfma_f32_16x16x32_bf16 v[236:239], a[44:47], a[156:159], v[236:239]
ds_read_b64_tr_b16 a[18:19], v9 offset:1024
s_waitcnt lgkmcnt(15)
v_mfma_f32_16x16x32_bf16 v[144:147], a[48:51], a[144:147], v[144:147]
v_mfma_f32_16x16x32_bf16 v[176:179], a[48:51], a[148:151], v[176:179]
ds_read_b64_tr_b16 a[20:21], v10 offset:1024
v_mfma_f32_16x16x32_bf16 v[208:211], a[48:51], a[152:155], v[208:211]
ds_read_b64_tr_b16 a[22:23], v11 offset:1024
buffer_load_dwordx4 a[136:139], v6, s[52:55], s75 offen offset:2048 sc1
v_mfma_f32_16x16x32_bf16 v[240:243], a[48:51], a[156:159], v[240:243]
s_waitcnt lgkmcnt(15)
v_mfma_f32_16x16x32_bf16 v[148:151], a[52:55], a[144:147], v[148:151]
ds_read_b64_tr_b16 a[24:25], v8 offset:1536
v_mfma_f32_16x16x32_bf16 v[180:183], a[52:55], a[148:151], v[180:183]
v_mfma_f32_16x16x32_bf16 v[212:215], a[52:55], a[152:155], v[212:215]
ds_read_b64_tr_b16 a[26:27], v9 offset:1536
v_mfma_f32_16x16x32_bf16 v[244:247], a[52:55], a[156:159], v[244:247]
ds_read_b64_tr_b16 a[28:29], v10 offset:1536
s_add_u32 s78, s69, 24576
s_waitcnt lgkmcnt(15)
v_mfma_f32_16x16x32_bf16 v[152:155], a[56:59], a[144:147], v[152:155]
buffer_store_dwordx4 v[84:87], v1, s[56:59], s78 offen offset:1024 sc1
v_mfma_f32_16x16x32_bf16 v[184:187], a[56:59], a[148:151], v[184:187]
ds_read_b64_tr_b16 a[30:31], v11 offset:1536
buffer_load_dwordx4 a[140:143], v7, s[52:55], s75 offen offset:2048 sc1
v_mfma_f32_16x16x32_bf16 v[216:219], a[56:59], a[152:155], v[216:219]
v_mfma_f32_16x16x32_bf16 v[248:251], a[56:59], a[156:159], v[248:251]
v_mfma_f32_16x16x32_bf16 v[156:159], a[60:63], a[144:147], v[156:159]
v_mfma_f32_16x16x32_bf16 v[188:191], a[60:63], a[148:151], v[188:191]
v_mfma_f32_16x16x32_bf16 v[220:223], a[60:63], a[152:155], v[220:223]
v_mfma_f32_16x16x32_bf16 v[252:255], a[60:63], a[156:159], v[252:255]
s_waitcnt lgkmcnt(14)
v_mfma_f32_16x16x32_bf16 v[128:131], a[0:3], a[160:163], v[128:131]
v_mfma_f32_16x16x32_bf16 v[160:163], a[0:3], a[164:167], v[160:163]
ds_read_b64_tr_b16 a[32:33], v8 offset:8192
v_mfma_f32_16x16x32_bf16 v[192:195], a[0:3], a[168:171], v[192:195]
ds_read_b64_tr_b16 a[34:35], v9 offset:8192
v_mfma_f32_16x16x32_bf16 v[224:227], a[0:3], a[172:175], v[224:227]
s_waitcnt lgkmcnt(14)
v_mfma_f32_16x16x32_bf16 v[132:135], a[4:7], a[160:163], v[132:135]
ds_read_b64_tr_b16 a[36:37], v10 offset:8192
buffer_load_dwordx4 a[144:147], v4, s[52:55], s75 offen offset:3072 sc1
v_mfma_f32_16x16x32_bf16 v[164:167], a[4:7], a[164:167], v[164:167]
ds_read_b64_tr_b16 a[38:39], v11 offset:8192
v_mfma_f32_16x16x32_bf16 v[196:199], a[4:7], a[168:171], v[196:199]
v_mfma_f32_16x16x32_bf16 v[228:231], a[4:7], a[172:175], v[228:231]
ds_read_b64_tr_b16 a[40:41], v8 offset:8704
s_waitcnt lgkmcnt(15)
v_mfma_f32_16x16x32_bf16 v[136:139], a[8:11], a[160:163], v[136:139]
v_mfma_f32_16x16x32_bf16 v[168:171], a[8:11], a[164:167], v[168:171]
ds_read_b64_tr_b16 a[42:43], v9 offset:8704
v_mfma_f32_16x16x32_bf16 v[200:203], a[8:11], a[168:171], v[200:203]
ds_read_b64_tr_b16 a[44:45], v10 offset:8704
v_mfma_f32_16x16x32_bf16 v[232:235], a[8:11], a[172:175], v[232:235]
buffer_load_dwordx4 a[148:151], v5, s[52:55], s75 offen offset:3072 sc1
s_waitcnt lgkmcnt(15)
v_mfma_f32_16x16x32_bf16 v[140:143], a[12:15], a[160:163], v[140:143]
ds_read_b64_tr_b16 a[46:47], v11 offset:8704
s_add_u32 s78, s69, 24576
v_mfma_f32_16x16x32_bf16 v[172:175], a[12:15], a[164:167], v[172:175]
buffer_store_dwordx4 v[88:91], v1, s[56:59], s78 offen offset:2048 sc1
v_mfma_f32_16x16x32_bf16 v[204:207], a[12:15], a[168:171], v[204:207]
ds_read_b64_tr_b16 a[48:49], v8 offset:9216
v_mfma_f32_16x16x32_bf16 v[236:239], a[12:15], a[172:175], v[236:239]
ds_read_b64_tr_b16 a[50:51], v9 offset:9216
s_waitcnt lgkmcnt(15)
v_mfma_f32_16x16x32_bf16 v[144:147], a[16:19], a[160:163], v[144:147]
v_mfma_f32_16x16x32_bf16 v[176:179], a[16:19], a[164:167], v[176:179]
ds_read_b64_tr_b16 a[52:53], v10 offset:9216
v_mfma_f32_16x16x32_bf16 v[208:211], a[16:19], a[168:171], v[208:211]
ds_read_b64_tr_b16 a[54:55], v11 offset:9216
buffer_load_dwordx4 a[152:155], v6, s[52:55], s75 offen offset:3072 sc1
v_mfma_f32_16x16x32_bf16 v[240:243], a[16:19], a[172:175], v[240:243]
s_waitcnt lgkmcnt(15)
v_mfma_f32_16x16x32_bf16 v[148:151], a[20:23], a[160:163], v[148:151]
ds_read_b64_tr_b16 a[56:57], v8 offset:9728
v_mfma_f32_16x16x32_bf16 v[180:183], a[20:23], a[164:167], v[180:183]
v_mfma_f32_16x16x32_bf16 v[212:215], a[20:23], a[168:171], v[212:215]
ds_read_b64_tr_b16 a[58:59], v9 offset:9728
v_mfma_f32_16x16x32_bf16 v[244:247], a[20:23], a[172:175], v[244:247]
ds_read_b64_tr_b16 a[60:61], v10 offset:9728
s_add_u32 s78, s69, 24576
s_waitcnt lgkmcnt(15)
v_mfma_f32_16x16x32_bf16 v[152:155], a[24:27], a[160:163], v[152:155]
buffer_store_dwordx4 v[92:95], v1, s[56:59], s78 offen offset:3072 sc1
v_mfma_f32_16x16x32_bf16 v[184:187], a[24:27], a[164:167], v[184:187]
ds_read_b64_tr_b16 a[62:63], v11 offset:9728
buffer_load_dwordx4 a[156:159], v7, s[52:55], s75 offen offset:3072 sc1
v_mfma_f32_16x16x32_bf16 v[216:219], a[24:27], a[168:171], v[216:219]
v_mfma_f32_16x16x32_bf16 v[248:251], a[24:27], a[172:175], v[248:251]
v_mfma_f32_16x16x32_bf16 v[156:159], a[28:31], a[160:163], v[156:159]
v_mfma_f32_16x16x32_bf16 v[188:191], a[28:31], a[164:167], v[188:191]
v_mfma_f32_16x16x32_bf16 v[220:223], a[28:31], a[168:171], v[220:223]
v_mfma_f32_16x16x32_bf16 v[252:255], a[28:31], a[172:175], v[252:255]
s_waitcnt lgkmcnt(14)
v_mfma_f32_16x16x32_bf16 v[128:131], a[32:35], a[176:179], v[128:131]
s_mov_b32 s72, 0xf149f2ca
s_mov_b32 s73, 0
v_mfma_f32_16x16x32_bf16 v[160:163], a[32:35], a[180:183], v[160:163]
ds_read_b64_tr_b16 a[0:1], v8 offset:16384
s_waitcnt vmcnt(37)
v_mfma_f32_16x16x32_bf16 v[192:195], a[32:35], a[184:187], v[192:195]
ds_read_b64_tr_b16 a[2:3], v9 offset:16384
v_lshrrev_b32 v21, 4, v1
s_waitcnt vmcnt(36)
v_mfma_f32_16x16x32_bf16 v[224:227], a[32:35], a[188:191], v[224:227]
v_and_b32 v5, 15, v21
v_lshrrev_b32 v21, 4, v21
s_waitcnt lgkmcnt(14)
v_mfma_f32_16x16x32_bf16 v[132:135], a[36:39], a[176:179], v[132:135]
ds_read_b64_tr_b16 a[4:5], v10 offset:16384
v_lshlrev_b32 v21, 2, v21
v_mfma_f32_16x16x32_bf16 v[164:167], a[36:39], a[180:183], v[164:167]
ds_read_b64_tr_b16 a[6:7], v11 offset:16384
v_sub_u32 v5, v5, v21
v_mfma_f32_16x16x32_bf16 v[196:199], a[36:39], a[184:187], v[196:199]
v_mov_b32 v20, s72
v_mov_b32 v6, 0
v_mfma_f32_16x16x32_bf16 v[228:231], a[36:39], a[188:191], v[228:231]
ds_read_b64_tr_b16 a[8:9], v8 offset:16896
v_mov_b32 v7, 0
s_waitcnt lgkmcnt(15)
v_mfma_f32_16x16x32_bf16 v[136:139], a[40:43], a[176:179], v[136:139]
v_mfma_f32_16x16x32_bf16 v[168:171], a[40:43], a[180:183], v[168:171]
ds_read_b64_tr_b16 a[10:11], v9 offset:16896
v_mfma_f32_16x16x32_bf16 v[200:203], a[40:43], a[184:187], v[200:203]
ds_read_b64_tr_b16 a[12:13], v10 offset:16896
s_add_u32 s78, s68, 32768
v_mfma_f32_16x16x32_bf16 v[232:235], a[40:43], a[188:191], v[232:235]
buffer_store_dwordx4 v[36:39], v1, s[56:59], s78 offen offset:0 sc1
s_waitcnt lgkmcnt(15)
v_mfma_f32_16x16x32_bf16 v[140:143], a[44:47], a[176:179], v[140:143]
ds_read_b64_tr_b16 a[14:15], v11 offset:16896
v_mfma_f32_16x16x32_bf16 v[172:175], a[44:47], a[180:183], v[172:175]
v_mfma_f32_16x16x32_bf16 v[204:207], a[44:47], a[184:187], v[204:207]
ds_read_b64_tr_b16 a[16:17], v8 offset:17408
v_mfma_f32_16x16x32_bf16 v[236:239], a[44:47], a[188:191], v[236:239]
ds_read_b64_tr_b16 a[18:19], v9 offset:17408
s_waitcnt lgkmcnt(15)
v_mfma_f32_16x16x32_bf16 v[144:147], a[48:51], a[176:179], v[144:147]
v_mfma_f32_16x16x32_bf16 v[176:179], a[48:51], a[180:183], v[176:179]
ds_read_b64_tr_b16 a[20:21], v10 offset:17408
v_mfma_f32_16x16x32_bf16 v[208:211], a[48:51], a[184:187], v[208:211]
ds_read_b64_tr_b16 a[22:23], v11 offset:17408
v_mfma_f32_16x16x32_bf16 v[240:243], a[48:51], a[188:191], v[240:243]
s_waitcnt lgkmcnt(15)
v_mfma_f32_16x16x32_bf16 v[148:151], a[52:55], a[176:179], v[148:151]
ds_read_b64_tr_b16 a[24:25], v8 offset:17920
s_add_u32 s78, s68, 32768
v_mfma_f32_16x16x32_bf16 v[180:183], a[52:55], a[180:183], v[180:183]
buffer_store_dwordx4 v[32:35], v1, s[56:59], s78 offen offset:1024 sc1
v_mfma_f32_16x16x32_bf16 v[212:215], a[52:55], a[184:187], v[212:215]
ds_read_b64_tr_b16 a[26:27], v9 offset:17920
v_mfma_f32_16x16x32_bf16 v[244:247], a[52:55], a[188:191], v[244:247]
)ASM"
R"ASM(ds_read_b64_tr_b16 a[28:29], v10 offset:17920
s_waitcnt lgkmcnt(15)
v_mfma_f32_16x16x32_bf16 v[152:155], a[56:59], a[176:179], v[152:155]
v_mfma_f32_16x16x32_bf16 v[184:187], a[56:59], a[180:183], v[184:187]
ds_read_b64_tr_b16 a[30:31], v11 offset:17920
v_mfma_f32_16x16x32_bf16 v[216:219], a[56:59], a[184:187], v[216:219]
v_mfma_f32_16x16x32_bf16 v[248:251], a[56:59], a[188:191], v[248:251]
v_mfma_f32_16x16x32_bf16 v[156:159], a[60:63], a[176:179], v[156:159]
v_mfma_f32_16x16x32_bf16 v[188:191], a[60:63], a[180:183], v[188:191]
v_mfma_f32_16x16x32_bf16 v[220:223], a[60:63], a[184:187], v[220:223]
v_mfma_f32_16x16x32_bf16 v[252:255], a[60:63], a[188:191], v[252:255]
s_waitcnt vmcnt(37) lgkmcnt(14)
v_mfma_f32_16x16x32_bf16 v[128:131], a[0:3], a[192:195], v[128:131]
v_cmp_gt_i32 vcc, 0, v5
s_waitcnt vmcnt(36)
v_mfma_f32_16x16x32_bf16 v[160:163], a[0:3], a[196:199], v[160:163]
ds_read_b64_tr_b16 a[32:33], v8 offset:24576
s_waitcnt vmcnt(34)
v_mfma_f32_16x16x32_bf16 v[192:195], a[0:3], a[200:203], v[192:195]
ds_read_b64_tr_b16 a[34:35], v9 offset:24576
v_cndmask_b32 v112, 0, v20, vcc
s_waitcnt vmcnt(32)
v_mfma_f32_16x16x32_bf16 v[224:227], a[0:3], a[204:207], v[224:227]
s_waitcnt lgkmcnt(14)
v_mfma_f32_16x16x32_bf16 v[132:135], a[4:7], a[192:195], v[132:135]
ds_read_b64_tr_b16 a[36:37], v10 offset:24576
v_cmp_gt_i32 vcc, 1, v5
v_mfma_f32_16x16x32_bf16 v[164:167], a[4:7], a[196:199], v[164:167]
ds_read_b64_tr_b16 a[38:39], v11 offset:24576
v_mfma_f32_16x16x32_bf16 v[196:199], a[4:7], a[200:203], v[196:199]
v_cndmask_b32 v113, 0, v20, vcc
v_mfma_f32_16x16x32_bf16 v[228:231], a[4:7], a[204:207], v[228:231]
ds_read_b64_tr_b16 a[40:41], v8 offset:25088
s_waitcnt lgkmcnt(15)
v_mfma_f32_16x16x32_bf16 v[136:139], a[8:11], a[192:195], v[136:139]
v_cmp_gt_i32 vcc, 2, v5
v_mfma_f32_16x16x32_bf16 v[168:171], a[8:11], a[196:199], v[168:171]
ds_read_b64_tr_b16 a[42:43], v9 offset:25088
v_mfma_f32_16x16x32_bf16 v[200:203], a[8:11], a[200:203], v[200:203]
ds_read_b64_tr_b16 a[44:45], v10 offset:25088
s_add_u32 s78, s68, 36864
v_cndmask_b32 v114, 0, v20, vcc
v_mfma_f32_16x16x32_bf16 v[232:235], a[8:11], a[204:207], v[232:235]
buffer_store_dwordx4 v[44:47], v1, s[56:59], s78 offen offset:0 sc1
s_waitcnt lgkmcnt(15)
v_mfma_f32_16x16x32_bf16 v[140:143], a[12:15], a[192:195], v[140:143]
ds_read_b64_tr_b16 a[46:47], v11 offset:25088
v_cmp_gt_i32 vcc, 3, v5
v_mfma_f32_16x16x32_bf16 v[172:175], a[12:15], a[196:199], v[172:175]
v_mfma_f32_16x16x32_bf16 v[204:207], a[12:15], a[200:203], v[204:207]
ds_read_b64_tr_b16 a[48:49], v8 offset:25600
v_cndmask_b32 v115, 0, v20, vcc
v_mfma_f32_16x16x32_bf16 v[236:239], a[12:15], a[204:207], v[236:239]
ds_read_b64_tr_b16 a[50:51], v9 offset:25600
s_waitcnt lgkmcnt(15)
v_mfma_f32_16x16x32_bf16 v[144:147], a[16:19], a[192:195], v[144:147]
v_cmp_gt_i32 vcc, -16, v5
v_mfma_f32_16x16x32_bf16 v[176:179], a[16:19], a[196:199], v[176:179]
ds_read_b64_tr_b16 a[52:53], v10 offset:25600
v_mfma_f32_16x16x32_bf16 v[208:211], a[16:19], a[200:203], v[208:211]
ds_read_b64_tr_b16 a[54:55], v11 offset:25600
v_cndmask_b32 v116, 0, v20, vcc
v_mfma_f32_16x16x32_bf16 v[240:243], a[16:19], a[204:207], v[240:243]
s_waitcnt lgkmcnt(15)
v_mfma_f32_16x16x32_bf16 v[148:151], a[20:23], a[192:195], v[148:151]
ds_read_b64_tr_b16 a[56:57], v8 offset:26112
s_add_u32 s78, s68, 36864
v_cmp_gt_i32 vcc, -15, v5
v_mfma_f32_16x16x32_bf16 v[180:183], a[20:23], a[196:199], v[180:183]
buffer_store_dwordx4 v[40:43], v1, s[56:59], s78 offen offset:1024 sc1
v_mfma_f32_16x16x32_bf16 v[212:215], a[20:23], a[200:203], v[212:215]
ds_read_b64_tr_b16 a[58:59], v9 offset:26112
v_cndmask_b32 v117, 0, v20, vcc
v_mfma_f32_16x16x32_bf16 v[244:247], a[20:23], a[204:207], v[244:247]
ds_read_b64_tr_b16 a[60:61], v10 offset:26112
s_waitcnt lgkmcnt(15)
v_mfma_f32_16x16x32_bf16 v[152:155], a[24:27], a[192:195], v[152:155]
v_cmp_gt_i32 vcc, -14, v5
v_mfma_f32_16x16x32_bf16 v[184:187], a[24:27], a[196:199], v[184:187]
ds_read_b64_tr_b16 a[62:63], v11 offset:26112
v_mfma_f32_16x16x32_bf16 v[216:219], a[24:27], a[200:203], v[216:219]
v_cndmask_b32 v118, 0, v20, vcc
v_mfma_f32_16x16x32_bf16 v[248:251], a[24:27], a[204:207], v[248:251]
v_mfma_f32_16x16x32_bf16 v[156:159], a[28:31], a[192:195], v[156:159]
v_cmp_gt_i32 vcc, -13, v5
v_mfma_f32_16x16x32_bf16 v[188:191], a[28:31], a[196:199], v[188:191]
v_mfma_f32_16x16x32_bf16 v[220:223], a[28:31], a[200:203], v[220:223]
v_cndmask_b32 v119, 0, v20, vcc
v_mfma_f32_16x16x32_bf16 v[252:255], a[28:31], a[204:207], v[252:255]
s_waitcnt vmcnt(33) lgkmcnt(14)
v_mfma_f32_16x16x32_bf16 v[128:131], a[32:35], a[208:211], v[128:131]
v_cmp_gt_i32 vcc, 16, v5
s_waitcnt vmcnt(32)
v_mfma_f32_16x16x32_bf16 v[160:163], a[32:35], a[212:215], v[160:163]
ds_read_b64_tr_b16 a[0:1], v8 offset:32768
s_waitcnt vmcnt(30)
v_mfma_f32_16x16x32_bf16 v[192:195], a[32:35], a[216:219], v[192:195]
ds_read_b64_tr_b16 a[2:3], v9 offset:32768
v_cndmask_b32 v120, 0, v20, vcc
s_waitcnt vmcnt(28)
v_mfma_f32_16x16x32_bf16 v[224:227], a[32:35], a[220:223], v[224:227]
s_waitcnt lgkmcnt(14)
v_mfma_f32_16x16x32_bf16 v[132:135], a[36:39], a[208:211], v[132:135]
ds_read_b64_tr_b16 a[4:5], v10 offset:32768
v_cmp_gt_i32 vcc, 17, v5
v_mfma_f32_16x16x32_bf16 v[164:167], a[36:39], a[212:215], v[164:167]
ds_read_b64_tr_b16 a[6:7], v11 offset:32768
v_mfma_f32_16x16x32_bf16 v[196:199], a[36:39], a[216:219], v[196:199]
v_cndmask_b32 v121, 0, v20, vcc
v_mfma_f32_16x16x32_bf16 v[228:231], a[36:39], a[220:223], v[228:231]
ds_read_b64_tr_b16 a[8:9], v8 offset:33280
s_waitcnt lgkmcnt(15)
v_mfma_f32_16x16x32_bf16 v[136:139], a[40:43], a[208:211], v[136:139]
v_cmp_gt_i32 vcc, 18, v5
v_mfma_f32_16x16x32_bf16 v[168:171], a[40:43], a[212:215], v[168:171]
ds_read_b64_tr_b16 a[10:11], v9 offset:33280
v_mfma_f32_16x16x32_bf16 v[200:203], a[40:43], a[216:219], v[200:203]
ds_read_b64_tr_b16 a[12:13], v10 offset:33280
v_cndmask_b32 v122, 0, v20, vcc
v_mfma_f32_16x16x32_bf16 v[232:235], a[40:43], a[220:223], v[232:235]
s_waitcnt lgkmcnt(15)
v_mfma_f32_16x16x32_bf16 v[140:143], a[44:47], a[208:211], v[140:143]
ds_read_b64_tr_b16 a[14:15], v11 offset:33280
v_cmp_gt_i32 vcc, 19, v5
v_mfma_f32_16x16x32_bf16 v[172:175], a[44:47], a[212:215], v[172:175]
v_mfma_f32_16x16x32_bf16 v[204:207], a[44:47], a[216:219], v[204:207]
ds_read_b64_tr_b16 a[16:17], v8 offset:33792
v_cndmask_b32 v123, 0, v20, vcc
v_mfma_f32_16x16x32_bf16 v[236:239], a[44:47], a[220:223], v[236:239]
ds_read_b64_tr_b16 a[18:19], v9 offset:33792
s_waitcnt lgkmcnt(15)
v_mfma_f32_16x16x32_bf16 v[144:147], a[48:51], a[208:211], v[144:147]
v_cmp_gt_i32 vcc, 0, v5
v_mfma_f32_16x16x32_bf16 v[176:179], a[48:51], a[212:215], v[176:179]
ds_read_b64_tr_b16 a[20:21], v10 offset:33792
v_mfma_f32_16x16x32_bf16 v[208:211], a[48:51], a[216:219], v[208:211]
ds_read_b64_tr_b16 a[22:23], v11 offset:33792
v_cndmask_b32 v124, 0, v20, vcc
v_mfma_f32_16x16x32_bf16 v[240:243], a[48:51], a[220:223], v[240:243]
s_waitcnt lgkmcnt(15)
v_mfma_f32_16x16x32_bf16 v[148:151], a[52:55], a[208:211], v[148:151]
ds_read_b64_tr_b16 a[24:25], v8 offset:34304
v_cmp_gt_i32 vcc, 1, v5
v_mfma_f32_16x16x32_bf16 v[180:183], a[52:55], a[212:215], v[180:183]
v_mfma_f32_16x16x32_bf16 v[212:215], a[52:55], a[216:219], v[212:215]
ds_read_b64_tr_b16 a[26:27], v9 offset:34304
v_cndmask_b32 v125, 0, v20, vcc
v_mfma_f32_16x16x32_bf16 v[244:247], a[52:55], a[220:223], v[244:247]
ds_read_b64_tr_b16 a[28:29], v10 offset:34304
s_waitcnt lgkmcnt(15)
v_mfma_f32_16x16x32_bf16 v[152:155], a[56:59], a[208:211], v[152:155]
v_cmp_gt_i32 vcc, 2, v5
v_mfma_f32_16x16x32_bf16 v[184:187], a[56:59], a[212:215], v[184:187]
ds_read_b64_tr_b16 a[30:31], v11 offset:34304
v_mfma_f32_16x16x32_bf16 v[216:219], a[56:59], a[216:219], v[216:219]
v_cndmask_b32 v126, 0, v20, vcc
v_mfma_f32_16x16x32_bf16 v[248:251], a[56:59], a[220:223], v[248:251]
v_mfma_f32_16x16x32_bf16 v[156:159], a[60:63], a[208:211], v[156:159]
v_cmp_gt_i32 vcc, 3, v5
v_mfma_f32_16x16x32_bf16 v[188:191], a[60:63], a[212:215], v[188:191]
v_mov_b32 v16, s33
v_mfma_f32_16x16x32_bf16 v[220:223], a[60:63], a[216:219], v[220:223]
v_mov_b32 v17, 0
v_cndmask_b32 v127, 0, v20, vcc
v_mfma_f32_16x16x32_bf16 v[252:255], a[60:63], a[220:223], v[252:255]
s_waitcnt vmcnt(27) lgkmcnt(14)
v_mfma_f32_16x16x32_bf16 v[128:131], a[0:3], a[224:227], v[128:131]
s_waitcnt vmcnt(0)
s_barrier
v_mfma_f32_16x16x32_bf16 v[160:163], a[0:3], a[228:231], v[160:163]
ds_read_b64_tr_b16 a[32:33], v8 offset:40960
v_mfma_f32_16x16x32_bf16 v[192:195], a[0:3], a[232:235], v[192:195]
ds_read_b64_tr_b16 a[34:35], v9 offset:40960
s_lshl_b32 s79, s38, 7
s_add_u32 s79, s79, 65536
s_mov_b64 exec, s[34:35]
buffer_store_dword v16, v17, s[40:43], s79 offen sc1
s_mov_b64 exec, -1
v_mfma_f32_16x16x32_bf16 v[224:227], a[0:3], a[236:239], v[224:227]
s_waitcnt lgkmcnt(14)
v_mfma_f32_16x16x32_bf16 v[132:135], a[4:7], a[224:227], v[132:135]
ds_read_b64_tr_b16 a[36:37], v10 offset:40960
v_mfma_f32_16x16x32_bf16 v[164:167], a[4:7], a[228:231], v[164:167]
ds_read_b64_tr_b16 a[38:39], v11 offset:40960
v_mfma_f32_16x16x32_bf16 v[196:199], a[4:7], a[232:235], v[196:199]
v_mov_b32 v96, 0
v_mov_b32 v97, 0
v_mfma_f32_16x16x32_bf16 v[228:231], a[4:7], a[236:239], v[228:231]
ds_read_b64_tr_b16 a[40:41], v8 offset:41472
v_mov_b32 v98, 0
s_waitcnt lgkmcnt(15)
v_mfma_f32_16x16x32_bf16 v[136:139], a[8:11], a[224:227], v[136:139]
v_mov_b32 v99, 0
v_mov_b32 v100, 0
v_mfma_f32_16x16x32_bf16 v[168:171], a[8:11], a[228:231], v[168:171]
ds_read_b64_tr_b16 a[42:43], v9 offset:41472
v_mov_b32 v101, 0
v_mfma_f32_16x16x32_bf16 v[200:203], a[8:11], a[232:235], v[200:203]
ds_read_b64_tr_b16 a[44:45], v10 offset:41472
v_mov_b32 v102, 0
v_mfma_f32_16x16x32_bf16 v[232:235], a[8:11], a[236:239], v[232:235]
v_mov_b32 v103, 0
v_mov_b32 v104, 0
s_waitcnt lgkmcnt(15)
v_mfma_f32_16x16x32_bf16 v[140:143], a[12:15], a[224:227], v[140:143]
ds_read_b64_tr_b16 a[46:47], v11 offset:41472
v_mov_b32 v105, 0
v_mfma_f32_16x16x32_bf16 v[172:175], a[12:15], a[228:231], v[172:175]
v_mov_b32 v106, 0
v_mov_b32 v107, 0
v_mfma_f32_16x16x32_bf16 v[204:207], a[12:15], a[232:235], v[204:207]
ds_read_b64_tr_b16 a[48:49], v8 offset:41984
v_mov_b32 v108, 0
v_mfma_f32_16x16x32_bf16 v[236:239], a[12:15], a[236:239], v[236:239]
ds_read_b64_tr_b16 a[50:51], v9 offset:41984
v_mov_b32 v109, 0
s_waitcnt lgkmcnt(15)
v_mfma_f32_16x16x32_bf16 v[144:147], a[16:19], a[224:227], v[144:147]
v_mov_b32 v110, 0
v_mov_b32 v111, 0
v_mfma_f32_16x16x32_bf16 v[176:179], a[16:19], a[228:231], v[176:179]
ds_read_b64_tr_b16 a[52:53], v10 offset:41984
v_mfma_f32_16x16x32_bf16 v[208:211], a[16:19], a[232:235], v[208:211]
ds_read_b64_tr_b16 a[54:55], v11 offset:41984
v_mfma_f32_16x16x32_bf16 v[240:243], a[16:19], a[236:239], v[240:243]
s_waitcnt lgkmcnt(15)
v_mfma_f32_16x16x32_bf16 v[148:151], a[20:23], a[224:227], v[148:151]
ds_read_b64_tr_b16 a[56:57], v8 offset:42496
v_mfma_f32_16x16x32_bf16 v[180:183], a[20:23], a[228:231], v[180:183]
v_mfma_f32_16x16x32_bf16 v[212:215], a[20:23], a[232:235], v[212:215]
ds_read_b64_tr_b16 a[58:59], v9 offset:42496
v_mfma_f32_16x16x32_bf16 v[244:247], a[20:23], a[236:239], v[244:247]
ds_read_b64_tr_b16 a[60:61], v10 offset:42496
s_waitcnt lgkmcnt(15)
v_mfma_f32_16x16x32_bf16 v[152:155], a[24:27], a[224:227], v[152:155]
v_mfma_f32_16x16x32_bf16 v[184:187], a[24:27], a[228:231], v[184:187]
ds_read_b64_tr_b16 a[62:63], v11 offset:42496
v_mfma_f32_16x16x32_bf16 v[216:219], a[24:27], a[232:235], v[216:219]
v_mfma_f32_16x16x32_bf16 v[248:251], a[24:27], a[236:239], v[248:251]
v_mfma_f32_16x16x32_bf16 v[156:159], a[28:31], a[224:227], v[156:159]
v_mfma_f32_16x16x32_bf16 v[188:191], a[28:31], a[228:231], v[188:191]
v_mfma_f32_16x16x32_bf16 v[220:223], a[28:31], a[232:235], v[220:223]
v_mfma_f32_16x16x32_bf16 v[252:255], a[28:31], a[236:239], v[252:255]
s_waitcnt lgkmcnt(14)
v_mfma_f32_16x16x32_bf16 v[128:131], a[32:35], a[240:243], v[128:131]
v_mov_b32 v64, 0
v_mov_b32 v65, 0
v_mfma_f32_16x16x32_bf16 v[160:163], a[32:35], a[244:247], v[160:163]
ds_read_b64_tr_b16 a[0:1], v8 offset:49152
v_mov_b32 v66, 0
v_mfma_f32_16x16x32_bf16 v[192:195], a[32:35], a[248:251], v[192:195]
ds_read_b64_tr_b16 a[2:3], v9 offset:49152
v_mov_b32 v67, 0
v_mfma_f32_16x16x32_bf16 v[224:227], a[32:35], a[252:255], v[224:227]
v_mov_b32 v68, 0
v_mov_b32 v69, 0
s_waitcnt lgkmcnt(14)
v_mfma_f32_16x16x32_bf16 v[132:135], a[36:39], a[240:243], v[132:135]
ds_read_b64_tr_b16 a[4:5], v10 offset:49152
v_mov_b32 v70, 0
v_mfma_f32_16x16x32_bf16 v[164:167], a[36:39], a[244:247], v[164:167]
ds_read_b64_tr_b16 a[6:7], v11 offset:49152
v_mov_b32 v71, 0
v_mfma_f32_16x16x32_bf16 v[196:199], a[36:39], a[248:251], v[196:199]
v_mov_b32 v72, 0
v_mov_b32 v73, 0
v_mfma_f32_16x16x32_bf16 v[228:231], a[36:39], a[252:255], v[228:231]
ds_read_b64_tr_b16 a[8:9], v8 offset:49664
v_mov_b32 v74, 0
s_waitcnt lgkmcnt(15)
v_mfma_f32_16x16x32_bf16 v[136:139], a[40:43], a[240:243], v[136:139]
v_mov_b32 v75, 0
v_mov_b32 v76, 0
v_mfma_f32_16x16x32_bf16 v[168:171], a[40:43], a[244:247], v[168:171]
ds_read_b64_tr_b16 a[10:11], v9 offset:49664
v_mov_b32 v77, 0
v_mfma_f32_16x16x32_bf16 v[200:203], a[40:43], a[248:251], v[200:203]
ds_read_b64_tr_b16 a[12:13], v10 offset:49664
v_mov_b32 v78, 0
v_mfma_f32_16x16x32_bf16 v[232:235], a[40:43], a[252:255], v[232:235]
v_mov_b32 v79, 0
v_mov_b32 v48, 0
s_waitcnt lgkmcnt(15)
v_mfma_f32_16x16x32_bf16 v[140:143], a[44:47], a[240:243], v[140:143]
ds_read_b64_tr_b16 a[14:15], v11 offset:49664
v_mov_b32 v49, 0
v_mfma_f32_16x16x32_bf16 v[172:175], a[44:47], a[244:247], v[172:175]
v_mov_b32 v50, 0
v_mov_b32 v51, 0
v_mfma_f32_16x16x32_bf16 v[204:207], a[44:47], a[248:251], v[204:207]
ds_read_b64_tr_b16 a[16:17], v8 offset:50176
v_mov_b32 v52, 0
v_mfma_f32_16x16x32_bf16 v[236:239], a[44:47], a[252:255], v[236:239]
ds_read_b64_tr_b16 a[18:19], v9 offset:50176
v_mov_b32 v53, 0
s_waitcnt lgkmcnt(15)
v_mfma_f32_16x16x32_bf16 v[144:147], a[48:51], a[240:243], v[144:147]
s_add_u32 s56, s30, 35651584
v_mov_b32 v54, 0
v_mov_b32 v55, 0
)ASM"
R"ASM(v_mfma_f32_16x16x32_bf16 v[176:179], a[48:51], a[244:247], v[176:179]
ds_read_b64_tr_b16 a[20:21], v10 offset:50176
s_addc_u32 s57, s31, 0
v_mov_b32 v56, 0
v_mfma_f32_16x16x32_bf16 v[208:211], a[48:51], a[248:251], v[208:211]
ds_read_b64_tr_b16 a[22:23], v11 offset:50176
s_and_b32 s57, s57, 0xffff
v_mov_b32 v57, 0
v_mfma_f32_16x16x32_bf16 v[240:243], a[48:51], a[252:255], v[240:243]
s_mov_b32 s58, 16777216
v_mov_b32 v58, 0
v_mov_b32 v59, 0
s_waitcnt lgkmcnt(15)
v_mfma_f32_16x16x32_bf16 v[148:151], a[52:55], a[240:243], v[148:151]
ds_read_b64_tr_b16 a[24:25], v8 offset:50688
s_mov_b32 s59, 0x20000
v_mov_b32 v60, 0
v_mfma_f32_16x16x32_bf16 v[180:183], a[52:55], a[244:247], v[180:183]
v_mov_b32 v61, 0
v_mov_b32 v62, 0
v_mfma_f32_16x16x32_bf16 v[212:215], a[52:55], a[248:251], v[212:215]
ds_read_b64_tr_b16 a[26:27], v9 offset:50688
v_mov_b32 v63, 0
v_mfma_f32_16x16x32_bf16 v[244:247], a[52:55], a[252:255], v[244:247]
ds_read_b64_tr_b16 a[28:29], v10 offset:50688
s_waitcnt lgkmcnt(15)
v_mfma_f32_16x16x32_bf16 v[152:155], a[56:59], a[240:243], v[152:155]
v_mfma_f32_16x16x32_bf16 v[184:187], a[56:59], a[244:247], v[184:187]
ds_read_b64_tr_b16 a[30:31], v11 offset:50688
v_mfma_f32_16x16x32_bf16 v[216:219], a[56:59], a[248:251], v[216:219]
v_mfma_f32_16x16x32_bf16 v[248:251], a[56:59], a[252:255], v[248:251]
v_mfma_f32_16x16x32_bf16 v[156:159], a[60:63], a[240:243], v[156:159]
v_mfma_f32_16x16x32_bf16 v[188:191], a[60:63], a[244:247], v[188:191]
v_mfma_f32_16x16x32_bf16 v[220:223], a[60:63], a[248:251], v[220:223]
v_mfma_f32_16x16x32_bf16 v[252:255], a[60:63], a[252:255], v[252:255]
s_waitcnt lgkmcnt(14)
v_mfma_f32_16x16x32_bf16 v[128:131], a[0:3], a[128:131], v[128:131]
s_lshl_b32 s62, s39, 13
v_mfma_f32_16x16x32_bf16 v[160:163], a[0:3], a[132:135], v[160:163]
ds_read_b64_tr_b16 a[32:33], v8 offset:57344
s_mov_b32 s66, s62
v_mfma_f32_16x16x32_bf16 v[192:195], a[0:3], a[136:139], v[192:195]
ds_read_b64_tr_b16 a[34:35], v9 offset:57344
s_mov_b32 s64, 0
v_mfma_f32_16x16x32_bf16 v[224:227], a[0:3], a[140:143], v[224:227]
s_mov_b32 s65, 4096
s_waitcnt lgkmcnt(14)
v_mfma_f32_16x16x32_bf16 v[132:135], a[4:7], a[128:131], v[132:135]
ds_read_b64_tr_b16 a[36:37], v10 offset:57344
v_mfma_f32_16x16x32_bf16 v[164:167], a[4:7], a[132:135], v[164:167]
ds_read_b64_tr_b16 a[38:39], v11 offset:57344
v_mfma_f32_16x16x32_bf16 v[196:199], a[4:7], a[136:139], v[196:199]
v_mfma_f32_16x16x32_bf16 v[228:231], a[4:7], a[140:143], v[228:231]
ds_read_b64_tr_b16 a[40:41], v8 offset:57856
s_waitcnt lgkmcnt(15)
v_mfma_f32_16x16x32_bf16 v[136:139], a[8:11], a[128:131], v[136:139]
s_mov_b32 s79, 65536
buffer_load_dword v19, v18, s[40:43], s79 offen sc1
v_mfma_f32_16x16x32_bf16 v[168:171], a[8:11], a[132:135], v[168:171]
ds_read_b64_tr_b16 a[42:43], v9 offset:57856
v_mfma_f32_16x16x32_bf16 v[200:203], a[8:11], a[136:139], v[200:203]
ds_read_b64_tr_b16 a[44:45], v10 offset:57856
v_mfma_f32_16x16x32_bf16 v[232:235], a[8:11], a[140:143], v[232:235]
s_waitcnt lgkmcnt(15)
v_mfma_f32_16x16x32_bf16 v[140:143], a[12:15], a[128:131], v[140:143]
ds_read_b64_tr_b16 a[46:47], v11 offset:57856
v_mfma_f32_16x16x32_bf16 v[172:175], a[12:15], a[132:135], v[172:175]
v_mfma_f32_16x16x32_bf16 v[204:207], a[12:15], a[136:139], v[204:207]
ds_read_b64_tr_b16 a[48:49], v8 offset:58368
v_mfma_f32_16x16x32_bf16 v[236:239], a[12:15], a[140:143], v[236:239]
ds_read_b64_tr_b16 a[50:51], v9 offset:58368
s_waitcnt lgkmcnt(15)
v_mfma_f32_16x16x32_bf16 v[144:147], a[16:19], a[128:131], v[144:147]
v_mfma_f32_16x16x32_bf16 v[176:179], a[16:19], a[132:135], v[176:179]
ds_read_b64_tr_b16 a[52:53], v10 offset:58368
v_mfma_f32_16x16x32_bf16 v[208:211], a[16:19], a[136:139], v[208:211]
ds_read_b64_tr_b16 a[54:55], v11 offset:58368
v_mfma_f32_16x16x32_bf16 v[240:243], a[16:19], a[140:143], v[240:243]
s_waitcnt lgkmcnt(15)
v_mfma_f32_16x16x32_bf16 v[148:151], a[20:23], a[128:131], v[148:151]
ds_read_b64_tr_b16 a[56:57], v8 offset:58880
v_mfma_f32_16x16x32_bf16 v[180:183], a[20:23], a[132:135], v[180:183]
v_mfma_f32_16x16x32_bf16 v[212:215], a[20:23], a[136:139], v[212:215]
ds_read_b64_tr_b16 a[58:59], v9 offset:58880
v_mfma_f32_16x16x32_bf16 v[244:247], a[20:23], a[140:143], v[244:247]
ds_read_b64_tr_b16 a[60:61], v10 offset:58880
s_waitcnt lgkmcnt(15)
v_mfma_f32_16x16x32_bf16 v[152:155], a[24:27], a[128:131], v[152:155]
v_mfma_f32_16x16x32_bf16 v[184:187], a[24:27], a[132:135], v[184:187]
ds_read_b64_tr_b16 a[62:63], v11 offset:58880
v_mfma_f32_16x16x32_bf16 v[216:219], a[24:27], a[136:139], v[216:219]
v_mfma_f32_16x16x32_bf16 v[248:251], a[24:27], a[140:143], v[248:251]
v_mfma_f32_16x16x32_bf16 v[156:159], a[28:31], a[128:131], v[156:159]
v_add_u32 v12, s62, v1
v_mfma_f32_16x16x32_bf16 v[188:191], a[28:31], a[132:135], v[188:191]
v_mov_b32 v2, v1
v_mfma_f32_16x16x32_bf16 v[220:223], a[28:31], a[136:139], v[220:223]
v_add_u32 v3, 0x18000, v1
v_mfma_f32_16x16x32_bf16 v[252:255], a[28:31], a[140:143], v[252:255]
s_waitcnt lgkmcnt(14)
v_mfma_f32_16x16x32_bf16 v[128:131], a[32:35], a[144:147], v[128:131]
v_mfma_f32_16x16x32_bf16 v[160:163], a[32:35], a[148:151], v[160:163]
v_mfma_f32_16x16x32_bf16 v[192:195], a[32:35], a[152:155], v[192:195]
v_mfma_f32_16x16x32_bf16 v[224:227], a[32:35], a[156:159], v[224:227]
s_waitcnt lgkmcnt(12)
v_mfma_f32_16x16x32_bf16 v[132:135], a[36:39], a[144:147], v[132:135]
v_mfma_f32_16x16x32_bf16 v[164:167], a[36:39], a[148:151], v[164:167]
v_mfma_f32_16x16x32_bf16 v[196:199], a[36:39], a[152:155], v[196:199]
v_mfma_f32_16x16x32_bf16 v[228:231], a[36:39], a[156:159], v[228:231]
s_waitcnt lgkmcnt(10)
v_mfma_f32_16x16x32_bf16 v[136:139], a[40:43], a[144:147], v[136:139]
v_mfma_f32_16x16x32_bf16 v[168:171], a[40:43], a[148:151], v[168:171]
v_mfma_f32_16x16x32_bf16 v[200:203], a[40:43], a[152:155], v[200:203]
v_mfma_f32_16x16x32_bf16 v[232:235], a[40:43], a[156:159], v[232:235]
s_waitcnt lgkmcnt(8)
v_mfma_f32_16x16x32_bf16 v[140:143], a[44:47], a[144:147], v[140:143]
v_mfma_f32_16x16x32_bf16 v[172:175], a[44:47], a[148:151], v[172:175]
v_mfma_f32_16x16x32_bf16 v[204:207], a[44:47], a[152:155], v[204:207]
v_mfma_f32_16x16x32_bf16 v[236:239], a[44:47], a[156:159], v[236:239]
s_waitcnt lgkmcnt(6)
v_mfma_f32_16x16x32_bf16 v[144:147], a[48:51], a[144:147], v[144:147]
v_mfma_f32_16x16x32_bf16 v[176:179], a[48:51], a[148:151], v[176:179]
v_mfma_f32_16x16x32_bf16 v[208:211], a[48:51], a[152:155], v[208:211]
v_mfma_f32_16x16x32_bf16 v[240:243], a[48:51], a[156:159], v[240:243]
s_waitcnt lgkmcnt(4)
v_mfma_f32_16x16x32_bf16 v[148:151], a[52:55], a[144:147], v[148:151]
v_mfma_f32_16x16x32_bf16 v[180:183], a[52:55], a[148:151], v[180:183]
v_mfma_f32_16x16x32_bf16 v[212:215], a[52:55], a[152:155], v[212:215]
v_mfma_f32_16x16x32_bf16 v[244:247], a[52:55], a[156:159], v[244:247]
s_waitcnt lgkmcnt(2)
v_mfma_f32_16x16x32_bf16 v[152:155], a[56:59], a[144:147], v[152:155]
v_mfma_f32_16x16x32_bf16 v[184:187], a[56:59], a[148:151], v[184:187]
v_mfma_f32_16x16x32_bf16 v[216:219], a[56:59], a[152:155], v[216:219]
v_mfma_f32_16x16x32_bf16 v[248:251], a[56:59], a[156:159], v[248:251]
s_waitcnt lgkmcnt(0)
v_mfma_f32_16x16x32_bf16 v[156:159], a[60:63], a[144:147], v[156:159]
v_mfma_f32_16x16x32_bf16 v[188:191], a[60:63], a[148:151], v[188:191]
v_mfma_f32_16x16x32_bf16 v[220:223], a[60:63], a[152:155], v[220:223]
v_mfma_f32_16x16x32_bf16 v[252:255], a[60:63], a[156:159], v[252:255]
s_waitcnt vmcnt(0) lgkmcnt(0)
v_cmp_ne_u32 vcc, s33, v19
s_barrier
s_nop 3
s_cbranch_vccnz L_slow2%=
L_back2%=:
s_nop 7
v_mov_b32 v8, 0
v_mov_b32 v9, 0
s_mov_b32 m0, s66
s_nop 0
buffer_load_dwordx4 v12, s[44:47], s64 offen offset:0 sc1 lds
buffer_load_dwordx4 v12, s[44:47], s64 offen offset:1024 sc1 lds
buffer_load_dwordx4 v12, s[44:47], s64 offen offset:2048 sc1 lds
buffer_load_dwordx4 v12, s[44:47], s64 offen offset:3072 sc1 lds
v_cvt_pk_bf16_f32 v16, v128, v129
v_cvt_pk_bf16_f32 v17, v130, v131
v_cvt_pk_bf16_f32 v18, v132, v133
v_cvt_pk_bf16_f32 v19, v134, v135
v_mov_b32 v128, 0
v_mov_b32 v129, 0
v_mov_b32 v130, 0
v_mov_b32 v131, 0
v_accvgpr_write_b32 a64, v16
v_accvgpr_write_b32 a65, v17
v_accvgpr_write_b32 a66, v18
v_accvgpr_write_b32 a67, v19
v_mov_b32 v132, 0
v_mov_b32 v133, 0
v_mov_b32 v134, 0
v_mov_b32 v135, 0
s_add_u32 m0, s66, 4096
s_nop 0
buffer_load_dwordx4 v12, s[44:47], s65 offen offset:0 sc1 lds
buffer_load_dwordx4 v12, s[44:47], s65 offen offset:1024 sc1 lds
buffer_load_dwordx4 v12, s[44:47], s65 offen offset:2048 sc1 lds
buffer_load_dwordx4 v12, s[44:47], s65 offen offset:3072 sc1 lds
s_add_u32 s64, s64, 32768
s_add_u32 s65, s65, 32768
s_add_u32 s66, s66, 32768
s_and_b32 s66, s66, 0x1ffff
v_cvt_pk_bf16_f32 v20, v136, v137
v_cvt_pk_bf16_f32 v21, v138, v139
v_cvt_pk_bf16_f32 v22, v140, v141
v_cvt_pk_bf16_f32 v23, v142, v143
v_mov_b32 v136, 0
v_mov_b32 v137, 0
v_mov_b32 v138, 0
v_mov_b32 v139, 0
v_accvgpr_write_b32 a68, v20
v_accvgpr_write_b32 a69, v21
v_accvgpr_write_b32 a70, v22
v_accvgpr_write_b32 a71, v23
v_mov_b32 v140, 0
v_mov_b32 v141, 0
v_mov_b32 v142, 0
v_mov_b32 v143, 0
s_mov_b32 m0, s66
s_nop 0
buffer_load_dwordx4 v12, s[44:47], s64 offen offset:0 sc1 lds
buffer_load_dwordx4 v12, s[44:47], s64 offen offset:1024 sc1 lds
buffer_load_dwordx4 v12, s[44:47], s64 offen offset:2048 sc1 lds
buffer_load_dwordx4 v12, s[44:47], s64 offen offset:3072 sc1 lds
v_cvt_pk_bf16_f32 v24, v144, v145
v_cvt_pk_bf16_f32 v25, v146, v147
v_cvt_pk_bf16_f32 v26, v148, v149
v_cvt_pk_bf16_f32 v27, v150, v151
v_mov_b32 v144, 0
v_mov_b32 v145, 0
v_mov_b32 v146, 0
v_mov_b32 v147, 0
v_accvgpr_write_b32 a72, v24
v_accvgpr_write_b32 a73, v25
v_accvgpr_write_b32 a74, v26
v_accvgpr_write_b32 a75, v27
v_mov_b32 v148, 0
v_mov_b32 v149, 0
v_mov_b32 v150, 0
v_mov_b32 v151, 0
s_add_u32 m0, s66, 4096
s_nop 0
buffer_load_dwordx4 v12, s[44:47], s65 offen offset:0 sc1 lds
buffer_load_dwordx4 v12, s[44:47], s65 offen offset:1024 sc1 lds
buffer_load_dwordx4 v12, s[44:47], s65 offen offset:2048 sc1 lds
buffer_load_dwordx4 v12, s[44:47], s65 offen offset:3072 sc1 lds
s_add_u32 s64, s64, 32768
s_add_u32 s65, s65, 32768
s_add_u32 s66, s66, 32768
s_and_b32 s66, s66, 0x1ffff
v_cvt_pk_bf16_f32 v28, v152, v153
v_cvt_pk_bf16_f32 v29, v154, v155
v_cvt_pk_bf16_f32 v30, v156, v157
v_cvt_pk_bf16_f32 v31, v158, v159
v_mov_b32 v152, 0
v_mov_b32 v153, 0
v_mov_b32 v154, 0
v_mov_b32 v155, 0
v_accvgpr_write_b32 a76, v28
v_accvgpr_write_b32 a77, v29
v_accvgpr_write_b32 a78, v30
v_accvgpr_write_b32 a79, v31
v_mov_b32 v156, 0
v_mov_b32 v157, 0
v_mov_b32 v158, 0
v_mov_b32 v159, 0
s_mov_b32 m0, s66
s_nop 0
buffer_load_dwordx4 v12, s[44:47], s64 offen offset:0 sc1 lds
buffer_load_dwordx4 v12, s[44:47], s64 offen offset:1024 sc1 lds
buffer_load_dwordx4 v12, s[44:47], s64 offen offset:2048 sc1 lds
buffer_load_dwordx4 v12, s[44:47], s64 offen offset:3072 sc1 lds
v_cvt_pk_bf16_f32 v16, v160, v161
v_cvt_pk_bf16_f32 v17, v162, v163
v_cvt_pk_bf16_f32 v18, v164, v165
v_cvt_pk_bf16_f32 v19, v166, v167
v_mov_b32 v160, 0
v_mov_b32 v161, 0
v_mov_b32 v162, 0
v_mov_b32 v163, 0
v_accvgpr_write_b32 a80, v16
v_accvgpr_write_b32 a81, v17
v_accvgpr_write_b32 a82, v18
v_accvgpr_write_b32 a83, v19
v_mov_b32 v164, 0
v_mov_b32 v165, 0
v_mov_b32 v166, 0
v_mov_b32 v167, 0
s_add_u32 m0, s66, 4096
s_nop 0
buffer_load_dwordx4 v12, s[44:47], s65 offen offset:0 sc1 lds
buffer_load_dwordx4 v12, s[44:47], s65 offen offset:1024 sc1 lds
buffer_load_dwordx4 v12, s[44:47], s65 offen offset:2048 sc1 lds
buffer_load_dwordx4 v12, s[44:47], s65 offen offset:3072 sc1 lds
s_add_u32 s64, s64, 32768
s_add_u32 s65, s65, 32768
s_add_u32 s66, s66, 32768
s_and_b32 s66, s66, 0x1ffff
v_cvt_pk_bf16_f32 v20, v168, v169
v_cvt_pk_bf16_f32 v21, v170, v171
v_cvt_pk_bf16_f32 v22, v172, v173
v_cvt_pk_bf16_f32 v23, v174, v175
v_mov_b32 v168, 0
v_mov_b32 v169, 0
v_mov_b32 v170, 0
v_mov_b32 v171, 0
v_accvgpr_write_b32 a84, v20
v_accvgpr_write_b32 a85, v21
v_accvgpr_write_b32 a86, v22
v_accvgpr_write_b32 a87, v23
v_mov_b32 v172, 0
v_mov_b32 v173, 0
v_mov_b32 v174, 0
v_mov_b32 v175, 0
s_mov_b32 m0, s66
s_nop 0
buffer_load_dwordx4 v12, s[44:47], s64 offen offset:0 sc1 lds
buffer_load_dwordx4 v12, s[44:47], s64 offen offset:1024 sc1 lds
buffer_load_dwordx4 v12, s[44:47], s64 offen offset:2048 sc1 lds
buffer_load_dwordx4 v12, s[44:47], s64 offen offset:3072 sc1 lds
v_cvt_pk_bf16_f32 v24, v176, v177
v_cvt_pk_bf16_f32 v25, v178, v179
)ASM"
R"ASM(v_cvt_pk_bf16_f32 v26, v180, v181
v_cvt_pk_bf16_f32 v27, v182, v183
v_mov_b32 v176, 0
v_mov_b32 v177, 0
v_mov_b32 v178, 0
v_mov_b32 v179, 0
v_accvgpr_write_b32 a88, v24
v_accvgpr_write_b32 a89, v25
v_accvgpr_write_b32 a90, v26
v_accvgpr_write_b32 a91, v27
v_mov_b32 v180, 0
v_mov_b32 v181, 0
v_mov_b32 v182, 0
v_mov_b32 v183, 0
v_cvt_pk_bf16_f32 v28, v184, v185
v_cvt_pk_bf16_f32 v29, v186, v187
v_cvt_pk_bf16_f32 v30, v188, v189
v_cvt_pk_bf16_f32 v31, v190, v191
v_mov_b32 v184, 0
v_mov_b32 v185, 0
v_mov_b32 v186, 0
v_mov_b32 v187, 0
v_accvgpr_write_b32 a92, v28
v_accvgpr_write_b32 a93, v29
v_accvgpr_write_b32 a94, v30
v_accvgpr_write_b32 a95, v31
v_mov_b32 v188, 0
v_mov_b32 v189, 0
v_mov_b32 v190, 0
v_mov_b32 v191, 0
v_cvt_pk_bf16_f32 v16, v192, v193
v_cvt_pk_bf16_f32 v17, v194, v195
v_cvt_pk_bf16_f32 v18, v196, v197
v_cvt_pk_bf16_f32 v19, v198, v199
v_mov_b32 v192, 0
v_mov_b32 v193, 0
v_mov_b32 v194, 0
v_mov_b32 v195, 0
v_accvgpr_write_b32 a96, v16
v_accvgpr_write_b32 a97, v17
v_accvgpr_write_b32 a98, v18
v_accvgpr_write_b32 a99, v19
v_mov_b32 v196, 0
v_mov_b32 v197, 0
v_mov_b32 v198, 0
v_mov_b32 v199, 0
v_cvt_pk_bf16_f32 v20, v200, v201
v_cvt_pk_bf16_f32 v21, v202, v203
v_cvt_pk_bf16_f32 v22, v204, v205
v_cvt_pk_bf16_f32 v23, v206, v207
v_mov_b32 v200, 0
v_mov_b32 v201, 0
v_mov_b32 v202, 0
v_mov_b32 v203, 0
v_accvgpr_write_b32 a100, v20
v_accvgpr_write_b32 a101, v21
v_accvgpr_write_b32 a102, v22
v_accvgpr_write_b32 a103, v23
v_mov_b32 v204, 0
v_mov_b32 v205, 0
v_mov_b32 v206, 0
v_mov_b32 v207, 0
v_cvt_pk_bf16_f32 v24, v208, v209
v_cvt_pk_bf16_f32 v25, v210, v211
v_cvt_pk_bf16_f32 v26, v212, v213
v_cvt_pk_bf16_f32 v27, v214, v215
v_mov_b32 v208, 0
v_mov_b32 v209, 0
v_mov_b32 v210, 0
v_mov_b32 v211, 0
v_accvgpr_write_b32 a104, v24
v_accvgpr_write_b32 a105, v25
v_accvgpr_write_b32 a106, v26
v_accvgpr_write_b32 a107, v27
v_mov_b32 v212, 0
v_mov_b32 v213, 0
v_mov_b32 v214, 0
v_mov_b32 v215, 0
v_cvt_pk_bf16_f32 v28, v216, v217
v_cvt_pk_bf16_f32 v29, v218, v219
v_cvt_pk_bf16_f32 v30, v220, v221
v_cvt_pk_bf16_f32 v31, v222, v223
v_mov_b32 v216, 0
v_mov_b32 v217, 0
v_mov_b32 v218, 0
v_mov_b32 v219, 0
v_accvgpr_write_b32 a108, v28
v_accvgpr_write_b32 a109, v29
v_accvgpr_write_b32 a110, v30
v_accvgpr_write_b32 a111, v31
v_mov_b32 v220, 0
v_mov_b32 v221, 0
v_mov_b32 v222, 0
v_mov_b32 v223, 0
v_cvt_pk_bf16_f32 v16, v224, v225
v_cvt_pk_bf16_f32 v17, v226, v227
v_cvt_pk_bf16_f32 v18, v228, v229
v_cvt_pk_bf16_f32 v19, v230, v231
v_mov_b32 v224, 0
v_mov_b32 v225, 0
v_mov_b32 v226, 0
v_mov_b32 v227, 0
v_accvgpr_write_b32 a112, v16
v_accvgpr_write_b32 a113, v17
v_accvgpr_write_b32 a114, v18
v_accvgpr_write_b32 a115, v19
v_mov_b32 v228, 0
v_mov_b32 v229, 0
v_mov_b32 v230, 0
v_mov_b32 v231, 0
v_cvt_pk_bf16_f32 v20, v232, v233
v_cvt_pk_bf16_f32 v21, v234, v235
v_cvt_pk_bf16_f32 v22, v236, v237
v_cvt_pk_bf16_f32 v23, v238, v239
v_mov_b32 v232, 0
v_mov_b32 v233, 0
v_mov_b32 v234, 0
v_mov_b32 v235, 0
v_accvgpr_write_b32 a116, v20
v_accvgpr_write_b32 a117, v21
v_accvgpr_write_b32 a118, v22
v_accvgpr_write_b32 a119, v23
v_mov_b32 v236, 0
v_mov_b32 v237, 0
v_mov_b32 v238, 0
v_mov_b32 v239, 0
v_cvt_pk_bf16_f32 v24, v240, v241
v_cvt_pk_bf16_f32 v25, v242, v243
v_cvt_pk_bf16_f32 v26, v244, v245
v_cvt_pk_bf16_f32 v27, v246, v247
v_mov_b32 v240, 0
v_mov_b32 v241, 0
v_mov_b32 v242, 0
v_mov_b32 v243, 0
v_accvgpr_write_b32 a120, v24
v_accvgpr_write_b32 a121, v25
v_accvgpr_write_b32 a122, v26
v_accvgpr_write_b32 a123, v27
v_mov_b32 v244, 0
v_mov_b32 v245, 0
v_mov_b32 v246, 0
v_mov_b32 v247, 0
v_cvt_pk_bf16_f32 v28, v248, v249
v_cvt_pk_bf16_f32 v29, v250, v251
v_cvt_pk_bf16_f32 v30, v252, v253
v_cvt_pk_bf16_f32 v31, v254, v255
v_mov_b32 v248, 0
v_mov_b32 v249, 0
v_mov_b32 v250, 0
v_mov_b32 v251, 0
v_accvgpr_write_b32 a124, v28
v_accvgpr_write_b32 a125, v29
v_accvgpr_write_b32 a126, v30
v_accvgpr_write_b32 a127, v31
v_mov_b32 v252, 0
v_mov_b32 v253, 0
v_mov_b32 v254, 0
v_mov_b32 v255, 0
s_lshl_b32 s62, s61, 2
s_add_u32 s62, s62, s39
s_lshl_b32 s62, s62, 5
s_sub_u32 s68, 0, s62
s_sub_u32 s62, 15, s61
s_lshl_b32 s62, s62, 2
s_add_u32 s62, s62, s39
s_lshl_b32 s62, s62, 5
s_lshl_b32 s63, s70, 6
s_sub_u32 s69, s63, s62
s_sub_i32 s68, s68, 32
s_sub_i32 s69, s69, 32
v_mov_b32 v16, 0x3f803f80
v_accvgpr_write_b32 a128, v16
v_accvgpr_write_b32 a129, v16
v_accvgpr_write_b32 a130, v16
v_accvgpr_write_b32 a131, v16
s_add_i32 s68, s68, 32
s_cmp_eq_i32 s68, 0
s_cselect_b32 s74, 1.0, 0
s_cmp_gt_i32 s68, 0
s_cselect_b32 s75, s72, 0
v_add_f32 v10, s75, v6
v_add_f32 v11, s75, v7
v_fma_f32 v80, s74, v112, v10
v_fma_f32 v81, s74, v113, v10
v_fma_f32 v82, s74, v114, v10
v_fma_f32 v83, s74, v115, v10
v_fma_f32 v84, s74, v116, v11
v_fma_f32 v85, s74, v117, v11
v_fma_f32 v86, s74, v118, v11
v_fma_f32 v87, s74, v119, v11
v_fma_f32 v88, s74, v120, v10
v_fma_f32 v89, s74, v121, v10
v_fma_f32 v90, s74, v122, v10
v_fma_f32 v91, s74, v123, v10
v_fma_f32 v92, s74, v124, v11
v_fma_f32 v93, s74, v125, v11
v_fma_f32 v94, s74, v126, v11
v_fma_f32 v95, s74, v127, v11
s_lshr_b32 s62, s38, 5
s_lshl_b32 s62, s62, 7
s_lshl_b32 s63, s39, 5
s_add_u32 s62, s62, s63
s_lshl_b32 s62, s62, 12
s_add_u32 s62, s62, 75497472
s_add_u32 s52, s30, s62
s_addc_u32 s53, s31, 0
s_and_b32 s53, s53, 0xffff
s_mov_b32 s54, 131072
s_mov_b32 s55, 0x20000
s_waitcnt vmcnt(12)
s_barrier
v_lshlrev_b32 v24, 3, v1
s_cmp_lt_u32 s39, 2
s_cselect_b32 s48, s18, s20
s_cselect_b32 s49, s19, s21
s_mov_b32 s50, 1835008
s_cselect_b32 s50, 4194304, s50
s_mov_b32 s62, 7168
s_cselect_b32 s62, 16384, s62
s_cmp_eq_u32 s39, 3
s_cselect_b32 s48, s22, s48
s_cselect_b32 s49, s23, s49
s_and_b32 s49, s49, 0xffff
s_mov_b32 s51, 0x20000
s_mul_i32 s62, s62, s38
s_cmp_eq_u32 s39, 1
s_cselect_b32 s63, 8192, 0
s_add_u32 s62, s62, s63
buffer_load_dword a132, v24, s[48:51], s62 offen
ds_read_b128 a[0:3], v2 offset:0
ds_read_b128 a[16:19], v2 offset:4096
ds_read_b128 a[4:7], v2 offset:1024
ds_read_b128 a[20:23], v2 offset:5120
ds_read_b128 a[8:11], v2 offset:2048
ds_read_b128 a[24:27], v2 offset:6144
ds_read_b128 a[12:15], v2 offset:3072
ds_read_b128 a[28:31], v2 offset:7168
s_waitcnt lgkmcnt(0)
L_pre%=:
v_mfma_f32_16x16x32_bf16 v[16:19], a[0:3], a[64:67], v[80:83]
v_mfma_f32_16x16x32_bf16 v[20:23], a[0:3], a[80:83], v[84:87]
v_mfma_f32_16x16x32_bf16 v[24:27], a[16:19], a[64:67], v[88:91]
v_mfma_f32_16x16x32_bf16 v[28:31], a[16:19], a[80:83], v[92:95]
v_mfma_f32_16x16x32_bf16 v[16:19], a[4:7], a[68:71], v[16:19]
v_mfma_f32_16x16x32_bf16 v[20:23], a[4:7], a[84:87], v[20:23]
v_mfma_f32_16x16x32_bf16 v[24:27], a[20:23], a[68:71], v[24:27]
v_mfma_f32_16x16x32_bf16 v[28:31], a[20:23], a[84:87], v[28:31]
v_mfma_f32_16x16x32_bf16 v[16:19], a[8:11], a[72:75], v[16:19]
v_mfma_f32_16x16x32_bf16 v[20:23], a[8:11], a[88:91], v[20:23]
v_mfma_f32_16x16x32_bf16 v[24:27], a[24:27], a[72:75], v[24:27]
v_mfma_f32_16x16x32_bf16 v[28:31], a[24:27], a[88:91], v[28:31]
v_mfma_f32_16x16x32_bf16 v[16:19], a[12:15], a[76:79], v[16:19]
v_mfma_f32_16x16x32_bf16 v[20:23], a[12:15], a[92:95], v[20:23]
v_mfma_f32_16x16x32_bf16 v[24:27], a[28:31], a[76:79], v[24:27]
v_mfma_f32_16x16x32_bf16 v[28:31], a[28:31], a[92:95], v[28:31]
s_nop 7
v_exp_f32 v16, v16
v_exp_f32 v17, v17
v_exp_f32 v18, v18
v_exp_f32 v19, v19
v_exp_f32 v20, v20
s_nop 1
s_add_i32 s68, s68, 32
s_cmp_eq_i32 s68, 0
s_cselect_b32 s74, 1.0, 0
s_cmp_gt_i32 s68, 0
s_cselect_b32 s75, s72, 0
v_add_f32 v10, s75, v6
v_add_f32 v11, s75, v7
v_fma_f32 v80, s74, v112, v10
v_fma_f32 v81, s74, v113, v10
v_fma_f32 v82, s74, v114, v10
v_fma_f32 v83, s74, v115, v10
v_fma_f32 v84, s74, v116, v11
v_fma_f32 v85, s74, v117, v11
v_fma_f32 v86, s74, v118, v11
v_fma_f32 v87, s74, v119, v11
v_fma_f32 v88, s74, v120, v10
v_fma_f32 v89, s74, v121, v10
v_fma_f32 v90, s74, v122, v10
v_fma_f32 v91, s74, v123, v10
v_fma_f32 v92, s74, v124, v11
v_fma_f32 v93, s74, v125, v11
v_fma_f32 v94, s74, v126, v11
v_fma_f32 v95, s74, v127, v11
s_nop 1
s_lshr_b32 s67, s70, 1
v_mfma_f32_16x16x32_bf16 v[32:35], a[0:3], a[96:99], v[96:99]
s_lshl_b32 s62, s39, 12
s_mov_b32 s63, 0
buffer_load_dwordx4 a[132:135], v1, s[52:55], s63 offen offset:0 sc1
v_exp_f32 v21, v21
v_mfma_f32_16x16x32_bf16 v[36:39], a[0:3], a[112:115], v[100:103]
ds_read_b128 a[0:3], v2 offset:8192
v_cvt_pk_bf16_f32 v48, v16, v17
v_mfma_f32_16x16x32_bf16 v[40:43], a[16:19], a[96:99], v[104:107]
v_exp_f32 v22, v22
v_mfma_f32_16x16x32_bf16 v[44:47], a[16:19], a[112:115], v[108:111]
ds_read_b128 a[16:19], v2 offset:12288
v_cvt_pk_bf16_f32 v49, v18, v19
)ASM"
R"ASM(v_mfma_f32_16x16x32_bf16 v[32:35], a[4:7], a[100:103], v[32:35]
v_exp_f32 v23, v23
v_mfma_f32_16x16x32_bf16 v[36:39], a[4:7], a[116:119], v[36:39]
ds_read_b128 a[4:7], v2 offset:9216
buffer_load_dwordx4 a[136:139], v1, s[52:55], s63 offen offset:1024 sc1
v_cvt_pk_bf16_f32 v52, v20, v21
v_mfma_f32_16x16x32_bf16 v[40:43], a[20:23], a[100:103], v[40:43]
v_exp_f32 v24, v24
v_mfma_f32_16x16x32_bf16 v[44:47], a[20:23], a[116:119], v[44:47]
ds_read_b128 a[20:23], v2 offset:13312
v_cvt_pk_bf16_f32 v53, v22, v23
v_mfma_f32_16x16x32_bf16 v[32:35], a[8:11], a[104:107], v[32:35]
v_exp_f32 v25, v25
v_mfma_f32_16x16x32_bf16 v[36:39], a[8:11], a[120:123], v[36:39]
ds_read_b128 a[8:11], v2 offset:10240
v_cvt_pk_bf16_f32 v50, v24, v25
v_mfma_f32_16x16x32_bf16 v[40:43], a[24:27], a[104:107], v[40:43]
s_mov_b32 s63, 45056
s_add_u32 m0, s62, 131072
v_exp_f32 v26, v26
v_mfma_f32_16x16x32_bf16 v[44:47], a[24:27], a[120:123], v[44:47]
ds_read_b128 a[24:27], v2 offset:14336
buffer_load_dwordx4 v1, s[52:55], s63 offen offset:0 sc1 lds
v_xor_b32 v3, 0x10000, v3
v_mfma_f32_16x16x32_bf16 v[32:35], a[12:15], a[108:111], v[32:35]
v_exp_f32 v27, v27
v_mfma_f32_16x16x32_bf16 v[36:39], a[12:15], a[124:127], v[36:39]
ds_read_b128 a[12:15], v2 offset:11264
v_cvt_pk_bf16_f32 v51, v26, v27
v_mfma_f32_16x16x32_bf16 v[40:43], a[28:31], a[108:111], v[40:43]
v_exp_f32 v28, v28
v_mfma_f32_16x16x32_bf16 v[44:47], a[28:31], a[124:127], v[44:47]
ds_read_b128 a[28:31], v2 offset:15360
v_mfma_f32_16x16x32_bf16 v[192:195], a[32:35], v[56:59], v[192:195]
v_exp_f32 v29, v29
v_mfma_f32_16x16x32_bf16 v[224:227], a[32:35], v[60:63], v[224:227]
ds_read_b128 a[32:35], v2 offset:16384
v_cvt_pk_bf16_f32 v54, v28, v29
v_mfma_f32_16x16x32_bf16 v[196:199], a[36:39], v[56:59], v[196:199]
v_exp_f32 v30, v30
v_mfma_f32_16x16x32_bf16 v[228:231], a[36:39], v[60:63], v[228:231]
ds_read_b128 a[36:39], v2 offset:17408
v_mfma_f32_16x16x32_bf16 v[200:203], a[40:43], v[56:59], v[200:203]
v_exp_f32 v31, v31
v_mfma_f32_16x16x32_bf16 v[232:235], a[40:43], v[60:63], v[232:235]
ds_read_b128 a[40:43], v2 offset:18432
v_cvt_pk_bf16_f32 v55, v30, v31
v_mfma_f32_16x16x32_bf16 v[204:207], a[44:47], v[56:59], v[204:207]
buffer_load_dwordx4 v1, s[52:55], s63 offen offset:1024 sc1 lds
v_mfma_f32_16x16x32_bf16 v[236:239], a[44:47], v[60:63], v[236:239]
ds_read_b128 a[44:47], v2 offset:19456
buffer_load_dwordx4 v1, s[52:55], s63 offen offset:2048 sc1 lds
v_mfma_f32_16x16x32_bf16 v[208:211], a[48:51], v[56:59], v[208:211]
s_add_i32 s68, s68, 32
v_exp_f32 v32, v32
v_mfma_f32_16x16x32_bf16 v[240:243], a[48:51], v[60:63], v[240:243]
ds_read_b128 a[48:51], v2 offset:20480
v_mfma_f32_16x16x32_bf16 v[212:215], a[52:55], v[56:59], v[212:215]
s_cmp_eq_i32 s68, 0
v_exp_f32 v33, v33
v_mfma_f32_16x16x32_bf16 v[244:247], a[52:55], v[60:63], v[244:247]
ds_read_b128 a[52:55], v2 offset:21504
v_mfma_f32_16x16x32_bf16 v[216:219], a[56:59], v[56:59], v[216:219]
s_cselect_b32 s76, 1.0, 0
v_exp_f32 v34, v34
v_mfma_f32_16x16x32_bf16 v[248:251], a[56:59], v[60:63], v[248:251]
ds_read_b128 a[56:59], v2 offset:22528
v_mfma_f32_16x16x32_bf16 v[220:223], a[60:63], v[56:59], v[220:223]
s_cmp_gt_i32 s68, 0
v_exp_f32 v35, v35
v_mfma_f32_16x16x32_bf16 v[252:255], a[60:63], v[60:63], v[252:255]
ds_read_b128 a[60:63], v2 offset:23552
v_mfma_f32_16x16x32_bf16 v[72:75], a[128:131], v[56:59], v[72:75]
s_cselect_b32 s77, s72, 0
v_exp_f32 v36, v36
v_mfma_f32_16x16x32_bf16 v[76:79], a[128:131], v[60:63], v[76:79]
s_waitcnt lgkmcnt(2)
buffer_load_dwordx4 v1, s[52:55], s63 offen offset:3072 sc1 lds
v_mfma_f32_16x16x32_bf16 v[16:19], a[0:3], a[64:67], v[80:83]
v_exp_f32 v37, v37
v_mfma_f32_16x16x32_bf16 v[20:23], a[0:3], a[80:83], v[84:87]
s_mov_b32 s63, 0
buffer_load_dwordx4 a[140:143], v1, s[52:55], s63 offen offset:2048 sc1
v_cvt_pk_bf16_f32 v56, v32, v33
v_mfma_f32_16x16x32_bf16 v[24:27], a[16:19], a[64:67], v[88:91]
v_exp_f32 v38, v38
v_mfma_f32_16x16x32_bf16 v[28:31], a[16:19], a[80:83], v[92:95]
v_cvt_pk_bf16_f32 v57, v34, v35
v_mfma_f32_16x16x32_bf16 v[16:19], a[4:7], a[68:71], v[16:19]
v_exp_f32 v39, v39
v_mfma_f32_16x16x32_bf16 v[20:23], a[4:7], a[84:87], v[20:23]
buffer_load_dwordx4 a[144:147], v1, s[52:55], s63 offen offset:3072 sc1
v_cvt_pk_bf16_f32 v60, v36, v37
v_add_f32 v10, s77, v6
v_mfma_f32_16x16x32_bf16 v[24:27], a[20:23], a[68:71], v[24:27]
v_exp_f32 v40, v40
v_mfma_f32_16x16x32_bf16 v[28:31], a[20:23], a[84:87], v[28:31]
v_cvt_pk_bf16_f32 v61, v38, v39
v_add_f32 v11, s77, v7
v_mfma_f32_16x16x32_bf16 v[16:19], a[8:11], a[72:75], v[16:19]
v_exp_f32 v41, v41
v_mfma_f32_16x16x32_bf16 v[20:23], a[8:11], a[88:91], v[20:23]
v_cvt_pk_bf16_f32 v58, v40, v41
v_fma_f32 v80, s76, v112, v10
v_mfma_f32_16x16x32_bf16 v[24:27], a[24:27], a[72:75], v[24:27]
s_add_u32 m0, s66, 4096
v_exp_f32 v42, v42
v_mfma_f32_16x16x32_bf16 v[28:31], a[24:27], a[88:91], v[28:31]
s_mov_b32 s63, 4096
buffer_load_dwordx4 v12, s[44:47], s65 offen offset:0 sc1 lds
buffer_load_dwordx4 a[148:151], v1, s[52:55], s63 offen offset:0 sc1
v_mfma_f32_16x16x32_bf16 v[16:19], a[12:15], a[76:79], v[16:19]
v_exp_f32 v43, v43
v_mfma_f32_16x16x32_bf16 v[20:23], a[12:15], a[92:95], v[20:23]
v_cvt_pk_bf16_f32 v59, v42, v43
v_fma_f32 v81, s76, v113, v10
v_mfma_f32_16x16x32_bf16 v[24:27], a[28:31], a[76:79], v[24:27]
v_exp_f32 v44, v44
v_mfma_f32_16x16x32_bf16 v[28:31], a[28:31], a[92:95], v[28:31]
v_fma_f32 v82, s76, v114, v10
v_fma_f32 v83, s76, v115, v10
v_mfma_f32_16x16x32_bf16 v[128:131], a[32:35], v[48:51], v[128:131]
v_exp_f32 v45, v45
v_mfma_f32_16x16x32_bf16 v[160:163], a[32:35], v[52:55], v[160:163]
buffer_load_dwordx4 a[152:155], v1, s[52:55], s63 offen offset:1024 sc1
v_cvt_pk_bf16_f32 v62, v44, v45
v_fma_f32 v84, s76, v116, v11
v_mfma_f32_16x16x32_bf16 v[132:135], a[36:39], v[48:51], v[132:135]
v_exp_f32 v46, v46
v_mfma_f32_16x16x32_bf16 v[164:167], a[36:39], v[52:55], v[164:167]
buffer_load_dwordx4 v12, s[44:47], s65 offen offset:1024 sc1 lds
v_mfma_f32_16x16x32_bf16 v[136:139], a[40:43], v[48:51], v[136:139]
v_exp_f32 v47, v47
v_mfma_f32_16x16x32_bf16 v[168:171], a[40:43], v[52:55], v[168:171]
v_cvt_pk_bf16_f32 v63, v46, v47
v_fma_f32 v85, s76, v117, v11
v_mfma_f32_16x16x32_bf16 v[140:143], a[44:47], v[48:51], v[140:143]
buffer_load_dwordx4 a[156:159], v1, s[52:55], s63 offen offset:2048 sc1
v_fma_f32 v86, s76, v118, v11
v_fma_f32 v87, s76, v119, v11
v_mfma_f32_16x16x32_bf16 v[172:175], a[44:47], v[52:55], v[172:175]
v_fma_f32 v88, s76, v120, v10
v_fma_f32 v89, s76, v121, v10
v_mfma_f32_16x16x32_bf16 v[144:147], a[48:51], v[48:51], v[144:147]
v_exp_f32 v16, v16
v_mfma_f32_16x16x32_bf16 v[176:179], a[48:51], v[52:55], v[176:179]
buffer_load_dwordx4 v12, s[44:47], s65 offen offset:2048 sc1 lds
v_mfma_f32_16x16x32_bf16 v[148:151], a[52:55], v[48:51], v[148:151]
v_exp_f32 v17, v17
v_mfma_f32_16x16x32_bf16 v[180:183], a[52:55], v[52:55], v[180:183]
s_waitcnt lgkmcnt(0)
v_fma_f32 v90, s76, v122, v10
v_fma_f32 v91, s76, v123, v10
v_mfma_f32_16x16x32_bf16 v[152:155], a[56:59], v[48:51], v[152:155]
buffer_load_dwordx4 a[160:163], v1, s[52:55], s63 offen offset:3072 sc1
v_exp_f32 v18, v18
v_mfma_f32_16x16x32_bf16 v[184:187], a[56:59], v[52:55], v[184:187]
v_fma_f32 v92, s76, v124, v11
v_fma_f32 v93, s76, v125, v11
v_mfma_f32_16x16x32_bf16 v[156:159], a[60:63], v[48:51], v[156:159]
v_exp_f32 v19, v19
v_mfma_f32_16x16x32_bf16 v[188:191], a[60:63], v[52:55], v[188:191]
buffer_load_dwordx4 v12, s[44:47], s65 offen offset:3072 sc1 lds
v_mfma_f32_16x16x32_bf16 v[64:67], a[128:131], v[48:51], v[64:67]
v_exp_f32 v20, v20
v_mfma_f32_16x16x32_bf16 v[68:71], a[128:131], v[52:55], v[68:71]
v_fma_f32 v94, s76, v126, v11
v_fma_f32 v95, s76, v127, v11
v_mfma_f32_16x16x32_bf16 v[32:35], a[0:3], a[96:99], v[96:99]
s_mov_b32 s63, 49152
s_add_u32 m0, s62, 147456
v_exp_f32 v21, v21
v_mfma_f32_16x16x32_bf16 v[36:39], a[0:3], a[112:115], v[100:103]
ds_read_b128 a[0:3], v3 offset:0
buffer_load_dwordx4 v1, s[52:55], s63 offen offset:0 sc1 lds
v_cvt_pk_bf16_f32 v48, v16, v17
v_mfma_f32_16x16x32_bf16 v[40:43], a[16:19], a[96:99], v[104:107]
s_add_u32 s64, s64, 32768
v_exp_f32 v22, v22
v_mfma_f32_16x16x32_bf16 v[44:47], a[16:19], a[112:115], v[108:111]
ds_read_b128 a[16:19], v3 offset:4096
v_cvt_pk_bf16_f32 v49, v18, v19
v_mfma_f32_16x16x32_bf16 v[32:35], a[4:7], a[100:103], v[32:35]
s_add_u32 s65, s65, 32768
v_exp_f32 v23, v23
v_mfma_f32_16x16x32_bf16 v[36:39], a[4:7], a[116:119], v[36:39]
ds_read_b128 a[4:7], v3 offset:1024
v_cvt_pk_bf16_f32 v52, v20, v21
v_mfma_f32_16x16x32_bf16 v[40:43], a[20:23], a[100:103], v[40:43]
s_add_u32 s66, s66, 32768
v_exp_f32 v24, v24
v_mfma_f32_16x16x32_bf16 v[44:47], a[20:23], a[116:119], v[44:47]
ds_read_b128 a[20:23], v3 offset:5120
v_cvt_pk_bf16_f32 v53, v22, v23
v_mfma_f32_16x16x32_bf16 v[32:35], a[8:11], a[104:107], v[32:35]
s_and_b32 s66, s66, 0x1ffff
v_exp_f32 v25, v25
v_mfma_f32_16x16x32_bf16 v[36:39], a[8:11], a[120:123], v[36:39]
ds_read_b128 a[8:11], v3 offset:2048
v_cvt_pk_bf16_f32 v50, v24, v25
v_mfma_f32_16x16x32_bf16 v[40:43], a[24:27], a[104:107], v[40:43]
v_exp_f32 v26, v26
v_mfma_f32_16x16x32_bf16 v[44:47], a[24:27], a[120:123], v[44:47]
ds_read_b128 a[24:27], v3 offset:6144
buffer_load_dwordx4 v1, s[52:55], s63 offen offset:1024 sc1 lds
v_mfma_f32_16x16x32_bf16 v[32:35], a[12:15], a[108:111], v[32:35]
v_exp_f32 v27, v27
v_mfma_f32_16x16x32_bf16 v[36:39], a[12:15], a[124:127], v[36:39]
ds_read_b128 a[12:15], v3 offset:3072
v_cvt_pk_bf16_f32 v51, v26, v27
v_mfma_f32_16x16x32_bf16 v[40:43], a[28:31], a[108:111], v[40:43]
v_exp_f32 v28, v28
v_mfma_f32_16x16x32_bf16 v[44:47], a[28:31], a[124:127], v[44:47]
ds_read_b128 a[28:31], v3 offset:7168
v_mfma_f32_16x16x32_bf16 v[192:195], a[32:35], v[56:59], v[192:195]
v_exp_f32 v29, v29
v_mfma_f32_16x16x32_bf16 v[224:227], a[32:35], v[60:63], v[224:227]
ds_read_b128 a[32:35], v2 offset:24576
v_cvt_pk_bf16_f32 v54, v28, v29
v_mfma_f32_16x16x32_bf16 v[196:199], a[36:39], v[56:59], v[196:199]
v_exp_f32 v30, v30
v_mfma_f32_16x16x32_bf16 v[228:231], a[36:39], v[60:63], v[228:231]
ds_read_b128 a[36:39], v2 offset:25600
v_mfma_f32_16x16x32_bf16 v[200:203], a[40:43], v[56:59], v[200:203]
v_exp_f32 v31, v31
v_mfma_f32_16x16x32_bf16 v[232:235], a[40:43], v[60:63], v[232:235]
ds_read_b128 a[40:43], v2 offset:26624
v_cvt_pk_bf16_f32 v55, v30, v31
v_mfma_f32_16x16x32_bf16 v[204:207], a[44:47], v[56:59], v[204:207]
buffer_load_dwordx4 v1, s[52:55], s63 offen offset:2048 sc1 lds
v_mfma_f32_16x16x32_bf16 v[236:239], a[44:47], v[60:63], v[236:239]
ds_read_b128 a[44:47], v2 offset:27648
v_mfma_f32_16x16x32_bf16 v[208:211], a[48:51], v[56:59], v[208:211]
s_add_i32 s68, s68, 32
v_exp_f32 v32, v32
v_mfma_f32_16x16x32_bf16 v[240:243], a[48:51], v[60:63], v[240:243]
ds_read_b128 a[48:51], v2 offset:28672
buffer_load_dwordx4 v1, s[52:55], s63 offen offset:3072 sc1 lds
v_mfma_f32_16x16x32_bf16 v[212:215], a[52:55], v[56:59], v[212:215]
s_cmp_eq_i32 s68, 0
v_exp_f32 v33, v33
v_mfma_f32_16x16x32_bf16 v[244:247], a[52:55], v[60:63], v[244:247]
ds_read_b128 a[52:55], v2 offset:29696
v_mfma_f32_16x16x32_bf16 v[216:219], a[56:59], v[56:59], v[216:219]
s_cselect_b32 s76, 1.0, 0
v_exp_f32 v34, v34
v_mfma_f32_16x16x32_bf16 v[248:251], a[56:59], v[60:63], v[248:251]
ds_read_b128 a[56:59], v2 offset:30720
v_mfma_f32_16x16x32_bf16 v[220:223], a[60:63], v[56:59], v[220:223]
s_cmp_gt_i32 s68, 0
v_exp_f32 v35, v35
v_mfma_f32_16x16x32_bf16 v[252:255], a[60:63], v[60:63], v[252:255]
ds_read_b128 a[60:63], v2 offset:31744
v_mfma_f32_16x16x32_bf16 v[72:75], a[128:131], v[56:59], v[72:75]
s_cselect_b32 s77, s72, 0
v_exp_f32 v36, v36
v_mfma_f32_16x16x32_bf16 v[76:79], a[128:131], v[60:63], v[76:79]
s_waitcnt lgkmcnt(2)
v_mfma_f32_16x16x32_bf16 v[16:19], a[0:3], a[64:67], v[80:83]
v_exp_f32 v37, v37
v_mfma_f32_16x16x32_bf16 v[20:23], a[0:3], a[80:83], v[84:87]
v_cvt_pk_bf16_f32 v56, v32, v33
v_cvt_pk_bf16_f32 v57, v34, v35
v_mfma_f32_16x16x32_bf16 v[24:27], a[16:19], a[64:67], v[88:91]
v_exp_f32 v38, v38
v_mfma_f32_16x16x32_bf16 v[28:31], a[16:19], a[80:83], v[92:95]
s_waitcnt vmcnt(24) lgkmcnt(0)
s_barrier
v_mfma_f32_16x16x32_bf16 v[16:19], a[4:7], a[68:71], v[16:19]
v_exp_f32 v39, v39
v_mfma_f32_16x16x32_bf16 v[20:23], a[4:7], a[84:87], v[20:23]
v_cvt_pk_bf16_f32 v60, v36, v37
v_add_f32 v10, s77, v6
v_mfma_f32_16x16x32_bf16 v[24:27], a[20:23], a[68:71], v[24:27]
v_exp_f32 v40, v40
v_mfma_f32_16x16x32_bf16 v[28:31], a[20:23], a[84:87], v[28:31]
v_cvt_pk_bf16_f32 v61, v38, v39
v_add_f32 v11, s77, v7
v_mfma_f32_16x16x32_bf16 v[16:19], a[8:11], a[72:75], v[16:19]
v_exp_f32 v41, v41
v_mfma_f32_16x16x32_bf16 v[20:23], a[8:11], a[88:91], v[20:23]
v_cvt_pk_bf16_f32 v58, v40, v41
v_fma_f32 v80, s76, v112, v10
v_mfma_f32_16x16x32_bf16 v[24:27], a[24:27], a[72:75], v[24:27]
s_mov_b32 m0, s66
v_exp_f32 v42, v42
v_mfma_f32_16x16x32_bf16 v[28:31], a[24:27], a[88:91], v[28:31]
buffer_load_dwordx4 v12, s[44:47], s64 offen offset:0 sc1 lds
v_mfma_f32_16x16x32_bf16 v[16:19], a[12:15], a[76:79], v[16:19]
v_exp_f32 v43, v43
v_mfma_f32_16x16x32_bf16 v[20:23], a[12:15], a[92:95], v[20:23]
v_cvt_pk_bf16_f32 v59, v42, v43
v_fma_f32 v81, s76, v113, v10
v_mfma_f32_16x16x32_bf16 v[24:27], a[28:31], a[76:79], v[24:27]
v_exp_f32 v44, v44
v_mfma_f32_16x16x32_bf16 v[28:31], a[28:31], a[92:95], v[28:31]
v_fma_f32 v82, s76, v114, v10
v_fma_f32 v83, s76, v115, v10
v_mfma_f32_16x16x32_bf16 v[128:131], a[32:35], v[48:51], v[128:131]
v_exp_f32 v45, v45
)ASM"
R"ASM(v_mfma_f32_16x16x32_bf16 v[160:163], a[32:35], v[52:55], v[160:163]
v_cvt_pk_bf16_f32 v62, v44, v45
v_fma_f32 v84, s76, v116, v11
v_mfma_f32_16x16x32_bf16 v[132:135], a[36:39], v[48:51], v[132:135]
v_exp_f32 v46, v46
v_mfma_f32_16x16x32_bf16 v[164:167], a[36:39], v[52:55], v[164:167]
buffer_load_dwordx4 v12, s[44:47], s64 offen offset:1024 sc1 lds
v_mfma_f32_16x16x32_bf16 v[136:139], a[40:43], v[48:51], v[136:139]
v_exp_f32 v47, v47
v_mfma_f32_16x16x32_bf16 v[168:171], a[40:43], v[52:55], v[168:171]
v_cvt_pk_bf16_f32 v63, v46, v47
v_fma_f32 v85, s76, v117, v11
v_mfma_f32_16x16x32_bf16 v[140:143], a[44:47], v[48:51], v[140:143]
v_fma_f32 v86, s76, v118, v11
v_fma_f32 v87, s76, v119, v11
v_mfma_f32_16x16x32_bf16 v[172:175], a[44:47], v[52:55], v[172:175]
v_fma_f32 v88, s76, v120, v10
v_fma_f32 v89, s76, v121, v10
v_mfma_f32_16x16x32_bf16 v[144:147], a[48:51], v[48:51], v[144:147]
v_exp_f32 v16, v16
v_mfma_f32_16x16x32_bf16 v[176:179], a[48:51], v[52:55], v[176:179]
buffer_load_dwordx4 v12, s[44:47], s64 offen offset:2048 sc1 lds
v_mfma_f32_16x16x32_bf16 v[148:151], a[52:55], v[48:51], v[148:151]
v_exp_f32 v17, v17
v_mfma_f32_16x16x32_bf16 v[180:183], a[52:55], v[52:55], v[180:183]
v_fma_f32 v90, s76, v122, v10
v_fma_f32 v91, s76, v123, v10
v_mfma_f32_16x16x32_bf16 v[152:155], a[56:59], v[48:51], v[152:155]
v_exp_f32 v18, v18
v_mfma_f32_16x16x32_bf16 v[184:187], a[56:59], v[52:55], v[184:187]
v_fma_f32 v92, s76, v124, v11
v_fma_f32 v93, s76, v125, v11
v_mfma_f32_16x16x32_bf16 v[156:159], a[60:63], v[48:51], v[156:159]
v_exp_f32 v19, v19
v_mfma_f32_16x16x32_bf16 v[188:191], a[60:63], v[52:55], v[188:191]
buffer_load_dwordx4 v12, s[44:47], s64 offen offset:3072 sc1 lds
v_mfma_f32_16x16x32_bf16 v[64:67], a[128:131], v[48:51], v[64:67]
v_exp_f32 v20, v20
v_mfma_f32_16x16x32_bf16 v[68:71], a[128:131], v[52:55], v[68:71]
v_fma_f32 v94, s76, v126, v11
v_fma_f32 v95, s76, v127, v11
s_sub_u32 s67, s67, 1
s_cmp_eq_u32 s67, 0
s_cbranch_scc1 L_L%=
L_D%=:
v_mfma_f32_16x16x32_bf16 v[32:35], a[0:3], a[96:99], v[96:99]
v_exp_f32 v21, v21
v_mfma_f32_16x16x32_bf16 v[36:39], a[0:3], a[112:115], v[100:103]
ds_read_b128 a[0:3], v3 offset:8192
v_cvt_pk_bf16_f32 v48, v16, v17
v_mfma_f32_16x16x32_bf16 v[40:43], a[16:19], a[96:99], v[104:107]
v_exp_f32 v22, v22
v_mfma_f32_16x16x32_bf16 v[44:47], a[16:19], a[112:115], v[108:111]
ds_read_b128 a[16:19], v3 offset:12288
v_cvt_pk_bf16_f32 v49, v18, v19
v_mfma_f32_16x16x32_bf16 v[32:35], a[4:7], a[100:103], v[32:35]
v_exp_f32 v23, v23
v_mfma_f32_16x16x32_bf16 v[36:39], a[4:7], a[116:119], v[36:39]
ds_read_b128 a[4:7], v3 offset:9216
v_cvt_pk_bf16_f32 v52, v20, v21
v_mfma_f32_16x16x32_bf16 v[40:43], a[20:23], a[100:103], v[40:43]
v_exp_f32 v24, v24
v_mfma_f32_16x16x32_bf16 v[44:47], a[20:23], a[116:119], v[44:47]
ds_read_b128 a[20:23], v3 offset:13312
v_cvt_pk_bf16_f32 v53, v22, v23
v_mfma_f32_16x16x32_bf16 v[32:35], a[8:11], a[104:107], v[32:35]
v_exp_f32 v25, v25
v_mfma_f32_16x16x32_bf16 v[36:39], a[8:11], a[120:123], v[36:39]
ds_read_b128 a[8:11], v3 offset:10240
v_cvt_pk_bf16_f32 v50, v24, v25
v_mfma_f32_16x16x32_bf16 v[40:43], a[24:27], a[104:107], v[40:43]
v_exp_f32 v26, v26
v_mfma_f32_16x16x32_bf16 v[44:47], a[24:27], a[120:123], v[44:47]
ds_read_b128 a[24:27], v3 offset:14336
v_xor_b32 v2, 0x10000, v2
v_mfma_f32_16x16x32_bf16 v[32:35], a[12:15], a[108:111], v[32:35]
v_exp_f32 v27, v27
v_mfma_f32_16x16x32_bf16 v[36:39], a[12:15], a[124:127], v[36:39]
ds_read_b128 a[12:15], v3 offset:11264
v_cvt_pk_bf16_f32 v51, v26, v27
v_mfma_f32_16x16x32_bf16 v[40:43], a[28:31], a[108:111], v[40:43]
v_exp_f32 v28, v28
v_mfma_f32_16x16x32_bf16 v[44:47], a[28:31], a[124:127], v[44:47]
ds_read_b128 a[28:31], v3 offset:15360
v_mfma_f32_16x16x32_bf16 v[192:195], a[32:35], v[56:59], v[192:195]
v_exp_f32 v29, v29
v_mfma_f32_16x16x32_bf16 v[224:227], a[32:35], v[60:63], v[224:227]
ds_read_b128 a[32:35], v3 offset:16384
v_cvt_pk_bf16_f32 v54, v28, v29
v_mfma_f32_16x16x32_bf16 v[196:199], a[36:39], v[56:59], v[196:199]
v_exp_f32 v30, v30
v_mfma_f32_16x16x32_bf16 v[228:231], a[36:39], v[60:63], v[228:231]
ds_read_b128 a[36:39], v3 offset:17408
v_mfma_f32_16x16x32_bf16 v[200:203], a[40:43], v[56:59], v[200:203]
v_exp_f32 v31, v31
v_mfma_f32_16x16x32_bf16 v[232:235], a[40:43], v[60:63], v[232:235]
ds_read_b128 a[40:43], v3 offset:18432
v_cvt_pk_bf16_f32 v55, v30, v31
v_mfma_f32_16x16x32_bf16 v[204:207], a[44:47], v[56:59], v[204:207]
v_mfma_f32_16x16x32_bf16 v[236:239], a[44:47], v[60:63], v[236:239]
ds_read_b128 a[44:47], v3 offset:19456
v_mfma_f32_16x16x32_bf16 v[208:211], a[48:51], v[56:59], v[208:211]
s_add_i32 s68, s68, 32
v_exp_f32 v32, v32
v_mfma_f32_16x16x32_bf16 v[240:243], a[48:51], v[60:63], v[240:243]
ds_read_b128 a[48:51], v3 offset:20480
v_mfma_f32_16x16x32_bf16 v[212:215], a[52:55], v[56:59], v[212:215]
s_cmp_eq_i32 s68, 0
v_exp_f32 v33, v33
v_mfma_f32_16x16x32_bf16 v[244:247], a[52:55], v[60:63], v[244:247]
ds_read_b128 a[52:55], v3 offset:21504
v_mfma_f32_16x16x32_bf16 v[216:219], a[56:59], v[56:59], v[216:219]
s_cselect_b32 s76, 1.0, 0
v_exp_f32 v34, v34
v_mfma_f32_16x16x32_bf16 v[248:251], a[56:59], v[60:63], v[248:251]
ds_read_b128 a[56:59], v3 offset:22528
v_mfma_f32_16x16x32_bf16 v[220:223], a[60:63], v[56:59], v[220:223]
s_cmp_gt_i32 s68, 0
v_exp_f32 v35, v35
v_mfma_f32_16x16x32_bf16 v[252:255], a[60:63], v[60:63], v[252:255]
ds_read_b128 a[60:63], v3 offset:23552
v_mfma_f32_16x16x32_bf16 v[72:75], a[128:131], v[56:59], v[72:75]
s_cselect_b32 s77, s72, 0
v_exp_f32 v36, v36
v_mfma_f32_16x16x32_bf16 v[76:79], a[128:131], v[60:63], v[76:79]
s_waitcnt lgkmcnt(2)
v_mfma_f32_16x16x32_bf16 v[16:19], a[0:3], a[64:67], v[80:83]
v_exp_f32 v37, v37
v_mfma_f32_16x16x32_bf16 v[20:23], a[0:3], a[80:83], v[84:87]
v_cvt_pk_bf16_f32 v56, v32, v33
v_mfma_f32_16x16x32_bf16 v[24:27], a[16:19], a[64:67], v[88:91]
v_exp_f32 v38, v38
v_mfma_f32_16x16x32_bf16 v[28:31], a[16:19], a[80:83], v[92:95]
v_cvt_pk_bf16_f32 v57, v34, v35
v_mfma_f32_16x16x32_bf16 v[16:19], a[4:7], a[68:71], v[16:19]
v_exp_f32 v39, v39
v_mfma_f32_16x16x32_bf16 v[20:23], a[4:7], a[84:87], v[20:23]
v_cvt_pk_bf16_f32 v60, v36, v37
v_add_f32 v10, s77, v6
v_mfma_f32_16x16x32_bf16 v[24:27], a[20:23], a[68:71], v[24:27]
v_exp_f32 v40, v40
v_mfma_f32_16x16x32_bf16 v[28:31], a[20:23], a[84:87], v[28:31]
v_cvt_pk_bf16_f32 v61, v38, v39
v_add_f32 v11, s77, v7
v_mfma_f32_16x16x32_bf16 v[16:19], a[8:11], a[72:75], v[16:19]
v_exp_f32 v41, v41
v_mfma_f32_16x16x32_bf16 v[20:23], a[8:11], a[88:91], v[20:23]
v_cvt_pk_bf16_f32 v58, v40, v41
v_fma_f32 v80, s76, v112, v10
v_mfma_f32_16x16x32_bf16 v[24:27], a[24:27], a[72:75], v[24:27]
s_add_u32 m0, s66, 4096
v_exp_f32 v42, v42
v_mfma_f32_16x16x32_bf16 v[28:31], a[24:27], a[88:91], v[28:31]
buffer_load_dwordx4 v12, s[44:47], s65 offen offset:0 sc1 lds
v_mfma_f32_16x16x32_bf16 v[16:19], a[12:15], a[76:79], v[16:19]
v_exp_f32 v43, v43
v_mfma_f32_16x16x32_bf16 v[20:23], a[12:15], a[92:95], v[20:23]
v_cvt_pk_bf16_f32 v59, v42, v43
v_fma_f32 v81, s76, v113, v10
v_mfma_f32_16x16x32_bf16 v[24:27], a[28:31], a[76:79], v[24:27]
v_exp_f32 v44, v44
v_mfma_f32_16x16x32_bf16 v[28:31], a[28:31], a[92:95], v[28:31]
v_fma_f32 v82, s76, v114, v10
v_fma_f32 v83, s76, v115, v10
v_mfma_f32_16x16x32_bf16 v[128:131], a[32:35], v[48:51], v[128:131]
v_exp_f32 v45, v45
v_mfma_f32_16x16x32_bf16 v[160:163], a[32:35], v[52:55], v[160:163]
v_cvt_pk_bf16_f32 v62, v44, v45
v_fma_f32 v84, s76, v116, v11
v_mfma_f32_16x16x32_bf16 v[132:135], a[36:39], v[48:51], v[132:135]
v_exp_f32 v46, v46
v_mfma_f32_16x16x32_bf16 v[164:167], a[36:39], v[52:55], v[164:167]
buffer_load_dwordx4 v12, s[44:47], s65 offen offset:1024 sc1 lds
v_mfma_f32_16x16x32_bf16 v[136:139], a[40:43], v[48:51], v[136:139]
v_exp_f32 v47, v47
v_mfma_f32_16x16x32_bf16 v[168:171], a[40:43], v[52:55], v[168:171]
v_cvt_pk_bf16_f32 v63, v46, v47
v_fma_f32 v85, s76, v117, v11
v_mfma_f32_16x16x32_bf16 v[140:143], a[44:47], v[48:51], v[140:143]
v_fma_f32 v86, s76, v118, v11
v_fma_f32 v87, s76, v119, v11
v_mfma_f32_16x16x32_bf16 v[172:175], a[44:47], v[52:55], v[172:175]
v_fma_f32 v88, s76, v120, v10
v_fma_f32 v89, s76, v121, v10
v_mfma_f32_16x16x32_bf16 v[144:147], a[48:51], v[48:51], v[144:147]
v_exp_f32 v16, v16
v_mfma_f32_16x16x32_bf16 v[176:179], a[48:51], v[52:55], v[176:179]
buffer_load_dwordx4 v12, s[44:47], s65 offen offset:2048 sc1 lds
v_mfma_f32_16x16x32_bf16 v[148:151], a[52:55], v[48:51], v[148:151]
v_exp_f32 v17, v17
v_mfma_f32_16x16x32_bf16 v[180:183], a[52:55], v[52:55], v[180:183]
s_waitcnt lgkmcnt(0)
v_fma_f32 v90, s76, v122, v10
v_fma_f32 v91, s76, v123, v10
v_mfma_f32_16x16x32_bf16 v[152:155], a[56:59], v[48:51], v[152:155]
v_exp_f32 v18, v18
v_mfma_f32_16x16x32_bf16 v[184:187], a[56:59], v[52:55], v[184:187]
v_fma_f32 v92, s76, v124, v11
v_fma_f32 v93, s76, v125, v11
v_mfma_f32_16x16x32_bf16 v[156:159], a[60:63], v[48:51], v[156:159]
v_exp_f32 v19, v19
v_mfma_f32_16x16x32_bf16 v[188:191], a[60:63], v[52:55], v[188:191]
buffer_load_dwordx4 v12, s[44:47], s65 offen offset:3072 sc1 lds
v_mfma_f32_16x16x32_bf16 v[64:67], a[128:131], v[48:51], v[64:67]
v_exp_f32 v20, v20
v_mfma_f32_16x16x32_bf16 v[68:71], a[128:131], v[52:55], v[68:71]
v_fma_f32 v94, s76, v126, v11
v_fma_f32 v95, s76, v127, v11
v_mfma_f32_16x16x32_bf16 v[32:35], a[0:3], a[96:99], v[96:99]
v_exp_f32 v21, v21
v_mfma_f32_16x16x32_bf16 v[36:39], a[0:3], a[112:115], v[100:103]
ds_read_b128 a[0:3], v2 offset:0
v_cvt_pk_bf16_f32 v48, v16, v17
v_mfma_f32_16x16x32_bf16 v[40:43], a[16:19], a[96:99], v[104:107]
s_add_u32 s64, s64, 32768
v_exp_f32 v22, v22
v_mfma_f32_16x16x32_bf16 v[44:47], a[16:19], a[112:115], v[108:111]
ds_read_b128 a[16:19], v2 offset:4096
v_cvt_pk_bf16_f32 v49, v18, v19
v_mfma_f32_16x16x32_bf16 v[32:35], a[4:7], a[100:103], v[32:35]
s_add_u32 s65, s65, 32768
v_exp_f32 v23, v23
v_mfma_f32_16x16x32_bf16 v[36:39], a[4:7], a[116:119], v[36:39]
ds_read_b128 a[4:7], v2 offset:1024
v_cvt_pk_bf16_f32 v52, v20, v21
v_mfma_f32_16x16x32_bf16 v[40:43], a[20:23], a[100:103], v[40:43]
s_add_u32 s66, s66, 32768
v_exp_f32 v24, v24
v_mfma_f32_16x16x32_bf16 v[44:47], a[20:23], a[116:119], v[44:47]
ds_read_b128 a[20:23], v2 offset:5120
v_cvt_pk_bf16_f32 v53, v22, v23
v_mfma_f32_16x16x32_bf16 v[32:35], a[8:11], a[104:107], v[32:35]
s_and_b32 s66, s66, 0x1ffff
v_exp_f32 v25, v25
v_mfma_f32_16x16x32_bf16 v[36:39], a[8:11], a[120:123], v[36:39]
ds_read_b128 a[8:11], v2 offset:2048
v_cvt_pk_bf16_f32 v50, v24, v25
v_mfma_f32_16x16x32_bf16 v[40:43], a[24:27], a[104:107], v[40:43]
v_exp_f32 v26, v26
v_mfma_f32_16x16x32_bf16 v[44:47], a[24:27], a[120:123], v[44:47]
ds_read_b128 a[24:27], v2 offset:6144
v_mfma_f32_16x16x32_bf16 v[32:35], a[12:15], a[108:111], v[32:35]
v_exp_f32 v27, v27
v_mfma_f32_16x16x32_bf16 v[36:39], a[12:15], a[124:127], v[36:39]
ds_read_b128 a[12:15], v2 offset:3072
v_cvt_pk_bf16_f32 v51, v26, v27
v_mfma_f32_16x16x32_bf16 v[40:43], a[28:31], a[108:111], v[40:43]
v_exp_f32 v28, v28
v_mfma_f32_16x16x32_bf16 v[44:47], a[28:31], a[124:127], v[44:47]
ds_read_b128 a[28:31], v2 offset:7168
v_mfma_f32_16x16x32_bf16 v[192:195], a[32:35], v[56:59], v[192:195]
v_exp_f32 v29, v29
v_mfma_f32_16x16x32_bf16 v[224:227], a[32:35], v[60:63], v[224:227]
ds_read_b128 a[32:35], v3 offset:24576
v_cvt_pk_bf16_f32 v54, v28, v29
v_mfma_f32_16x16x32_bf16 v[196:199], a[36:39], v[56:59], v[196:199]
v_exp_f32 v30, v30
v_mfma_f32_16x16x32_bf16 v[228:231], a[36:39], v[60:63], v[228:231]
ds_read_b128 a[36:39], v3 offset:25600
v_mfma_f32_16x16x32_bf16 v[200:203], a[40:43], v[56:59], v[200:203]
v_exp_f32 v31, v31
v_mfma_f32_16x16x32_bf16 v[232:235], a[40:43], v[60:63], v[232:235]
ds_read_b128 a[40:43], v3 offset:26624
v_cvt_pk_bf16_f32 v55, v30, v31
v_mfma_f32_16x16x32_bf16 v[204:207], a[44:47], v[56:59], v[204:207]
v_mfma_f32_16x16x32_bf16 v[236:239], a[44:47], v[60:63], v[236:239]
ds_read_b128 a[44:47], v3 offset:27648
v_mfma_f32_16x16x32_bf16 v[208:211], a[48:51], v[56:59], v[208:211]
s_add_i32 s68, s68, 32
v_exp_f32 v32, v32
v_mfma_f32_16x16x32_bf16 v[240:243], a[48:51], v[60:63], v[240:243]
ds_read_b128 a[48:51], v3 offset:28672
v_mfma_f32_16x16x32_bf16 v[212:215], a[52:55], v[56:59], v[212:215]
s_cmp_eq_i32 s68, 0
v_exp_f32 v33, v33
v_mfma_f32_16x16x32_bf16 v[244:247], a[52:55], v[60:63], v[244:247]
ds_read_b128 a[52:55], v3 offset:29696
v_mfma_f32_16x16x32_bf16 v[216:219], a[56:59], v[56:59], v[216:219]
s_cselect_b32 s76, 1.0, 0
v_exp_f32 v34, v34
v_mfma_f32_16x16x32_bf16 v[248:251], a[56:59], v[60:63], v[248:251]
ds_read_b128 a[56:59], v3 offset:30720
v_mfma_f32_16x16x32_bf16 v[220:223], a[60:63], v[56:59], v[220:223]
s_cmp_gt_i32 s68, 0
v_exp_f32 v35, v35
v_mfma_f32_16x16x32_bf16 v[252:255], a[60:63], v[60:63], v[252:255]
ds_read_b128 a[60:63], v3 offset:31744
v_mfma_f32_16x16x32_bf16 v[72:75], a[128:131], v[56:59], v[72:75]
s_cselect_b32 s77, s72, 0
v_exp_f32 v36, v36
v_mfma_f32_16x16x32_bf16 v[76:79], a[128:131], v[60:63], v[76:79]
s_waitcnt lgkmcnt(2)
v_mfma_f32_16x16x32_bf16 v[16:19], a[0:3], a[64:67], v[80:83]
v_exp_f32 v37, v37
v_mfma_f32_16x16x32_bf16 v[20:23], a[0:3], a[80:83], v[84:87]
v_cvt_pk_bf16_f32 v56, v32, v33
v_cvt_pk_bf16_f32 v57, v34, v35
v_mfma_f32_16x16x32_bf16 v[24:27], a[16:19], a[64:67], v[88:91]
v_exp_f32 v38, v38
v_mfma_f32_16x16x32_bf16 v[28:31], a[16:19], a[80:83], v[92:95]
)ASM"
R"ASM(s_waitcnt vmcnt(8) lgkmcnt(0)
s_barrier
v_mfma_f32_16x16x32_bf16 v[16:19], a[4:7], a[68:71], v[16:19]
v_exp_f32 v39, v39
v_mfma_f32_16x16x32_bf16 v[20:23], a[4:7], a[84:87], v[20:23]
v_cvt_pk_bf16_f32 v60, v36, v37
v_add_f32 v10, s77, v6
v_mfma_f32_16x16x32_bf16 v[24:27], a[20:23], a[68:71], v[24:27]
v_exp_f32 v40, v40
v_mfma_f32_16x16x32_bf16 v[28:31], a[20:23], a[84:87], v[28:31]
v_cvt_pk_bf16_f32 v61, v38, v39
v_add_f32 v11, s77, v7
v_mfma_f32_16x16x32_bf16 v[16:19], a[8:11], a[72:75], v[16:19]
v_exp_f32 v41, v41
v_mfma_f32_16x16x32_bf16 v[20:23], a[8:11], a[88:91], v[20:23]
v_cvt_pk_bf16_f32 v58, v40, v41
v_fma_f32 v80, s76, v112, v10
v_mfma_f32_16x16x32_bf16 v[24:27], a[24:27], a[72:75], v[24:27]
s_mov_b32 m0, s66
v_exp_f32 v42, v42
v_mfma_f32_16x16x32_bf16 v[28:31], a[24:27], a[88:91], v[28:31]
buffer_load_dwordx4 v12, s[44:47], s64 offen offset:0 sc1 lds
v_mfma_f32_16x16x32_bf16 v[16:19], a[12:15], a[76:79], v[16:19]
v_exp_f32 v43, v43
v_mfma_f32_16x16x32_bf16 v[20:23], a[12:15], a[92:95], v[20:23]
v_cvt_pk_bf16_f32 v59, v42, v43
v_fma_f32 v81, s76, v113, v10
v_mfma_f32_16x16x32_bf16 v[24:27], a[28:31], a[76:79], v[24:27]
v_exp_f32 v44, v44
v_mfma_f32_16x16x32_bf16 v[28:31], a[28:31], a[92:95], v[28:31]
v_fma_f32 v82, s76, v114, v10
v_fma_f32 v83, s76, v115, v10
v_mfma_f32_16x16x32_bf16 v[128:131], a[32:35], v[48:51], v[128:131]
v_exp_f32 v45, v45
v_mfma_f32_16x16x32_bf16 v[160:163], a[32:35], v[52:55], v[160:163]
v_cvt_pk_bf16_f32 v62, v44, v45
v_fma_f32 v84, s76, v116, v11
v_mfma_f32_16x16x32_bf16 v[132:135], a[36:39], v[48:51], v[132:135]
v_exp_f32 v46, v46
v_mfma_f32_16x16x32_bf16 v[164:167], a[36:39], v[52:55], v[164:167]
buffer_load_dwordx4 v12, s[44:47], s64 offen offset:1024 sc1 lds
v_mfma_f32_16x16x32_bf16 v[136:139], a[40:43], v[48:51], v[136:139]
v_exp_f32 v47, v47
v_mfma_f32_16x16x32_bf16 v[168:171], a[40:43], v[52:55], v[168:171]
v_cvt_pk_bf16_f32 v63, v46, v47
v_fma_f32 v85, s76, v117, v11
v_mfma_f32_16x16x32_bf16 v[140:143], a[44:47], v[48:51], v[140:143]
v_fma_f32 v86, s76, v118, v11
v_fma_f32 v87, s76, v119, v11
v_mfma_f32_16x16x32_bf16 v[172:175], a[44:47], v[52:55], v[172:175]
v_fma_f32 v88, s76, v120, v10
v_fma_f32 v89, s76, v121, v10
v_mfma_f32_16x16x32_bf16 v[144:147], a[48:51], v[48:51], v[144:147]
v_exp_f32 v16, v16
v_mfma_f32_16x16x32_bf16 v[176:179], a[48:51], v[52:55], v[176:179]
buffer_load_dwordx4 v12, s[44:47], s64 offen offset:2048 sc1 lds
v_mfma_f32_16x16x32_bf16 v[148:151], a[52:55], v[48:51], v[148:151]
v_exp_f32 v17, v17
v_mfma_f32_16x16x32_bf16 v[180:183], a[52:55], v[52:55], v[180:183]
v_fma_f32 v90, s76, v122, v10
v_fma_f32 v91, s76, v123, v10
v_mfma_f32_16x16x32_bf16 v[152:155], a[56:59], v[48:51], v[152:155]
v_exp_f32 v18, v18
v_mfma_f32_16x16x32_bf16 v[184:187], a[56:59], v[52:55], v[184:187]
v_fma_f32 v92, s76, v124, v11
v_fma_f32 v93, s76, v125, v11
v_mfma_f32_16x16x32_bf16 v[156:159], a[60:63], v[48:51], v[156:159]
v_exp_f32 v19, v19
v_mfma_f32_16x16x32_bf16 v[188:191], a[60:63], v[52:55], v[188:191]
buffer_load_dwordx4 v12, s[44:47], s64 offen offset:3072 sc1 lds
v_mfma_f32_16x16x32_bf16 v[64:67], a[128:131], v[48:51], v[64:67]
v_exp_f32 v20, v20
v_mfma_f32_16x16x32_bf16 v[68:71], a[128:131], v[52:55], v[68:71]
v_fma_f32 v94, s76, v126, v11
v_fma_f32 v95, s76, v127, v11
v_mfma_f32_16x16x32_bf16 v[32:35], a[0:3], a[96:99], v[96:99]
v_exp_f32 v21, v21
v_mfma_f32_16x16x32_bf16 v[36:39], a[0:3], a[112:115], v[100:103]
ds_read_b128 a[0:3], v2 offset:8192
v_cvt_pk_bf16_f32 v48, v16, v17
v_mfma_f32_16x16x32_bf16 v[40:43], a[16:19], a[96:99], v[104:107]
v_exp_f32 v22, v22
v_mfma_f32_16x16x32_bf16 v[44:47], a[16:19], a[112:115], v[108:111]
ds_read_b128 a[16:19], v2 offset:12288
v_cvt_pk_bf16_f32 v49, v18, v19
v_mfma_f32_16x16x32_bf16 v[32:35], a[4:7], a[100:103], v[32:35]
v_exp_f32 v23, v23
v_mfma_f32_16x16x32_bf16 v[36:39], a[4:7], a[116:119], v[36:39]
ds_read_b128 a[4:7], v2 offset:9216
v_cvt_pk_bf16_f32 v52, v20, v21
v_mfma_f32_16x16x32_bf16 v[40:43], a[20:23], a[100:103], v[40:43]
v_exp_f32 v24, v24
v_mfma_f32_16x16x32_bf16 v[44:47], a[20:23], a[116:119], v[44:47]
ds_read_b128 a[20:23], v2 offset:13312
v_cvt_pk_bf16_f32 v53, v22, v23
v_mfma_f32_16x16x32_bf16 v[32:35], a[8:11], a[104:107], v[32:35]
v_exp_f32 v25, v25
v_mfma_f32_16x16x32_bf16 v[36:39], a[8:11], a[120:123], v[36:39]
ds_read_b128 a[8:11], v2 offset:10240
v_cvt_pk_bf16_f32 v50, v24, v25
v_mfma_f32_16x16x32_bf16 v[40:43], a[24:27], a[104:107], v[40:43]
v_exp_f32 v26, v26
v_mfma_f32_16x16x32_bf16 v[44:47], a[24:27], a[120:123], v[44:47]
ds_read_b128 a[24:27], v2 offset:14336
v_xor_b32 v3, 0x10000, v3
v_mfma_f32_16x16x32_bf16 v[32:35], a[12:15], a[108:111], v[32:35]
v_exp_f32 v27, v27
v_mfma_f32_16x16x32_bf16 v[36:39], a[12:15], a[124:127], v[36:39]
ds_read_b128 a[12:15], v2 offset:11264
v_cvt_pk_bf16_f32 v51, v26, v27
v_mfma_f32_16x16x32_bf16 v[40:43], a[28:31], a[108:111], v[40:43]
v_exp_f32 v28, v28
v_mfma_f32_16x16x32_bf16 v[44:47], a[28:31], a[124:127], v[44:47]
ds_read_b128 a[28:31], v2 offset:15360
v_mfma_f32_16x16x32_bf16 v[192:195], a[32:35], v[56:59], v[192:195]
v_exp_f32 v29, v29
v_mfma_f32_16x16x32_bf16 v[224:227], a[32:35], v[60:63], v[224:227]
ds_read_b128 a[32:35], v2 offset:16384
v_cvt_pk_bf16_f32 v54, v28, v29
v_mfma_f32_16x16x32_bf16 v[196:199], a[36:39], v[56:59], v[196:199]
v_exp_f32 v30, v30
v_mfma_f32_16x16x32_bf16 v[228:231], a[36:39], v[60:63], v[228:231]
ds_read_b128 a[36:39], v2 offset:17408
v_mfma_f32_16x16x32_bf16 v[200:203], a[40:43], v[56:59], v[200:203]
v_exp_f32 v31, v31
v_mfma_f32_16x16x32_bf16 v[232:235], a[40:43], v[60:63], v[232:235]
ds_read_b128 a[40:43], v2 offset:18432
v_cvt_pk_bf16_f32 v55, v30, v31
v_mfma_f32_16x16x32_bf16 v[204:207], a[44:47], v[56:59], v[204:207]
v_mfma_f32_16x16x32_bf16 v[236:239], a[44:47], v[60:63], v[236:239]
ds_read_b128 a[44:47], v2 offset:19456
v_mfma_f32_16x16x32_bf16 v[208:211], a[48:51], v[56:59], v[208:211]
s_add_i32 s68, s68, 32
v_exp_f32 v32, v32
v_mfma_f32_16x16x32_bf16 v[240:243], a[48:51], v[60:63], v[240:243]
ds_read_b128 a[48:51], v2 offset:20480
v_mfma_f32_16x16x32_bf16 v[212:215], a[52:55], v[56:59], v[212:215]
s_cmp_eq_i32 s68, 0
v_exp_f32 v33, v33
v_mfma_f32_16x16x32_bf16 v[244:247], a[52:55], v[60:63], v[244:247]
ds_read_b128 a[52:55], v2 offset:21504
v_mfma_f32_16x16x32_bf16 v[216:219], a[56:59], v[56:59], v[216:219]
s_cselect_b32 s76, 1.0, 0
v_exp_f32 v34, v34
v_mfma_f32_16x16x32_bf16 v[248:251], a[56:59], v[60:63], v[248:251]
ds_read_b128 a[56:59], v2 offset:22528
v_mfma_f32_16x16x32_bf16 v[220:223], a[60:63], v[56:59], v[220:223]
s_cmp_gt_i32 s68, 0
v_exp_f32 v35, v35
v_mfma_f32_16x16x32_bf16 v[252:255], a[60:63], v[60:63], v[252:255]
ds_read_b128 a[60:63], v2 offset:23552
v_mfma_f32_16x16x32_bf16 v[72:75], a[128:131], v[56:59], v[72:75]
s_cselect_b32 s77, s72, 0
v_exp_f32 v36, v36
v_mfma_f32_16x16x32_bf16 v[76:79], a[128:131], v[60:63], v[76:79]
s_waitcnt lgkmcnt(2)
v_mfma_f32_16x16x32_bf16 v[16:19], a[0:3], a[64:67], v[80:83]
v_exp_f32 v37, v37
v_mfma_f32_16x16x32_bf16 v[20:23], a[0:3], a[80:83], v[84:87]
v_cvt_pk_bf16_f32 v56, v32, v33
v_mfma_f32_16x16x32_bf16 v[24:27], a[16:19], a[64:67], v[88:91]
v_exp_f32 v38, v38
v_mfma_f32_16x16x32_bf16 v[28:31], a[16:19], a[80:83], v[92:95]
v_cvt_pk_bf16_f32 v57, v34, v35
v_mfma_f32_16x16x32_bf16 v[16:19], a[4:7], a[68:71], v[16:19]
v_exp_f32 v39, v39
v_mfma_f32_16x16x32_bf16 v[20:23], a[4:7], a[84:87], v[20:23]
v_cvt_pk_bf16_f32 v60, v36, v37
v_add_f32 v10, s77, v6
v_mfma_f32_16x16x32_bf16 v[24:27], a[20:23], a[68:71], v[24:27]
v_exp_f32 v40, v40
v_mfma_f32_16x16x32_bf16 v[28:31], a[20:23], a[84:87], v[28:31]
v_cvt_pk_bf16_f32 v61, v38, v39
v_add_f32 v11, s77, v7
v_mfma_f32_16x16x32_bf16 v[16:19], a[8:11], a[72:75], v[16:19]
v_exp_f32 v41, v41
v_mfma_f32_16x16x32_bf16 v[20:23], a[8:11], a[88:91], v[20:23]
v_cvt_pk_bf16_f32 v58, v40, v41
v_fma_f32 v80, s76, v112, v10
v_mfma_f32_16x16x32_bf16 v[24:27], a[24:27], a[72:75], v[24:27]
s_add_u32 m0, s66, 4096
v_exp_f32 v42, v42
v_mfma_f32_16x16x32_bf16 v[28:31], a[24:27], a[88:91], v[28:31]
buffer_load_dwordx4 v12, s[44:47], s65 offen offset:0 sc1 lds
v_mfma_f32_16x16x32_bf16 v[16:19], a[12:15], a[76:79], v[16:19]
v_exp_f32 v43, v43
v_mfma_f32_16x16x32_bf16 v[20:23], a[12:15], a[92:95], v[20:23]
v_cvt_pk_bf16_f32 v59, v42, v43
v_fma_f32 v81, s76, v113, v10
v_mfma_f32_16x16x32_bf16 v[24:27], a[28:31], a[76:79], v[24:27]
v_exp_f32 v44, v44
v_mfma_f32_16x16x32_bf16 v[28:31], a[28:31], a[92:95], v[28:31]
v_fma_f32 v82, s76, v114, v10
v_fma_f32 v83, s76, v115, v10
v_mfma_f32_16x16x32_bf16 v[128:131], a[32:35], v[48:51], v[128:131]
v_exp_f32 v45, v45
v_mfma_f32_16x16x32_bf16 v[160:163], a[32:35], v[52:55], v[160:163]
v_cvt_pk_bf16_f32 v62, v44, v45
v_fma_f32 v84, s76, v116, v11
v_mfma_f32_16x16x32_bf16 v[132:135], a[36:39], v[48:51], v[132:135]
v_exp_f32 v46, v46
v_mfma_f32_16x16x32_bf16 v[164:167], a[36:39], v[52:55], v[164:167]
buffer_load_dwordx4 v12, s[44:47], s65 offen offset:1024 sc1 lds
v_mfma_f32_16x16x32_bf16 v[136:139], a[40:43], v[48:51], v[136:139]
v_exp_f32 v47, v47
v_mfma_f32_16x16x32_bf16 v[168:171], a[40:43], v[52:55], v[168:171]
v_cvt_pk_bf16_f32 v63, v46, v47
v_fma_f32 v85, s76, v117, v11
v_mfma_f32_16x16x32_bf16 v[140:143], a[44:47], v[48:51], v[140:143]
v_fma_f32 v86, s76, v118, v11
v_fma_f32 v87, s76, v119, v11
v_mfma_f32_16x16x32_bf16 v[172:175], a[44:47], v[52:55], v[172:175]
v_fma_f32 v88, s76, v120, v10
v_fma_f32 v89, s76, v121, v10
v_mfma_f32_16x16x32_bf16 v[144:147], a[48:51], v[48:51], v[144:147]
v_exp_f32 v16, v16
v_mfma_f32_16x16x32_bf16 v[176:179], a[48:51], v[52:55], v[176:179]
buffer_load_dwordx4 v12, s[44:47], s65 offen offset:2048 sc1 lds
v_mfma_f32_16x16x32_bf16 v[148:151], a[52:55], v[48:51], v[148:151]
v_exp_f32 v17, v17
v_mfma_f32_16x16x32_bf16 v[180:183], a[52:55], v[52:55], v[180:183]
s_waitcnt lgkmcnt(0)
v_fma_f32 v90, s76, v122, v10
v_fma_f32 v91, s76, v123, v10
v_mfma_f32_16x16x32_bf16 v[152:155], a[56:59], v[48:51], v[152:155]
v_exp_f32 v18, v18
v_mfma_f32_16x16x32_bf16 v[184:187], a[56:59], v[52:55], v[184:187]
v_fma_f32 v92, s76, v124, v11
v_fma_f32 v93, s76, v125, v11
v_mfma_f32_16x16x32_bf16 v[156:159], a[60:63], v[48:51], v[156:159]
v_exp_f32 v19, v19
v_mfma_f32_16x16x32_bf16 v[188:191], a[60:63], v[52:55], v[188:191]
buffer_load_dwordx4 v12, s[44:47], s65 offen offset:3072 sc1 lds
v_mfma_f32_16x16x32_bf16 v[64:67], a[128:131], v[48:51], v[64:67]
v_exp_f32 v20, v20
v_mfma_f32_16x16x32_bf16 v[68:71], a[128:131], v[52:55], v[68:71]
v_fma_f32 v94, s76, v126, v11
v_fma_f32 v95, s76, v127, v11
v_mfma_f32_16x16x32_bf16 v[32:35], a[0:3], a[96:99], v[96:99]
v_exp_f32 v21, v21
v_mfma_f32_16x16x32_bf16 v[36:39], a[0:3], a[112:115], v[100:103]
ds_read_b128 a[0:3], v3 offset:0
v_cvt_pk_bf16_f32 v48, v16, v17
v_mfma_f32_16x16x32_bf16 v[40:43], a[16:19], a[96:99], v[104:107]
s_add_u32 s64, s64, 32768
v_exp_f32 v22, v22
v_mfma_f32_16x16x32_bf16 v[44:47], a[16:19], a[112:115], v[108:111]
ds_read_b128 a[16:19], v3 offset:4096
v_cvt_pk_bf16_f32 v49, v18, v19
v_mfma_f32_16x16x32_bf16 v[32:35], a[4:7], a[100:103], v[32:35]
s_add_u32 s65, s65, 32768
v_exp_f32 v23, v23
v_mfma_f32_16x16x32_bf16 v[36:39], a[4:7], a[116:119], v[36:39]
ds_read_b128 a[4:7], v3 offset:1024
v_cvt_pk_bf16_f32 v52, v20, v21
v_mfma_f32_16x16x32_bf16 v[40:43], a[20:23], a[100:103], v[40:43]
s_add_u32 s66, s66, 32768
v_exp_f32 v24, v24
v_mfma_f32_16x16x32_bf16 v[44:47], a[20:23], a[116:119], v[44:47]
ds_read_b128 a[20:23], v3 offset:5120
v_cvt_pk_bf16_f32 v53, v22, v23
v_mfma_f32_16x16x32_bf16 v[32:35], a[8:11], a[104:107], v[32:35]
s_and_b32 s66, s66, 0x1ffff
v_exp_f32 v25, v25
v_mfma_f32_16x16x32_bf16 v[36:39], a[8:11], a[120:123], v[36:39]
ds_read_b128 a[8:11], v3 offset:2048
v_cvt_pk_bf16_f32 v50, v24, v25
v_mfma_f32_16x16x32_bf16 v[40:43], a[24:27], a[104:107], v[40:43]
v_exp_f32 v26, v26
v_mfma_f32_16x16x32_bf16 v[44:47], a[24:27], a[120:123], v[44:47]
ds_read_b128 a[24:27], v3 offset:6144
v_mfma_f32_16x16x32_bf16 v[32:35], a[12:15], a[108:111], v[32:35]
v_exp_f32 v27, v27
v_mfma_f32_16x16x32_bf16 v[36:39], a[12:15], a[124:127], v[36:39]
ds_read_b128 a[12:15], v3 offset:3072
v_cvt_pk_bf16_f32 v51, v26, v27
v_mfma_f32_16x16x32_bf16 v[40:43], a[28:31], a[108:111], v[40:43]
v_exp_f32 v28, v28
v_mfma_f32_16x16x32_bf16 v[44:47], a[28:31], a[124:127], v[44:47]
ds_read_b128 a[28:31], v3 offset:7168
v_mfma_f32_16x16x32_bf16 v[192:195], a[32:35], v[56:59], v[192:195]
v_exp_f32 v29, v29
v_mfma_f32_16x16x32_bf16 v[224:227], a[32:35], v[60:63], v[224:227]
ds_read_b128 a[32:35], v2 offset:24576
v_cvt_pk_bf16_f32 v54, v28, v29
v_mfma_f32_16x16x32_bf16 v[196:199], a[36:39], v[56:59], v[196:199]
v_exp_f32 v30, v30
v_mfma_f32_16x16x32_bf16 v[228:231], a[36:39], v[60:63], v[228:231]
ds_read_b128 a[36:39], v2 offset:25600
v_mfma_f32_16x16x32_bf16 v[200:203], a[40:43], v[56:59], v[200:203]
v_exp_f32 v31, v31
v_mfma_f32_16x16x32_bf16 v[232:235], a[40:43], v[60:63], v[232:235]
ds_read_b128 a[40:43], v2 offset:26624
v_cvt_pk_bf16_f32 v55, v30, v31
v_mfma_f32_16x16x32_bf16 v[204:207], a[44:47], v[56:59], v[204:207]
v_mfma_f32_16x16x32_bf16 v[236:239], a[44:47], v[60:63], v[236:239]
ds_read_b128 a[44:47], v2 offset:27648
v_mfma_f32_16x16x32_bf16 v[208:211], a[48:51], v[56:59], v[208:211]
s_add_i32 s68, s68, 32
v_exp_f32 v32, v32
)ASM"
R"ASM(v_mfma_f32_16x16x32_bf16 v[240:243], a[48:51], v[60:63], v[240:243]
ds_read_b128 a[48:51], v2 offset:28672
v_mfma_f32_16x16x32_bf16 v[212:215], a[52:55], v[56:59], v[212:215]
s_cmp_eq_i32 s68, 0
v_exp_f32 v33, v33
v_mfma_f32_16x16x32_bf16 v[244:247], a[52:55], v[60:63], v[244:247]
ds_read_b128 a[52:55], v2 offset:29696
v_mfma_f32_16x16x32_bf16 v[216:219], a[56:59], v[56:59], v[216:219]
s_cselect_b32 s76, 1.0, 0
v_exp_f32 v34, v34
v_mfma_f32_16x16x32_bf16 v[248:251], a[56:59], v[60:63], v[248:251]
ds_read_b128 a[56:59], v2 offset:30720
v_mfma_f32_16x16x32_bf16 v[220:223], a[60:63], v[56:59], v[220:223]
s_cmp_gt_i32 s68, 0
v_exp_f32 v35, v35
v_mfma_f32_16x16x32_bf16 v[252:255], a[60:63], v[60:63], v[252:255]
ds_read_b128 a[60:63], v2 offset:31744
v_mfma_f32_16x16x32_bf16 v[72:75], a[128:131], v[56:59], v[72:75]
s_cselect_b32 s77, s72, 0
v_exp_f32 v36, v36
v_mfma_f32_16x16x32_bf16 v[76:79], a[128:131], v[60:63], v[76:79]
s_waitcnt lgkmcnt(2)
v_mfma_f32_16x16x32_bf16 v[16:19], a[0:3], a[64:67], v[80:83]
v_exp_f32 v37, v37
v_mfma_f32_16x16x32_bf16 v[20:23], a[0:3], a[80:83], v[84:87]
v_cvt_pk_bf16_f32 v56, v32, v33
v_cvt_pk_bf16_f32 v57, v34, v35
v_mfma_f32_16x16x32_bf16 v[24:27], a[16:19], a[64:67], v[88:91]
v_exp_f32 v38, v38
v_mfma_f32_16x16x32_bf16 v[28:31], a[16:19], a[80:83], v[92:95]
s_waitcnt vmcnt(8) lgkmcnt(0)
s_barrier
v_mfma_f32_16x16x32_bf16 v[16:19], a[4:7], a[68:71], v[16:19]
v_exp_f32 v39, v39
v_mfma_f32_16x16x32_bf16 v[20:23], a[4:7], a[84:87], v[20:23]
v_cvt_pk_bf16_f32 v60, v36, v37
v_add_f32 v10, s77, v6
v_mfma_f32_16x16x32_bf16 v[24:27], a[20:23], a[68:71], v[24:27]
v_exp_f32 v40, v40
v_mfma_f32_16x16x32_bf16 v[28:31], a[20:23], a[84:87], v[28:31]
v_cvt_pk_bf16_f32 v61, v38, v39
v_add_f32 v11, s77, v7
v_mfma_f32_16x16x32_bf16 v[16:19], a[8:11], a[72:75], v[16:19]
v_exp_f32 v41, v41
v_mfma_f32_16x16x32_bf16 v[20:23], a[8:11], a[88:91], v[20:23]
v_cvt_pk_bf16_f32 v58, v40, v41
v_fma_f32 v80, s76, v112, v10
v_mfma_f32_16x16x32_bf16 v[24:27], a[24:27], a[72:75], v[24:27]
s_mov_b32 m0, s66
v_exp_f32 v42, v42
v_mfma_f32_16x16x32_bf16 v[28:31], a[24:27], a[88:91], v[28:31]
buffer_load_dwordx4 v12, s[44:47], s64 offen offset:0 sc1 lds
v_mfma_f32_16x16x32_bf16 v[16:19], a[12:15], a[76:79], v[16:19]
v_exp_f32 v43, v43
v_mfma_f32_16x16x32_bf16 v[20:23], a[12:15], a[92:95], v[20:23]
v_cvt_pk_bf16_f32 v59, v42, v43
v_fma_f32 v81, s76, v113, v10
v_mfma_f32_16x16x32_bf16 v[24:27], a[28:31], a[76:79], v[24:27]
v_exp_f32 v44, v44
v_mfma_f32_16x16x32_bf16 v[28:31], a[28:31], a[92:95], v[28:31]
v_fma_f32 v82, s76, v114, v10
v_fma_f32 v83, s76, v115, v10
v_mfma_f32_16x16x32_bf16 v[128:131], a[32:35], v[48:51], v[128:131]
v_exp_f32 v45, v45
v_mfma_f32_16x16x32_bf16 v[160:163], a[32:35], v[52:55], v[160:163]
v_cvt_pk_bf16_f32 v62, v44, v45
v_fma_f32 v84, s76, v116, v11
v_mfma_f32_16x16x32_bf16 v[132:135], a[36:39], v[48:51], v[132:135]
v_exp_f32 v46, v46
v_mfma_f32_16x16x32_bf16 v[164:167], a[36:39], v[52:55], v[164:167]
buffer_load_dwordx4 v12, s[44:47], s64 offen offset:1024 sc1 lds
v_mfma_f32_16x16x32_bf16 v[136:139], a[40:43], v[48:51], v[136:139]
v_exp_f32 v47, v47
v_mfma_f32_16x16x32_bf16 v[168:171], a[40:43], v[52:55], v[168:171]
v_cvt_pk_bf16_f32 v63, v46, v47
v_fma_f32 v85, s76, v117, v11
v_mfma_f32_16x16x32_bf16 v[140:143], a[44:47], v[48:51], v[140:143]
v_fma_f32 v86, s76, v118, v11
v_fma_f32 v87, s76, v119, v11
v_mfma_f32_16x16x32_bf16 v[172:175], a[44:47], v[52:55], v[172:175]
v_fma_f32 v88, s76, v120, v10
v_fma_f32 v89, s76, v121, v10
v_mfma_f32_16x16x32_bf16 v[144:147], a[48:51], v[48:51], v[144:147]
v_exp_f32 v16, v16
v_mfma_f32_16x16x32_bf16 v[176:179], a[48:51], v[52:55], v[176:179]
buffer_load_dwordx4 v12, s[44:47], s64 offen offset:2048 sc1 lds
v_mfma_f32_16x16x32_bf16 v[148:151], a[52:55], v[48:51], v[148:151]
v_exp_f32 v17, v17
v_mfma_f32_16x16x32_bf16 v[180:183], a[52:55], v[52:55], v[180:183]
v_fma_f32 v90, s76, v122, v10
v_fma_f32 v91, s76, v123, v10
v_mfma_f32_16x16x32_bf16 v[152:155], a[56:59], v[48:51], v[152:155]
v_exp_f32 v18, v18
v_mfma_f32_16x16x32_bf16 v[184:187], a[56:59], v[52:55], v[184:187]
v_fma_f32 v92, s76, v124, v11
v_fma_f32 v93, s76, v125, v11
v_mfma_f32_16x16x32_bf16 v[156:159], a[60:63], v[48:51], v[156:159]
v_exp_f32 v19, v19
v_mfma_f32_16x16x32_bf16 v[188:191], a[60:63], v[52:55], v[188:191]
buffer_load_dwordx4 v12, s[44:47], s64 offen offset:3072 sc1 lds
v_mfma_f32_16x16x32_bf16 v[64:67], a[128:131], v[48:51], v[64:67]
v_exp_f32 v20, v20
v_mfma_f32_16x16x32_bf16 v[68:71], a[128:131], v[52:55], v[68:71]
v_fma_f32 v94, s76, v126, v11
v_fma_f32 v95, s76, v127, v11
s_sub_u32 s67, s67, 1
s_cmp_eq_u32 s67, 0
s_cbranch_scc1 L_L%=
s_branch L_D%=
L_L%=:
v_mfma_f32_16x16x32_bf16 v[32:35], a[0:3], a[96:99], v[96:99]
s_mov_b32 s63, 8192
buffer_load_dwordx4 a[164:167], v1, s[52:55], s63 offen offset:0 sc1
v_exp_f32 v21, v21
v_mfma_f32_16x16x32_bf16 v[36:39], a[0:3], a[112:115], v[100:103]
ds_read_b128 a[0:3], v3 offset:8192
v_cvt_pk_bf16_f32 v48, v16, v17
v_mfma_f32_16x16x32_bf16 v[40:43], a[16:19], a[96:99], v[104:107]
v_exp_f32 v22, v22
v_mfma_f32_16x16x32_bf16 v[44:47], a[16:19], a[112:115], v[108:111]
ds_read_b128 a[16:19], v3 offset:12288
v_cvt_pk_bf16_f32 v49, v18, v19
v_mfma_f32_16x16x32_bf16 v[32:35], a[4:7], a[100:103], v[32:35]
buffer_load_dwordx4 a[168:171], v1, s[52:55], s63 offen offset:1024 sc1
v_exp_f32 v23, v23
v_mfma_f32_16x16x32_bf16 v[36:39], a[4:7], a[116:119], v[36:39]
ds_read_b128 a[4:7], v3 offset:9216
v_cvt_pk_bf16_f32 v52, v20, v21
v_mfma_f32_16x16x32_bf16 v[40:43], a[20:23], a[100:103], v[40:43]
v_exp_f32 v24, v24
v_mfma_f32_16x16x32_bf16 v[44:47], a[20:23], a[116:119], v[44:47]
ds_read_b128 a[20:23], v3 offset:13312
v_cvt_pk_bf16_f32 v53, v22, v23
v_mfma_f32_16x16x32_bf16 v[32:35], a[8:11], a[104:107], v[32:35]
v_exp_f32 v25, v25
v_mfma_f32_16x16x32_bf16 v[36:39], a[8:11], a[120:123], v[36:39]
ds_read_b128 a[8:11], v3 offset:10240
v_cvt_pk_bf16_f32 v50, v24, v25
v_mfma_f32_16x16x32_bf16 v[40:43], a[24:27], a[104:107], v[40:43]
v_exp_f32 v26, v26
v_mfma_f32_16x16x32_bf16 v[44:47], a[24:27], a[120:123], v[44:47]
ds_read_b128 a[24:27], v3 offset:14336
v_xor_b32 v2, 0x10000, v2
v_mfma_f32_16x16x32_bf16 v[32:35], a[12:15], a[108:111], v[32:35]
v_exp_f32 v27, v27
v_mfma_f32_16x16x32_bf16 v[36:39], a[12:15], a[124:127], v[36:39]
ds_read_b128 a[12:15], v3 offset:11264
v_cvt_pk_bf16_f32 v51, v26, v27
v_mfma_f32_16x16x32_bf16 v[40:43], a[28:31], a[108:111], v[40:43]
v_exp_f32 v28, v28
v_mfma_f32_16x16x32_bf16 v[44:47], a[28:31], a[124:127], v[44:47]
ds_read_b128 a[28:31], v3 offset:15360
buffer_load_dwordx4 a[172:175], v1, s[52:55], s63 offen offset:2048 sc1
v_mfma_f32_16x16x32_bf16 v[192:195], a[32:35], v[56:59], v[192:195]
v_exp_f32 v29, v29
v_mfma_f32_16x16x32_bf16 v[224:227], a[32:35], v[60:63], v[224:227]
ds_read_b128 a[32:35], v3 offset:16384
v_cvt_pk_bf16_f32 v54, v28, v29
v_mfma_f32_16x16x32_bf16 v[196:199], a[36:39], v[56:59], v[196:199]
v_exp_f32 v30, v30
v_mfma_f32_16x16x32_bf16 v[228:231], a[36:39], v[60:63], v[228:231]
ds_read_b128 a[36:39], v3 offset:17408
buffer_load_dwordx4 a[176:179], v1, s[52:55], s63 offen offset:3072 sc1
v_mfma_f32_16x16x32_bf16 v[200:203], a[40:43], v[56:59], v[200:203]
v_exp_f32 v31, v31
v_mfma_f32_16x16x32_bf16 v[232:235], a[40:43], v[60:63], v[232:235]
ds_read_b128 a[40:43], v3 offset:18432
v_cvt_pk_bf16_f32 v55, v30, v31
v_mfma_f32_16x16x32_bf16 v[204:207], a[44:47], v[56:59], v[204:207]
s_mov_b32 s63, 12288
buffer_load_dwordx4 a[180:183], v1, s[52:55], s63 offen offset:0 sc1
v_mfma_f32_16x16x32_bf16 v[236:239], a[44:47], v[60:63], v[236:239]
ds_read_b128 a[44:47], v3 offset:19456
v_mfma_f32_16x16x32_bf16 v[208:211], a[48:51], v[56:59], v[208:211]
v_exp_f32 v32, v32
v_mfma_f32_16x16x32_bf16 v[240:243], a[48:51], v[60:63], v[240:243]
ds_read_b128 a[48:51], v3 offset:20480
buffer_load_dwordx4 a[184:187], v1, s[52:55], s63 offen offset:1024 sc1
v_mfma_f32_16x16x32_bf16 v[212:215], a[52:55], v[56:59], v[212:215]
v_exp_f32 v33, v33
v_mfma_f32_16x16x32_bf16 v[244:247], a[52:55], v[60:63], v[244:247]
ds_read_b128 a[52:55], v3 offset:21504
v_mfma_f32_16x16x32_bf16 v[216:219], a[56:59], v[56:59], v[216:219]
v_exp_f32 v34, v34
v_mfma_f32_16x16x32_bf16 v[248:251], a[56:59], v[60:63], v[248:251]
ds_read_b128 a[56:59], v3 offset:22528
buffer_load_dwordx4 a[188:191], v1, s[52:55], s63 offen offset:2048 sc1
v_mfma_f32_16x16x32_bf16 v[220:223], a[60:63], v[56:59], v[220:223]
v_exp_f32 v35, v35
v_mfma_f32_16x16x32_bf16 v[252:255], a[60:63], v[60:63], v[252:255]
ds_read_b128 a[60:63], v3 offset:23552
v_mfma_f32_16x16x32_bf16 v[72:75], a[128:131], v[56:59], v[72:75]
v_exp_f32 v36, v36
v_mfma_f32_16x16x32_bf16 v[76:79], a[128:131], v[60:63], v[76:79]
s_waitcnt lgkmcnt(2)
v_mfma_f32_16x16x32_bf16 v[16:19], a[0:3], a[64:67], v[80:83]
v_exp_f32 v37, v37
v_mfma_f32_16x16x32_bf16 v[20:23], a[0:3], a[80:83], v[84:87]
v_cvt_pk_bf16_f32 v56, v32, v33
v_mfma_f32_16x16x32_bf16 v[24:27], a[16:19], a[64:67], v[88:91]
v_exp_f32 v38, v38
v_mfma_f32_16x16x32_bf16 v[28:31], a[16:19], a[80:83], v[92:95]
v_cvt_pk_bf16_f32 v57, v34, v35
v_mfma_f32_16x16x32_bf16 v[16:19], a[4:7], a[68:71], v[16:19]
v_exp_f32 v39, v39
v_mfma_f32_16x16x32_bf16 v[20:23], a[4:7], a[84:87], v[20:23]
v_cvt_pk_bf16_f32 v60, v36, v37
v_mfma_f32_16x16x32_bf16 v[24:27], a[20:23], a[68:71], v[24:27]
v_exp_f32 v40, v40
v_mfma_f32_16x16x32_bf16 v[28:31], a[20:23], a[84:87], v[28:31]
v_cvt_pk_bf16_f32 v61, v38, v39
v_mfma_f32_16x16x32_bf16 v[16:19], a[8:11], a[72:75], v[16:19]
v_exp_f32 v41, v41
v_mfma_f32_16x16x32_bf16 v[20:23], a[8:11], a[88:91], v[20:23]
v_cvt_pk_bf16_f32 v58, v40, v41
v_mfma_f32_16x16x32_bf16 v[24:27], a[24:27], a[72:75], v[24:27]
s_add_u32 m0, s66, 4096
v_exp_f32 v42, v42
v_mfma_f32_16x16x32_bf16 v[28:31], a[24:27], a[88:91], v[28:31]
buffer_load_dwordx4 v12, s[44:47], s65 offen offset:0 sc1 lds
v_mfma_f32_16x16x32_bf16 v[16:19], a[12:15], a[76:79], v[16:19]
v_exp_f32 v43, v43
v_mfma_f32_16x16x32_bf16 v[20:23], a[12:15], a[92:95], v[20:23]
v_cvt_pk_bf16_f32 v59, v42, v43
v_mfma_f32_16x16x32_bf16 v[24:27], a[28:31], a[76:79], v[24:27]
v_exp_f32 v44, v44
v_mfma_f32_16x16x32_bf16 v[28:31], a[28:31], a[92:95], v[28:31]
v_mfma_f32_16x16x32_bf16 v[128:131], a[32:35], v[48:51], v[128:131]
v_exp_f32 v45, v45
v_mfma_f32_16x16x32_bf16 v[160:163], a[32:35], v[52:55], v[160:163]
v_cvt_pk_bf16_f32 v62, v44, v45
v_mfma_f32_16x16x32_bf16 v[132:135], a[36:39], v[48:51], v[132:135]
v_exp_f32 v46, v46
v_mfma_f32_16x16x32_bf16 v[164:167], a[36:39], v[52:55], v[164:167]
buffer_load_dwordx4 v12, s[44:47], s65 offen offset:1024 sc1 lds
v_mfma_f32_16x16x32_bf16 v[136:139], a[40:43], v[48:51], v[136:139]
v_exp_f32 v47, v47
v_mfma_f32_16x16x32_bf16 v[168:171], a[40:43], v[52:55], v[168:171]
v_cvt_pk_bf16_f32 v63, v46, v47
v_mfma_f32_16x16x32_bf16 v[140:143], a[44:47], v[48:51], v[140:143]
v_mfma_f32_16x16x32_bf16 v[172:175], a[44:47], v[52:55], v[172:175]
v_mfma_f32_16x16x32_bf16 v[144:147], a[48:51], v[48:51], v[144:147]
s_add_i32 s69, s69, 32
v_exp_f32 v16, v16
v_mfma_f32_16x16x32_bf16 v[176:179], a[48:51], v[52:55], v[176:179]
buffer_load_dwordx4 v12, s[44:47], s65 offen offset:2048 sc1 lds
v_mfma_f32_16x16x32_bf16 v[148:151], a[52:55], v[48:51], v[148:151]
s_cmp_eq_i32 s69, 0
v_exp_f32 v17, v17
v_mfma_f32_16x16x32_bf16 v[180:183], a[52:55], v[52:55], v[180:183]
s_waitcnt lgkmcnt(0)
v_mfma_f32_16x16x32_bf16 v[152:155], a[56:59], v[48:51], v[152:155]
s_cselect_b32 s74, 1.0, 0
v_exp_f32 v18, v18
v_mfma_f32_16x16x32_bf16 v[184:187], a[56:59], v[52:55], v[184:187]
v_mfma_f32_16x16x32_bf16 v[156:159], a[60:63], v[48:51], v[156:159]
s_cmp_gt_i32 s69, 0
v_exp_f32 v19, v19
v_mfma_f32_16x16x32_bf16 v[188:191], a[60:63], v[52:55], v[188:191]
buffer_load_dwordx4 v12, s[44:47], s65 offen offset:3072 sc1 lds
v_mfma_f32_16x16x32_bf16 v[64:67], a[128:131], v[48:51], v[64:67]
s_cselect_b32 s75, s72, 0
v_exp_f32 v20, v20
v_mfma_f32_16x16x32_bf16 v[68:71], a[128:131], v[52:55], v[68:71]
v_mfma_f32_16x16x32_bf16 v[32:35], a[0:3], a[96:99], v[96:99]
s_mov_b32 s63, 12288
buffer_load_dwordx4 a[192:195], v1, s[52:55], s63 offen offset:3072 sc1
v_exp_f32 v21, v21
v_mfma_f32_16x16x32_bf16 v[36:39], a[0:3], a[112:115], v[100:103]
ds_read_b128 a[0:3], v2 offset:0
v_cvt_pk_bf16_f32 v48, v16, v17
v_mfma_f32_16x16x32_bf16 v[40:43], a[16:19], a[96:99], v[104:107]
s_add_u32 s64, s64, 32768
v_exp_f32 v22, v22
v_mfma_f32_16x16x32_bf16 v[44:47], a[16:19], a[112:115], v[108:111]
ds_read_b128 a[16:19], v2 offset:4096
v_cvt_pk_bf16_f32 v49, v18, v19
v_mfma_f32_16x16x32_bf16 v[32:35], a[4:7], a[100:103], v[32:35]
s_add_u32 s65, s65, 32768
s_mov_b32 s63, 16384
buffer_load_dwordx4 a[196:199], v1, s[52:55], s63 offen offset:0 sc1
v_exp_f32 v23, v23
v_mfma_f32_16x16x32_bf16 v[36:39], a[4:7], a[116:119], v[36:39]
ds_read_b128 a[4:7], v2 offset:1024
v_cvt_pk_bf16_f32 v52, v20, v21
v_add_f32 v10, s75, v8
v_mfma_f32_16x16x32_bf16 v[40:43], a[20:23], a[100:103], v[40:43]
s_add_u32 s66, s66, 32768
v_exp_f32 v24, v24
v_mfma_f32_16x16x32_bf16 v[44:47], a[20:23], a[116:119], v[44:47]
ds_read_b128 a[20:23], v2 offset:5120
v_cvt_pk_bf16_f32 v53, v22, v23
v_add_f32 v11, s75, v9
v_mfma_f32_16x16x32_bf16 v[32:35], a[8:11], a[104:107], v[32:35]
s_and_b32 s66, s66, 0x1ffff
v_exp_f32 v25, v25
v_mfma_f32_16x16x32_bf16 v[36:39], a[8:11], a[120:123], v[36:39]
ds_read_b128 a[8:11], v2 offset:2048
v_cvt_pk_bf16_f32 v50, v24, v25
v_fma_f32 v96, s74, v112, v10
)ASM"
R"ASM(v_mfma_f32_16x16x32_bf16 v[40:43], a[24:27], a[104:107], v[40:43]
buffer_load_dwordx4 a[200:203], v1, s[52:55], s63 offen offset:1024 sc1
v_exp_f32 v26, v26
v_mfma_f32_16x16x32_bf16 v[44:47], a[24:27], a[120:123], v[44:47]
ds_read_b128 a[24:27], v2 offset:6144
v_fma_f32 v97, s74, v113, v10
v_fma_f32 v98, s74, v114, v10
v_mfma_f32_16x16x32_bf16 v[32:35], a[12:15], a[108:111], v[32:35]
v_exp_f32 v27, v27
v_mfma_f32_16x16x32_bf16 v[36:39], a[12:15], a[124:127], v[36:39]
ds_read_b128 a[12:15], v2 offset:3072
v_cvt_pk_bf16_f32 v51, v26, v27
v_fma_f32 v99, s74, v115, v10
v_mfma_f32_16x16x32_bf16 v[40:43], a[28:31], a[108:111], v[40:43]
buffer_load_dwordx4 a[204:207], v1, s[52:55], s63 offen offset:2048 sc1
v_exp_f32 v28, v28
v_mfma_f32_16x16x32_bf16 v[44:47], a[28:31], a[124:127], v[44:47]
ds_read_b128 a[28:31], v2 offset:7168
v_fma_f32 v100, s74, v116, v11
v_fma_f32 v101, s74, v117, v11
v_mfma_f32_16x16x32_bf16 v[192:195], a[32:35], v[56:59], v[192:195]
v_exp_f32 v29, v29
v_mfma_f32_16x16x32_bf16 v[224:227], a[32:35], v[60:63], v[224:227]
ds_read_b128 a[32:35], v3 offset:24576
v_cvt_pk_bf16_f32 v54, v28, v29
v_mfma_f32_16x16x32_bf16 v[196:199], a[36:39], v[56:59], v[196:199]
v_exp_f32 v30, v30
v_mfma_f32_16x16x32_bf16 v[228:231], a[36:39], v[60:63], v[228:231]
ds_read_b128 a[36:39], v3 offset:25600
buffer_load_dwordx4 a[208:211], v1, s[52:55], s63 offen offset:3072 sc1
v_fma_f32 v102, s74, v118, v11
v_mfma_f32_16x16x32_bf16 v[200:203], a[40:43], v[56:59], v[200:203]
v_exp_f32 v31, v31
v_mfma_f32_16x16x32_bf16 v[232:235], a[40:43], v[60:63], v[232:235]
ds_read_b128 a[40:43], v3 offset:26624
v_cvt_pk_bf16_f32 v55, v30, v31
v_mfma_f32_16x16x32_bf16 v[204:207], a[44:47], v[56:59], v[204:207]
v_fma_f32 v103, s74, v119, v11
v_fma_f32 v104, s74, v120, v10
v_mfma_f32_16x16x32_bf16 v[236:239], a[44:47], v[60:63], v[236:239]
ds_read_b128 a[44:47], v3 offset:27648
v_fma_f32 v105, s74, v121, v10
v_mfma_f32_16x16x32_bf16 v[208:211], a[48:51], v[56:59], v[208:211]
s_add_i32 s69, s69, 32
s_mov_b32 s63, 20480
buffer_load_dwordx4 a[212:215], v1, s[52:55], s63 offen offset:0 sc1
v_exp_f32 v32, v32
v_mfma_f32_16x16x32_bf16 v[240:243], a[48:51], v[60:63], v[240:243]
ds_read_b128 a[48:51], v3 offset:28672
v_fma_f32 v106, s74, v122, v10
v_mfma_f32_16x16x32_bf16 v[212:215], a[52:55], v[56:59], v[212:215]
s_cmp_eq_i32 s69, 0
v_exp_f32 v33, v33
v_mfma_f32_16x16x32_bf16 v[244:247], a[52:55], v[60:63], v[244:247]
ds_read_b128 a[52:55], v3 offset:29696
v_fma_f32 v107, s74, v123, v10
v_mfma_f32_16x16x32_bf16 v[216:219], a[56:59], v[56:59], v[216:219]
s_cselect_b32 s76, 1.0, 0
v_exp_f32 v34, v34
v_mfma_f32_16x16x32_bf16 v[248:251], a[56:59], v[60:63], v[248:251]
ds_read_b128 a[56:59], v3 offset:30720
buffer_load_dwordx4 a[216:219], v1, s[52:55], s63 offen offset:1024 sc1
v_fma_f32 v108, s74, v124, v11
v_mfma_f32_16x16x32_bf16 v[220:223], a[60:63], v[56:59], v[220:223]
s_cmp_gt_i32 s69, 0
v_exp_f32 v35, v35
v_mfma_f32_16x16x32_bf16 v[252:255], a[60:63], v[60:63], v[252:255]
ds_read_b128 a[60:63], v3 offset:31744
v_fma_f32 v109, s74, v125, v11
v_mfma_f32_16x16x32_bf16 v[72:75], a[128:131], v[56:59], v[72:75]
s_cselect_b32 s77, s72, 0
v_exp_f32 v36, v36
v_mfma_f32_16x16x32_bf16 v[76:79], a[128:131], v[60:63], v[76:79]
s_waitcnt lgkmcnt(1)
v_fma_f32 v110, s74, v126, v11
v_fma_f32 v111, s74, v127, v11
v_mfma_f32_16x16x32_bf16 v[16:19], a[0:3], a[96:99], v[96:99]
s_mov_b32 s63, 20480
buffer_load_dwordx4 a[220:223], v1, s[52:55], s63 offen offset:2048 sc1
v_exp_f32 v37, v37
v_mfma_f32_16x16x32_bf16 v[20:23], a[0:3], a[112:115], v[100:103]
ds_read_b128 a[0:3], v2 offset:8192
v_cvt_pk_bf16_f32 v56, v32, v33
v_mfma_f32_16x16x32_bf16 v[24:27], a[16:19], a[96:99], v[104:107]
v_exp_f32 v38, v38
v_mfma_f32_16x16x32_bf16 v[28:31], a[16:19], a[112:115], v[108:111]
ds_read_b128 a[16:19], v2 offset:12288
v_cvt_pk_bf16_f32 v57, v34, v35
v_mfma_f32_16x16x32_bf16 v[16:19], a[4:7], a[100:103], v[16:19]
v_exp_f32 v39, v39
v_mfma_f32_16x16x32_bf16 v[20:23], a[4:7], a[116:119], v[20:23]
ds_read_b128 a[4:7], v2 offset:9216
buffer_load_dwordx4 a[224:227], v1, s[52:55], s63 offen offset:3072 sc1
v_cvt_pk_bf16_f32 v60, v36, v37
v_mfma_f32_16x16x32_bf16 v[24:27], a[20:23], a[100:103], v[24:27]
v_exp_f32 v40, v40
v_mfma_f32_16x16x32_bf16 v[28:31], a[20:23], a[116:119], v[28:31]
ds_read_b128 a[20:23], v2 offset:13312
v_cvt_pk_bf16_f32 v61, v38, v39
v_mfma_f32_16x16x32_bf16 v[16:19], a[8:11], a[104:107], v[16:19]
v_exp_f32 v41, v41
v_mfma_f32_16x16x32_bf16 v[20:23], a[8:11], a[120:123], v[20:23]
ds_read_b128 a[8:11], v2 offset:10240
v_cvt_pk_bf16_f32 v58, v40, v41
v_mfma_f32_16x16x32_bf16 v[24:27], a[24:27], a[104:107], v[24:27]
v_exp_f32 v42, v42
v_mfma_f32_16x16x32_bf16 v[28:31], a[24:27], a[120:123], v[28:31]
s_mov_b32 s63, 24576
ds_read_b128 a[24:27], v2 offset:14336
buffer_load_dwordx4 a[228:231], v1, s[52:55], s63 offen offset:0 sc1
v_add_f32 v10, s77, v8
v_mfma_f32_16x16x32_bf16 v[16:19], a[12:15], a[108:111], v[16:19]
v_exp_f32 v43, v43
v_mfma_f32_16x16x32_bf16 v[20:23], a[12:15], a[124:127], v[20:23]
ds_read_b128 a[12:15], v2 offset:11264
v_cvt_pk_bf16_f32 v59, v42, v43
v_mfma_f32_16x16x32_bf16 v[24:27], a[28:31], a[108:111], v[24:27]
v_exp_f32 v44, v44
v_mfma_f32_16x16x32_bf16 v[28:31], a[28:31], a[124:127], v[28:31]
ds_read_b128 a[28:31], v2 offset:15360
v_add_f32 v11, s77, v9
v_mfma_f32_16x16x32_bf16 v[128:131], a[32:35], v[48:51], v[128:131]
v_exp_f32 v45, v45
v_mfma_f32_16x16x32_bf16 v[160:163], a[32:35], v[52:55], v[160:163]
v_cvt_pk_bf16_f32 v62, v44, v45
v_fma_f32 v96, s76, v112, v10
v_mfma_f32_16x16x32_bf16 v[132:135], a[36:39], v[48:51], v[132:135]
v_exp_f32 v46, v46
v_mfma_f32_16x16x32_bf16 v[164:167], a[36:39], v[52:55], v[164:167]
v_fma_f32 v97, s76, v113, v10
v_fma_f32 v98, s76, v114, v10
v_mfma_f32_16x16x32_bf16 v[136:139], a[40:43], v[48:51], v[136:139]
v_exp_f32 v47, v47
v_mfma_f32_16x16x32_bf16 v[168:171], a[40:43], v[52:55], v[168:171]
v_cvt_pk_bf16_f32 v63, v46, v47
v_fma_f32 v99, s76, v115, v10
v_mfma_f32_16x16x32_bf16 v[140:143], a[44:47], v[48:51], v[140:143]
v_fma_f32 v100, s76, v116, v11
v_fma_f32 v101, s76, v117, v11
v_mfma_f32_16x16x32_bf16 v[172:175], a[44:47], v[52:55], v[172:175]
buffer_load_dwordx4 a[232:235], v1, s[52:55], s63 offen offset:1024 sc1
v_fma_f32 v102, s76, v118, v11
v_mfma_f32_16x16x32_bf16 v[144:147], a[48:51], v[48:51], v[144:147]
buffer_load_dwordx4 a[236:239], v1, s[52:55], s63 offen offset:2048 sc1
v_exp_f32 v16, v16
v_mfma_f32_16x16x32_bf16 v[176:179], a[48:51], v[52:55], v[176:179]
v_fma_f32 v103, s76, v119, v11
v_fma_f32 v104, s76, v120, v10
v_mfma_f32_16x16x32_bf16 v[148:151], a[52:55], v[48:51], v[148:151]
v_exp_f32 v17, v17
v_mfma_f32_16x16x32_bf16 v[180:183], a[52:55], v[52:55], v[180:183]
v_fma_f32 v105, s76, v121, v10
v_fma_f32 v106, s76, v122, v10
v_mfma_f32_16x16x32_bf16 v[152:155], a[56:59], v[48:51], v[152:155]
v_exp_f32 v18, v18
v_mfma_f32_16x16x32_bf16 v[184:187], a[56:59], v[52:55], v[184:187]
s_waitcnt lgkmcnt(0)
v_fma_f32 v107, s76, v123, v10
v_fma_f32 v108, s76, v124, v11
v_mfma_f32_16x16x32_bf16 v[156:159], a[60:63], v[48:51], v[156:159]
v_exp_f32 v19, v19
v_mfma_f32_16x16x32_bf16 v[188:191], a[60:63], v[52:55], v[188:191]
v_fma_f32 v109, s76, v125, v11
v_fma_f32 v110, s76, v126, v11
v_mfma_f32_16x16x32_bf16 v[64:67], a[128:131], v[48:51], v[64:67]
v_exp_f32 v20, v20
v_mfma_f32_16x16x32_bf16 v[68:71], a[128:131], v[52:55], v[68:71]
buffer_load_dwordx4 a[240:243], v1, s[52:55], s63 offen offset:3072 sc1
v_fma_f32 v111, s76, v127, v11
s_nop 7
v_rcp_f32 v84, v64
v_rcp_f32 v86, v68
s_nop 0
v_mov_b32 v85, v84
v_mov_b32 v87, v86
v_pk_mul_f32 v[128:129], v[128:129], v[84:85]
v_pk_mul_f32 v[130:131], v[130:131], v[84:85]
v_pk_mul_f32 v[132:133], v[132:133], v[84:85]
v_pk_mul_f32 v[134:135], v[134:135], v[84:85]
v_cvt_pk_bf16_f32 v128, v128, v129
v_cvt_pk_bf16_f32 v129, v130, v131
v_cvt_pk_bf16_f32 v130, v132, v133
v_cvt_pk_bf16_f32 v131, v134, v135
v_pk_mul_f32 v[136:137], v[136:137], v[84:85]
v_pk_mul_f32 v[138:139], v[138:139], v[84:85]
v_pk_mul_f32 v[140:141], v[140:141], v[84:85]
v_pk_mul_f32 v[142:143], v[142:143], v[84:85]
v_cvt_pk_bf16_f32 v136, v136, v137
v_cvt_pk_bf16_f32 v137, v138, v139
v_cvt_pk_bf16_f32 v138, v140, v141
v_cvt_pk_bf16_f32 v139, v142, v143
s_lshl_b32 s62, s61, 3
s_lshl_b32 s63, s39, 1
s_add_u32 s62, s62, s63
s_add_u32 s62, s62, 0
s_lshl_b32 s62, s62, 17
s_lshl_b32 s63, s60, 12
s_add_u32 s62, s62, s63
buffer_store_dwordx4 v[128:131], v1, s[56:59], s62 offen offset:0 sc1
v_mfma_f32_16x16x32_bf16 v[80:83], a[128:131], v[128:131], 0
v_pk_mul_f32 v[144:145], v[144:145], v[84:85]
v_pk_mul_f32 v[146:147], v[146:147], v[84:85]
v_pk_mul_f32 v[148:149], v[148:149], v[84:85]
v_pk_mul_f32 v[150:151], v[150:151], v[84:85]
v_cvt_pk_bf16_f32 v144, v144, v145
v_cvt_pk_bf16_f32 v145, v146, v147
v_cvt_pk_bf16_f32 v146, v148, v149
v_cvt_pk_bf16_f32 v147, v150, v151
buffer_store_dwordx4 v[136:139], v1, s[56:59], s62 offen offset:1024 sc1
v_mfma_f32_16x16x32_bf16 v[80:83], a[128:131], v[136:139], v[80:83]
v_pk_mul_f32 v[152:153], v[152:153], v[84:85]
v_pk_mul_f32 v[154:155], v[154:155], v[84:85]
v_pk_mul_f32 v[156:157], v[156:157], v[84:85]
v_pk_mul_f32 v[158:159], v[158:159], v[84:85]
v_cvt_pk_bf16_f32 v152, v152, v153
v_cvt_pk_bf16_f32 v153, v154, v155
v_cvt_pk_bf16_f32 v154, v156, v157
v_cvt_pk_bf16_f32 v155, v158, v159
buffer_store_dwordx4 v[144:147], v1, s[56:59], s62 offen offset:2048 sc1
v_mfma_f32_16x16x32_bf16 v[80:83], a[128:131], v[144:147], v[80:83]
v_pk_mul_f32 v[160:161], v[160:161], v[86:87]
v_pk_mul_f32 v[162:163], v[162:163], v[86:87]
v_pk_mul_f32 v[164:165], v[164:165], v[86:87]
v_pk_mul_f32 v[166:167], v[166:167], v[86:87]
v_cvt_pk_bf16_f32 v160, v160, v161
v_cvt_pk_bf16_f32 v161, v162, v163
v_cvt_pk_bf16_f32 v162, v164, v165
v_cvt_pk_bf16_f32 v163, v166, v167
buffer_store_dwordx4 v[152:155], v1, s[56:59], s62 offen offset:3072 sc1
v_mfma_f32_16x16x32_bf16 v[80:83], a[128:131], v[152:155], v[80:83]
v_pk_mul_f32 v[168:169], v[168:169], v[86:87]
v_pk_mul_f32 v[170:171], v[170:171], v[86:87]
v_pk_mul_f32 v[172:173], v[172:173], v[86:87]
v_pk_mul_f32 v[174:175], v[174:175], v[86:87]
v_cvt_pk_bf16_f32 v168, v168, v169
v_cvt_pk_bf16_f32 v169, v170, v171
v_cvt_pk_bf16_f32 v170, v172, v173
v_cvt_pk_bf16_f32 v171, v174, v175
s_lshl_b32 s62, s61, 3
s_lshl_b32 s63, s39, 1
s_add_u32 s62, s62, s63
s_add_u32 s62, s62, 1
s_lshl_b32 s62, s62, 17
s_lshl_b32 s63, s60, 12
s_add_u32 s62, s62, s63
buffer_store_dwordx4 v[160:163], v1, s[56:59], s62 offen offset:0 sc1
v_mfma_f32_16x16x32_bf16 v[80:83], a[128:131], v[160:163], v[80:83]
v_pk_mul_f32 v[176:177], v[176:177], v[86:87]
v_pk_mul_f32 v[178:179], v[178:179], v[86:87]
v_pk_mul_f32 v[180:181], v[180:181], v[86:87]
v_pk_mul_f32 v[182:183], v[182:183], v[86:87]
v_cvt_pk_bf16_f32 v176, v176, v177
v_cvt_pk_bf16_f32 v177, v178, v179
v_cvt_pk_bf16_f32 v178, v180, v181
v_cvt_pk_bf16_f32 v179, v182, v183
buffer_store_dwordx4 v[168:171], v1, s[56:59], s62 offen offset:1024 sc1
v_mfma_f32_16x16x32_bf16 v[80:83], a[128:131], v[168:171], v[80:83]
v_pk_mul_f32 v[184:185], v[184:185], v[86:87]
v_pk_mul_f32 v[186:187], v[186:187], v[86:87]
v_pk_mul_f32 v[188:189], v[188:189], v[86:87]
v_pk_mul_f32 v[190:191], v[190:191], v[86:87]
v_cvt_pk_bf16_f32 v184, v184, v185
v_cvt_pk_bf16_f32 v185, v186, v187
v_cvt_pk_bf16_f32 v186, v188, v189
v_cvt_pk_bf16_f32 v187, v190, v191
buffer_store_dwordx4 v[176:179], v1, s[56:59], s62 offen offset:2048 sc1
v_mfma_f32_16x16x32_bf16 v[80:83], a[128:131], v[176:179], v[80:83]
buffer_store_dwordx4 v[184:187], v1, s[56:59], s62 offen offset:3072 sc1
v_mfma_f32_16x16x32_bf16 v[80:83], a[128:131], v[184:187], v[80:83]
s_sub_u32 s67, s71, s70
s_lshr_b32 s67, s67, 1
s_sub_u32 s67, s67, 1
s_cmp_eq_u32 s67, 0
s_cbranch_scc1 L_Ue%=
L_S%=:
v_mfma_f32_16x16x32_bf16 v[32:35], a[0:3], a[96:99], v[96:99]
v_exp_f32 v21, v21
v_mfma_f32_16x16x32_bf16 v[36:39], a[0:3], a[112:115], v[100:103]
v_cvt_pk_bf16_f32 v48, v16, v17
v_cvt_pk_bf16_f32 v49, v18, v19
v_mfma_f32_16x16x32_bf16 v[40:43], a[16:19], a[96:99], v[104:107]
v_exp_f32 v22, v22
v_mfma_f32_16x16x32_bf16 v[44:47], a[16:19], a[112:115], v[108:111]
s_waitcnt vmcnt(8) lgkmcnt(0)
s_barrier
v_xor_b32 v3, 0x10000, v3
v_mfma_f32_16x16x32_bf16 v[32:35], a[4:7], a[100:103], v[32:35]
v_exp_f32 v23, v23
v_mfma_f32_16x16x32_bf16 v[36:39], a[4:7], a[116:119], v[36:39]
ds_read_b128 a[0:3], v3 offset:0
v_cvt_pk_bf16_f32 v52, v20, v21
v_mfma_f32_16x16x32_bf16 v[40:43], a[20:23], a[100:103], v[40:43]
v_exp_f32 v24, v24
v_mfma_f32_16x16x32_bf16 v[44:47], a[20:23], a[116:119], v[44:47]
ds_read_b128 a[16:19], v3 offset:4096
v_cvt_pk_bf16_f32 v53, v22, v23
v_mfma_f32_16x16x32_bf16 v[32:35], a[8:11], a[104:107], v[32:35]
v_exp_f32 v25, v25
v_mfma_f32_16x16x32_bf16 v[36:39], a[8:11], a[120:123], v[36:39]
ds_read_b128 a[4:7], v3 offset:1024
)ASM"
R"ASM(v_cvt_pk_bf16_f32 v50, v24, v25
v_mfma_f32_16x16x32_bf16 v[40:43], a[24:27], a[104:107], v[40:43]
v_exp_f32 v26, v26
v_mfma_f32_16x16x32_bf16 v[44:47], a[24:27], a[120:123], v[44:47]
ds_read_b128 a[20:23], v3 offset:5120
ds_read_b128 a[8:11], v3 offset:2048
v_mfma_f32_16x16x32_bf16 v[32:35], a[12:15], a[108:111], v[32:35]
v_exp_f32 v27, v27
v_mfma_f32_16x16x32_bf16 v[36:39], a[12:15], a[124:127], v[36:39]
ds_read_b128 a[24:27], v3 offset:6144
v_cvt_pk_bf16_f32 v51, v26, v27
v_mfma_f32_16x16x32_bf16 v[40:43], a[28:31], a[108:111], v[40:43]
v_exp_f32 v28, v28
v_mfma_f32_16x16x32_bf16 v[44:47], a[28:31], a[124:127], v[44:47]
ds_read_b128 a[12:15], v3 offset:3072
ds_read_b128 a[28:31], v3 offset:7168
v_mfma_f32_16x16x32_bf16 v[192:195], a[32:35], v[56:59], v[192:195]
v_exp_f32 v29, v29
v_mfma_f32_16x16x32_bf16 v[224:227], a[32:35], v[60:63], v[224:227]
ds_read_b128 a[32:35], v2 offset:16384
v_cvt_pk_bf16_f32 v54, v28, v29
v_mfma_f32_16x16x32_bf16 v[196:199], a[36:39], v[56:59], v[196:199]
v_exp_f32 v30, v30
v_mfma_f32_16x16x32_bf16 v[228:231], a[36:39], v[60:63], v[228:231]
ds_read_b128 a[36:39], v2 offset:17408
v_mfma_f32_16x16x32_bf16 v[200:203], a[40:43], v[56:59], v[200:203]
v_exp_f32 v31, v31
v_mfma_f32_16x16x32_bf16 v[232:235], a[40:43], v[60:63], v[232:235]
s_mov_b32 m0, s66
ds_read_b128 a[40:43], v2 offset:18432
v_cvt_pk_bf16_f32 v55, v30, v31
v_mfma_f32_16x16x32_bf16 v[204:207], a[44:47], v[56:59], v[204:207]
buffer_load_dwordx4 v12, s[44:47], s64 offen offset:0 sc1 lds
v_mfma_f32_16x16x32_bf16 v[236:239], a[44:47], v[60:63], v[236:239]
ds_read_b128 a[44:47], v2 offset:19456
v_mfma_f32_16x16x32_bf16 v[208:211], a[48:51], v[56:59], v[208:211]
v_exp_f32 v32, v32
v_mfma_f32_16x16x32_bf16 v[240:243], a[48:51], v[60:63], v[240:243]
buffer_load_dwordx4 v12, s[44:47], s64 offen offset:1024 sc1 lds
v_mfma_f32_16x16x32_bf16 v[212:215], a[52:55], v[56:59], v[212:215]
v_exp_f32 v33, v33
v_mfma_f32_16x16x32_bf16 v[244:247], a[52:55], v[60:63], v[244:247]
ds_read_b128 a[48:51], v2 offset:20480
ds_read_b128 a[52:55], v2 offset:21504
v_mfma_f32_16x16x32_bf16 v[216:219], a[56:59], v[56:59], v[216:219]
v_exp_f32 v34, v34
v_mfma_f32_16x16x32_bf16 v[248:251], a[56:59], v[60:63], v[248:251]
buffer_load_dwordx4 v12, s[44:47], s64 offen offset:2048 sc1 lds
v_mfma_f32_16x16x32_bf16 v[220:223], a[60:63], v[56:59], v[220:223]
v_exp_f32 v35, v35
v_mfma_f32_16x16x32_bf16 v[252:255], a[60:63], v[60:63], v[252:255]
ds_read_b128 a[56:59], v2 offset:22528
ds_read_b128 a[60:63], v2 offset:23552
v_mfma_f32_16x16x32_bf16 v[72:75], a[128:131], v[56:59], v[72:75]
v_exp_f32 v36, v36
v_mfma_f32_16x16x32_bf16 v[76:79], a[128:131], v[60:63], v[76:79]
s_waitcnt lgkmcnt(2)
buffer_load_dwordx4 v12, s[44:47], s64 offen offset:3072 sc1 lds
v_mfma_f32_16x16x32_bf16 v[16:19], a[0:3], a[96:99], v[96:99]
v_exp_f32 v37, v37
v_mfma_f32_16x16x32_bf16 v[20:23], a[0:3], a[112:115], v[100:103]
ds_read_b128 a[0:3], v3 offset:8192
v_cvt_pk_bf16_f32 v56, v32, v33
v_mfma_f32_16x16x32_bf16 v[24:27], a[16:19], a[96:99], v[104:107]
v_exp_f32 v38, v38
v_mfma_f32_16x16x32_bf16 v[28:31], a[16:19], a[112:115], v[108:111]
ds_read_b128 a[16:19], v3 offset:12288
v_cvt_pk_bf16_f32 v57, v34, v35
v_mfma_f32_16x16x32_bf16 v[16:19], a[4:7], a[100:103], v[16:19]
v_exp_f32 v39, v39
v_mfma_f32_16x16x32_bf16 v[20:23], a[4:7], a[116:119], v[20:23]
ds_read_b128 a[4:7], v3 offset:9216
v_cvt_pk_bf16_f32 v60, v36, v37
v_mfma_f32_16x16x32_bf16 v[24:27], a[20:23], a[100:103], v[24:27]
v_exp_f32 v40, v40
v_mfma_f32_16x16x32_bf16 v[28:31], a[20:23], a[116:119], v[28:31]
ds_read_b128 a[20:23], v3 offset:13312
v_cvt_pk_bf16_f32 v61, v38, v39
v_mfma_f32_16x16x32_bf16 v[16:19], a[8:11], a[104:107], v[16:19]
v_exp_f32 v41, v41
v_mfma_f32_16x16x32_bf16 v[20:23], a[8:11], a[120:123], v[20:23]
ds_read_b128 a[8:11], v3 offset:10240
v_cvt_pk_bf16_f32 v58, v40, v41
v_mfma_f32_16x16x32_bf16 v[24:27], a[24:27], a[104:107], v[24:27]
v_exp_f32 v42, v42
v_mfma_f32_16x16x32_bf16 v[28:31], a[24:27], a[120:123], v[28:31]
ds_read_b128 a[24:27], v3 offset:14336
v_mfma_f32_16x16x32_bf16 v[16:19], a[12:15], a[108:111], v[16:19]
s_add_u32 m0, s66, 4096
v_exp_f32 v43, v43
v_mfma_f32_16x16x32_bf16 v[20:23], a[12:15], a[124:127], v[20:23]
buffer_load_dwordx4 v12, s[44:47], s65 offen offset:0 sc1 lds
v_mfma_f32_16x16x32_bf16 v[24:27], a[28:31], a[108:111], v[24:27]
v_exp_f32 v44, v44
v_mfma_f32_16x16x32_bf16 v[28:31], a[28:31], a[124:127], v[28:31]
ds_read_b128 a[12:15], v3 offset:11264
ds_read_b128 a[28:31], v3 offset:15360
v_mfma_f32_16x16x32_bf16 v[192:195], a[32:35], v[48:51], v[192:195]
v_exp_f32 v45, v45
v_mfma_f32_16x16x32_bf16 v[224:227], a[32:35], v[52:55], v[224:227]
ds_read_b128 a[32:35], v2 offset:24576
v_cvt_pk_bf16_f32 v62, v44, v45
v_mfma_f32_16x16x32_bf16 v[196:199], a[36:39], v[48:51], v[196:199]
v_exp_f32 v46, v46
v_mfma_f32_16x16x32_bf16 v[228:231], a[36:39], v[52:55], v[228:231]
ds_read_b128 a[36:39], v2 offset:25600
v_cvt_pk_bf16_f32 v59, v42, v43
v_mfma_f32_16x16x32_bf16 v[200:203], a[40:43], v[48:51], v[200:203]
v_exp_f32 v47, v47
v_mfma_f32_16x16x32_bf16 v[232:235], a[40:43], v[52:55], v[232:235]
ds_read_b128 a[40:43], v2 offset:26624
v_cvt_pk_bf16_f32 v63, v46, v47
v_mfma_f32_16x16x32_bf16 v[204:207], a[44:47], v[48:51], v[204:207]
buffer_load_dwordx4 v12, s[44:47], s65 offen offset:1024 sc1 lds
v_mfma_f32_16x16x32_bf16 v[236:239], a[44:47], v[52:55], v[236:239]
ds_read_b128 a[44:47], v2 offset:27648
v_mfma_f32_16x16x32_bf16 v[208:211], a[48:51], v[48:51], v[208:211]
v_exp_f32 v16, v16
v_mfma_f32_16x16x32_bf16 v[240:243], a[48:51], v[52:55], v[240:243]
buffer_load_dwordx4 v12, s[44:47], s65 offen offset:2048 sc1 lds
v_mfma_f32_16x16x32_bf16 v[212:215], a[52:55], v[48:51], v[212:215]
v_exp_f32 v17, v17
v_mfma_f32_16x16x32_bf16 v[244:247], a[52:55], v[52:55], v[244:247]
s_waitcnt lgkmcnt(1)
ds_read_b128 a[48:51], v2 offset:28672
ds_read_b128 a[52:55], v2 offset:29696
v_mfma_f32_16x16x32_bf16 v[216:219], a[56:59], v[48:51], v[216:219]
v_exp_f32 v18, v18
v_mfma_f32_16x16x32_bf16 v[248:251], a[56:59], v[52:55], v[248:251]
buffer_load_dwordx4 v12, s[44:47], s65 offen offset:3072 sc1 lds
v_mfma_f32_16x16x32_bf16 v[220:223], a[60:63], v[48:51], v[220:223]
s_add_u32 s64, s64, 32768
v_exp_f32 v19, v19
v_mfma_f32_16x16x32_bf16 v[252:255], a[60:63], v[52:55], v[252:255]
ds_read_b128 a[56:59], v2 offset:30720
ds_read_b128 a[60:63], v2 offset:31744
v_mfma_f32_16x16x32_bf16 v[72:75], a[128:131], v[48:51], v[72:75]
s_add_u32 s65, s65, 32768
v_exp_f32 v20, v20
v_mfma_f32_16x16x32_bf16 v[76:79], a[128:131], v[52:55], v[76:79]
s_add_u32 s66, s66, 32768
s_and_b32 s66, s66, 0x1ffff
s_sub_u32 s67, s67, 1
s_cmp_eq_u32 s67, 0
s_cbranch_scc1 L_Uo%=
v_mfma_f32_16x16x32_bf16 v[32:35], a[0:3], a[96:99], v[96:99]
v_exp_f32 v21, v21
v_mfma_f32_16x16x32_bf16 v[36:39], a[0:3], a[112:115], v[100:103]
v_cvt_pk_bf16_f32 v48, v16, v17
v_cvt_pk_bf16_f32 v49, v18, v19
v_mfma_f32_16x16x32_bf16 v[40:43], a[16:19], a[96:99], v[104:107]
v_exp_f32 v22, v22
v_mfma_f32_16x16x32_bf16 v[44:47], a[16:19], a[112:115], v[108:111]
s_waitcnt vmcnt(8) lgkmcnt(0)
s_barrier
v_xor_b32 v2, 0x10000, v2
v_mfma_f32_16x16x32_bf16 v[32:35], a[4:7], a[100:103], v[32:35]
v_exp_f32 v23, v23
v_mfma_f32_16x16x32_bf16 v[36:39], a[4:7], a[116:119], v[36:39]
ds_read_b128 a[0:3], v2 offset:0
v_cvt_pk_bf16_f32 v52, v20, v21
v_mfma_f32_16x16x32_bf16 v[40:43], a[20:23], a[100:103], v[40:43]
v_exp_f32 v24, v24
v_mfma_f32_16x16x32_bf16 v[44:47], a[20:23], a[116:119], v[44:47]
ds_read_b128 a[16:19], v2 offset:4096
v_cvt_pk_bf16_f32 v53, v22, v23
v_mfma_f32_16x16x32_bf16 v[32:35], a[8:11], a[104:107], v[32:35]
v_exp_f32 v25, v25
v_mfma_f32_16x16x32_bf16 v[36:39], a[8:11], a[120:123], v[36:39]
ds_read_b128 a[4:7], v2 offset:1024
v_cvt_pk_bf16_f32 v50, v24, v25
v_mfma_f32_16x16x32_bf16 v[40:43], a[24:27], a[104:107], v[40:43]
v_exp_f32 v26, v26
v_mfma_f32_16x16x32_bf16 v[44:47], a[24:27], a[120:123], v[44:47]
ds_read_b128 a[20:23], v2 offset:5120
ds_read_b128 a[8:11], v2 offset:2048
v_mfma_f32_16x16x32_bf16 v[32:35], a[12:15], a[108:111], v[32:35]
v_exp_f32 v27, v27
v_mfma_f32_16x16x32_bf16 v[36:39], a[12:15], a[124:127], v[36:39]
ds_read_b128 a[24:27], v2 offset:6144
v_cvt_pk_bf16_f32 v51, v26, v27
v_mfma_f32_16x16x32_bf16 v[40:43], a[28:31], a[108:111], v[40:43]
v_exp_f32 v28, v28
v_mfma_f32_16x16x32_bf16 v[44:47], a[28:31], a[124:127], v[44:47]
ds_read_b128 a[12:15], v2 offset:3072
ds_read_b128 a[28:31], v2 offset:7168
v_mfma_f32_16x16x32_bf16 v[192:195], a[32:35], v[56:59], v[192:195]
v_exp_f32 v29, v29
v_mfma_f32_16x16x32_bf16 v[224:227], a[32:35], v[60:63], v[224:227]
ds_read_b128 a[32:35], v3 offset:16384
v_cvt_pk_bf16_f32 v54, v28, v29
v_mfma_f32_16x16x32_bf16 v[196:199], a[36:39], v[56:59], v[196:199]
v_exp_f32 v30, v30
v_mfma_f32_16x16x32_bf16 v[228:231], a[36:39], v[60:63], v[228:231]
ds_read_b128 a[36:39], v3 offset:17408
v_mfma_f32_16x16x32_bf16 v[200:203], a[40:43], v[56:59], v[200:203]
v_exp_f32 v31, v31
v_mfma_f32_16x16x32_bf16 v[232:235], a[40:43], v[60:63], v[232:235]
s_mov_b32 m0, s66
ds_read_b128 a[40:43], v3 offset:18432
v_cvt_pk_bf16_f32 v55, v30, v31
v_mfma_f32_16x16x32_bf16 v[204:207], a[44:47], v[56:59], v[204:207]
buffer_load_dwordx4 v12, s[44:47], s64 offen offset:0 sc1 lds
v_mfma_f32_16x16x32_bf16 v[236:239], a[44:47], v[60:63], v[236:239]
ds_read_b128 a[44:47], v3 offset:19456
v_mfma_f32_16x16x32_bf16 v[208:211], a[48:51], v[56:59], v[208:211]
v_exp_f32 v32, v32
v_mfma_f32_16x16x32_bf16 v[240:243], a[48:51], v[60:63], v[240:243]
buffer_load_dwordx4 v12, s[44:47], s64 offen offset:1024 sc1 lds
v_mfma_f32_16x16x32_bf16 v[212:215], a[52:55], v[56:59], v[212:215]
v_exp_f32 v33, v33
v_mfma_f32_16x16x32_bf16 v[244:247], a[52:55], v[60:63], v[244:247]
ds_read_b128 a[48:51], v3 offset:20480
ds_read_b128 a[52:55], v3 offset:21504
v_mfma_f32_16x16x32_bf16 v[216:219], a[56:59], v[56:59], v[216:219]
v_exp_f32 v34, v34
v_mfma_f32_16x16x32_bf16 v[248:251], a[56:59], v[60:63], v[248:251]
buffer_load_dwordx4 v12, s[44:47], s64 offen offset:2048 sc1 lds
v_mfma_f32_16x16x32_bf16 v[220:223], a[60:63], v[56:59], v[220:223]
v_exp_f32 v35, v35
v_mfma_f32_16x16x32_bf16 v[252:255], a[60:63], v[60:63], v[252:255]
ds_read_b128 a[56:59], v3 offset:22528
ds_read_b128 a[60:63], v3 offset:23552
v_mfma_f32_16x16x32_bf16 v[72:75], a[128:131], v[56:59], v[72:75]
v_exp_f32 v36, v36
v_mfma_f32_16x16x32_bf16 v[76:79], a[128:131], v[60:63], v[76:79]
s_waitcnt lgkmcnt(2)
buffer_load_dwordx4 v12, s[44:47], s64 offen offset:3072 sc1 lds
v_mfma_f32_16x16x32_bf16 v[16:19], a[0:3], a[96:99], v[96:99]
v_exp_f32 v37, v37
v_mfma_f32_16x16x32_bf16 v[20:23], a[0:3], a[112:115], v[100:103]
ds_read_b128 a[0:3], v2 offset:8192
v_cvt_pk_bf16_f32 v56, v32, v33
v_mfma_f32_16x16x32_bf16 v[24:27], a[16:19], a[96:99], v[104:107]
v_exp_f32 v38, v38
v_mfma_f32_16x16x32_bf16 v[28:31], a[16:19], a[112:115], v[108:111]
ds_read_b128 a[16:19], v2 offset:12288
v_cvt_pk_bf16_f32 v57, v34, v35
v_mfma_f32_16x16x32_bf16 v[16:19], a[4:7], a[100:103], v[16:19]
v_exp_f32 v39, v39
v_mfma_f32_16x16x32_bf16 v[20:23], a[4:7], a[116:119], v[20:23]
ds_read_b128 a[4:7], v2 offset:9216
v_cvt_pk_bf16_f32 v60, v36, v37
v_mfma_f32_16x16x32_bf16 v[24:27], a[20:23], a[100:103], v[24:27]
v_exp_f32 v40, v40
v_mfma_f32_16x16x32_bf16 v[28:31], a[20:23], a[116:119], v[28:31]
ds_read_b128 a[20:23], v2 offset:13312
v_cvt_pk_bf16_f32 v61, v38, v39
v_mfma_f32_16x16x32_bf16 v[16:19], a[8:11], a[104:107], v[16:19]
v_exp_f32 v41, v41
v_mfma_f32_16x16x32_bf16 v[20:23], a[8:11], a[120:123], v[20:23]
ds_read_b128 a[8:11], v2 offset:10240
v_cvt_pk_bf16_f32 v58, v40, v41
v_mfma_f32_16x16x32_bf16 v[24:27], a[24:27], a[104:107], v[24:27]
v_exp_f32 v42, v42
v_mfma_f32_16x16x32_bf16 v[28:31], a[24:27], a[120:123], v[28:31]
ds_read_b128 a[24:27], v2 offset:14336
v_mfma_f32_16x16x32_bf16 v[16:19], a[12:15], a[108:111], v[16:19]
s_add_u32 m0, s66, 4096
v_exp_f32 v43, v43
v_mfma_f32_16x16x32_bf16 v[20:23], a[12:15], a[124:127], v[20:23]
buffer_load_dwordx4 v12, s[44:47], s65 offen offset:0 sc1 lds
v_mfma_f32_16x16x32_bf16 v[24:27], a[28:31], a[108:111], v[24:27]
v_exp_f32 v44, v44
v_mfma_f32_16x16x32_bf16 v[28:31], a[28:31], a[124:127], v[28:31]
ds_read_b128 a[12:15], v2 offset:11264
ds_read_b128 a[28:31], v2 offset:15360
v_mfma_f32_16x16x32_bf16 v[192:195], a[32:35], v[48:51], v[192:195]
v_exp_f32 v45, v45
v_mfma_f32_16x16x32_bf16 v[224:227], a[32:35], v[52:55], v[224:227]
ds_read_b128 a[32:35], v3 offset:24576
v_cvt_pk_bf16_f32 v62, v44, v45
v_mfma_f32_16x16x32_bf16 v[196:199], a[36:39], v[48:51], v[196:199]
v_exp_f32 v46, v46
v_mfma_f32_16x16x32_bf16 v[228:231], a[36:39], v[52:55], v[228:231]
ds_read_b128 a[36:39], v3 offset:25600
v_cvt_pk_bf16_f32 v59, v42, v43
v_mfma_f32_16x16x32_bf16 v[200:203], a[40:43], v[48:51], v[200:203]
v_exp_f32 v47, v47
v_mfma_f32_16x16x32_bf16 v[232:235], a[40:43], v[52:55], v[232:235]
ds_read_b128 a[40:43], v3 offset:26624
v_cvt_pk_bf16_f32 v63, v46, v47
v_mfma_f32_16x16x32_bf16 v[204:207], a[44:47], v[48:51], v[204:207]
buffer_load_dwordx4 v12, s[44:47], s65 offen offset:1024 sc1 lds
v_mfma_f32_16x16x32_bf16 v[236:239], a[44:47], v[52:55], v[236:239]
ds_read_b128 a[44:47], v3 offset:27648
v_mfma_f32_16x16x32_bf16 v[208:211], a[48:51], v[48:51], v[208:211]
v_exp_f32 v16, v16
v_mfma_f32_16x16x32_bf16 v[240:243], a[48:51], v[52:55], v[240:243]
buffer_load_dwordx4 v12, s[44:47], s65 offen offset:2048 sc1 lds
v_mfma_f32_16x16x32_bf16 v[212:215], a[52:55], v[48:51], v[212:215]
v_exp_f32 v17, v17
v_mfma_f32_16x16x32_bf16 v[244:247], a[52:55], v[52:55], v[244:247]
s_waitcnt lgkmcnt(1)
ds_read_b128 a[48:51], v3 offset:28672
ds_read_b128 a[52:55], v3 offset:29696
v_mfma_f32_16x16x32_bf16 v[216:219], a[56:59], v[48:51], v[216:219]
v_exp_f32 v18, v18
v_mfma_f32_16x16x32_bf16 v[248:251], a[56:59], v[52:55], v[248:251]
buffer_load_dwordx4 v12, s[44:47], s65 offen offset:3072 sc1 lds
)ASM"
R"ASM(v_mfma_f32_16x16x32_bf16 v[220:223], a[60:63], v[48:51], v[220:223]
s_add_u32 s64, s64, 32768
v_exp_f32 v19, v19
v_mfma_f32_16x16x32_bf16 v[252:255], a[60:63], v[52:55], v[252:255]
ds_read_b128 a[56:59], v3 offset:30720
ds_read_b128 a[60:63], v3 offset:31744
v_mfma_f32_16x16x32_bf16 v[72:75], a[128:131], v[48:51], v[72:75]
s_add_u32 s65, s65, 32768
v_exp_f32 v20, v20
v_mfma_f32_16x16x32_bf16 v[76:79], a[128:131], v[52:55], v[76:79]
s_add_u32 s66, s66, 32768
s_and_b32 s66, s66, 0x1ffff
s_branch L_S%=
L_Uo%=:
s_lshl_b32 s69, s39, 5
s_sub_i32 s69, 0, s69
s_sub_i32 s69, s69, 32
v_mfma_f32_16x16x32_bf16 v[32:35], a[0:3], a[96:99], v[96:99]
s_add_i32 s69, s69, 32
v_exp_f32 v21, v21
v_mfma_f32_16x16x32_bf16 v[36:39], a[0:3], a[112:115], v[100:103]
v_cvt_pk_bf16_f32 v48, v16, v17
v_cvt_pk_bf16_f32 v49, v18, v19
v_mfma_f32_16x16x32_bf16 v[40:43], a[16:19], a[96:99], v[104:107]
s_cmp_eq_i32 s69, 0
v_exp_f32 v22, v22
v_mfma_f32_16x16x32_bf16 v[44:47], a[16:19], a[112:115], v[108:111]
s_waitcnt vmcnt(8) lgkmcnt(0)
s_barrier
v_xor_b32 v2, 0x10000, v2
v_mfma_f32_16x16x32_bf16 v[32:35], a[4:7], a[100:103], v[32:35]
s_cselect_b32 s74, 1.0, 0
v_exp_f32 v23, v23
v_mfma_f32_16x16x32_bf16 v[36:39], a[4:7], a[116:119], v[36:39]
ds_read_b128 a[0:3], v2 offset:0
v_cvt_pk_bf16_f32 v52, v20, v21
v_mfma_f32_16x16x32_bf16 v[40:43], a[20:23], a[100:103], v[40:43]
s_cmp_gt_i32 s69, 0
v_exp_f32 v24, v24
v_mfma_f32_16x16x32_bf16 v[44:47], a[20:23], a[116:119], v[44:47]
ds_read_b128 a[16:19], v2 offset:4096
v_cvt_pk_bf16_f32 v53, v22, v23
v_mfma_f32_16x16x32_bf16 v[32:35], a[8:11], a[104:107], v[32:35]
s_cselect_b32 s75, s72, 0
v_exp_f32 v25, v25
v_mfma_f32_16x16x32_bf16 v[36:39], a[8:11], a[120:123], v[36:39]
ds_read_b128 a[4:7], v2 offset:1024
v_cvt_pk_bf16_f32 v50, v24, v25
v_add_f32 v10, s75, v8
v_add_f32 v11, s75, v9
v_mfma_f32_16x16x32_bf16 v[40:43], a[24:27], a[104:107], v[40:43]
v_exp_f32 v26, v26
v_mfma_f32_16x16x32_bf16 v[44:47], a[24:27], a[120:123], v[44:47]
ds_read_b128 a[20:23], v2 offset:5120
ds_read_b128 a[8:11], v2 offset:2048
v_fma_f32 v96, s74, v112, v10
v_fma_f32 v97, s74, v113, v10
v_mfma_f32_16x16x32_bf16 v[32:35], a[12:15], a[108:111], v[32:35]
v_exp_f32 v27, v27
v_mfma_f32_16x16x32_bf16 v[36:39], a[12:15], a[124:127], v[36:39]
ds_read_b128 a[24:27], v2 offset:6144
v_cvt_pk_bf16_f32 v51, v26, v27
v_fma_f32 v98, s74, v114, v10
v_fma_f32 v99, s74, v115, v10
v_mfma_f32_16x16x32_bf16 v[40:43], a[28:31], a[108:111], v[40:43]
v_exp_f32 v28, v28
v_mfma_f32_16x16x32_bf16 v[44:47], a[28:31], a[124:127], v[44:47]
ds_read_b128 a[12:15], v2 offset:3072
ds_read_b128 a[28:31], v2 offset:7168
v_fma_f32 v100, s74, v116, v11
v_fma_f32 v101, s74, v117, v11
v_mfma_f32_16x16x32_bf16 v[192:195], a[32:35], v[56:59], v[192:195]
v_exp_f32 v29, v29
v_mfma_f32_16x16x32_bf16 v[224:227], a[32:35], v[60:63], v[224:227]
ds_read_b128 a[32:35], v3 offset:16384
v_cvt_pk_bf16_f32 v54, v28, v29
v_fma_f32 v102, s74, v118, v11
v_fma_f32 v103, s74, v119, v11
v_mfma_f32_16x16x32_bf16 v[196:199], a[36:39], v[56:59], v[196:199]
v_exp_f32 v30, v30
v_mfma_f32_16x16x32_bf16 v[228:231], a[36:39], v[60:63], v[228:231]
ds_read_b128 a[36:39], v3 offset:17408
v_fma_f32 v104, s74, v120, v10
v_fma_f32 v105, s74, v121, v10
v_fma_f32 v106, s74, v122, v10
v_mfma_f32_16x16x32_bf16 v[200:203], a[40:43], v[56:59], v[200:203]
v_exp_f32 v31, v31
v_mfma_f32_16x16x32_bf16 v[232:235], a[40:43], v[60:63], v[232:235]
s_mov_b32 m0, s66
ds_read_b128 a[40:43], v3 offset:18432
v_cvt_pk_bf16_f32 v55, v30, v31
v_fma_f32 v107, s74, v123, v10
v_mfma_f32_16x16x32_bf16 v[204:207], a[44:47], v[56:59], v[204:207]
buffer_load_dwordx4 v12, s[44:47], s64 offen offset:0 sc1 lds
v_mfma_f32_16x16x32_bf16 v[236:239], a[44:47], v[60:63], v[236:239]
ds_read_b128 a[44:47], v3 offset:19456
v_fma_f32 v108, s74, v124, v11
v_fma_f32 v109, s74, v125, v11
v_mfma_f32_16x16x32_bf16 v[208:211], a[48:51], v[56:59], v[208:211]
v_exp_f32 v32, v32
v_mfma_f32_16x16x32_bf16 v[240:243], a[48:51], v[60:63], v[240:243]
buffer_load_dwordx4 v12, s[44:47], s64 offen offset:1024 sc1 lds
v_mfma_f32_16x16x32_bf16 v[212:215], a[52:55], v[56:59], v[212:215]
v_exp_f32 v33, v33
v_mfma_f32_16x16x32_bf16 v[244:247], a[52:55], v[60:63], v[244:247]
ds_read_b128 a[48:51], v3 offset:20480
ds_read_b128 a[52:55], v3 offset:21504
v_fma_f32 v110, s74, v126, v11
v_mfma_f32_16x16x32_bf16 v[216:219], a[56:59], v[56:59], v[216:219]
v_exp_f32 v34, v34
v_mfma_f32_16x16x32_bf16 v[248:251], a[56:59], v[60:63], v[248:251]
buffer_load_dwordx4 v12, s[44:47], s64 offen offset:2048 sc1 lds
v_mfma_f32_16x16x32_bf16 v[220:223], a[60:63], v[56:59], v[220:223]
v_exp_f32 v35, v35
v_mfma_f32_16x16x32_bf16 v[252:255], a[60:63], v[60:63], v[252:255]
ds_read_b128 a[56:59], v3 offset:22528
ds_read_b128 a[60:63], v3 offset:23552
v_fma_f32 v111, s74, v127, v11
v_mfma_f32_16x16x32_bf16 v[72:75], a[128:131], v[56:59], v[72:75]
v_exp_f32 v36, v36
v_mfma_f32_16x16x32_bf16 v[76:79], a[128:131], v[60:63], v[76:79]
s_waitcnt lgkmcnt(2)
buffer_load_dwordx4 v12, s[44:47], s64 offen offset:3072 sc1 lds
v_mfma_f32_16x16x32_bf16 v[16:19], a[0:3], a[96:99], v[96:99]
s_add_i32 s69, s69, 32
v_exp_f32 v37, v37
v_mfma_f32_16x16x32_bf16 v[20:23], a[0:3], a[112:115], v[100:103]
ds_read_b128 a[0:3], v2 offset:8192
v_cvt_pk_bf16_f32 v56, v32, v33
v_mfma_f32_16x16x32_bf16 v[24:27], a[16:19], a[96:99], v[104:107]
s_cmp_eq_i32 s69, 0
v_exp_f32 v38, v38
v_mfma_f32_16x16x32_bf16 v[28:31], a[16:19], a[112:115], v[108:111]
ds_read_b128 a[16:19], v2 offset:12288
v_cvt_pk_bf16_f32 v57, v34, v35
v_mfma_f32_16x16x32_bf16 v[16:19], a[4:7], a[100:103], v[16:19]
s_cselect_b32 s76, 1.0, 0
v_exp_f32 v39, v39
v_mfma_f32_16x16x32_bf16 v[20:23], a[4:7], a[116:119], v[20:23]
ds_read_b128 a[4:7], v2 offset:9216
v_cvt_pk_bf16_f32 v60, v36, v37
v_mfma_f32_16x16x32_bf16 v[24:27], a[20:23], a[100:103], v[24:27]
s_cmp_gt_i32 s69, 0
v_exp_f32 v40, v40
v_mfma_f32_16x16x32_bf16 v[28:31], a[20:23], a[116:119], v[28:31]
ds_read_b128 a[20:23], v2 offset:13312
v_cvt_pk_bf16_f32 v61, v38, v39
v_mfma_f32_16x16x32_bf16 v[16:19], a[8:11], a[104:107], v[16:19]
s_cselect_b32 s77, s72, 0
v_exp_f32 v41, v41
v_mfma_f32_16x16x32_bf16 v[20:23], a[8:11], a[120:123], v[20:23]
ds_read_b128 a[8:11], v2 offset:10240
v_cvt_pk_bf16_f32 v58, v40, v41
v_add_f32 v10, s77, v8
v_add_f32 v11, s77, v9
v_mfma_f32_16x16x32_bf16 v[24:27], a[24:27], a[104:107], v[24:27]
v_exp_f32 v42, v42
v_mfma_f32_16x16x32_bf16 v[28:31], a[24:27], a[120:123], v[28:31]
ds_read_b128 a[24:27], v2 offset:14336
v_fma_f32 v96, s76, v112, v10
v_fma_f32 v97, s76, v113, v10
v_fma_f32 v98, s76, v114, v10
v_mfma_f32_16x16x32_bf16 v[16:19], a[12:15], a[108:111], v[16:19]
s_add_u32 m0, s66, 4096
v_exp_f32 v43, v43
v_mfma_f32_16x16x32_bf16 v[20:23], a[12:15], a[124:127], v[20:23]
buffer_load_dwordx4 v12, s[44:47], s65 offen offset:0 sc1 lds
v_mfma_f32_16x16x32_bf16 v[24:27], a[28:31], a[108:111], v[24:27]
v_exp_f32 v44, v44
v_mfma_f32_16x16x32_bf16 v[28:31], a[28:31], a[124:127], v[28:31]
ds_read_b128 a[12:15], v2 offset:11264
ds_read_b128 a[28:31], v2 offset:15360
v_fma_f32 v99, s76, v115, v10
v_fma_f32 v100, s76, v116, v11
v_mfma_f32_16x16x32_bf16 v[192:195], a[32:35], v[48:51], v[192:195]
v_exp_f32 v45, v45
v_mfma_f32_16x16x32_bf16 v[224:227], a[32:35], v[52:55], v[224:227]
ds_read_b128 a[32:35], v3 offset:24576
v_cvt_pk_bf16_f32 v62, v44, v45
v_fma_f32 v101, s76, v117, v11
v_fma_f32 v102, s76, v118, v11
v_mfma_f32_16x16x32_bf16 v[196:199], a[36:39], v[48:51], v[196:199]
v_exp_f32 v46, v46
v_mfma_f32_16x16x32_bf16 v[228:231], a[36:39], v[52:55], v[228:231]
ds_read_b128 a[36:39], v3 offset:25600
v_cvt_pk_bf16_f32 v59, v42, v43
v_fma_f32 v103, s76, v119, v11
v_fma_f32 v104, s76, v120, v10
v_mfma_f32_16x16x32_bf16 v[200:203], a[40:43], v[48:51], v[200:203]
v_exp_f32 v47, v47
v_mfma_f32_16x16x32_bf16 v[232:235], a[40:43], v[52:55], v[232:235]
ds_read_b128 a[40:43], v3 offset:26624
v_cvt_pk_bf16_f32 v63, v46, v47
v_fma_f32 v105, s76, v121, v10
v_mfma_f32_16x16x32_bf16 v[204:207], a[44:47], v[48:51], v[204:207]
buffer_load_dwordx4 v12, s[44:47], s65 offen offset:1024 sc1 lds
v_mfma_f32_16x16x32_bf16 v[236:239], a[44:47], v[52:55], v[236:239]
ds_read_b128 a[44:47], v3 offset:27648
v_fma_f32 v106, s76, v122, v10
v_fma_f32 v107, s76, v123, v10
v_mfma_f32_16x16x32_bf16 v[208:211], a[48:51], v[48:51], v[208:211]
v_exp_f32 v16, v16
v_mfma_f32_16x16x32_bf16 v[240:243], a[48:51], v[52:55], v[240:243]
buffer_load_dwordx4 v12, s[44:47], s65 offen offset:2048 sc1 lds
v_mfma_f32_16x16x32_bf16 v[212:215], a[52:55], v[48:51], v[212:215]
v_exp_f32 v17, v17
v_mfma_f32_16x16x32_bf16 v[244:247], a[52:55], v[52:55], v[244:247]
s_waitcnt lgkmcnt(0)
ds_read_b128 a[48:51], v3 offset:28672
ds_read_b128 a[52:55], v3 offset:29696
v_fma_f32 v108, s76, v124, v11
v_mfma_f32_16x16x32_bf16 v[216:219], a[56:59], v[48:51], v[216:219]
v_exp_f32 v18, v18
v_mfma_f32_16x16x32_bf16 v[248:251], a[56:59], v[52:55], v[248:251]
buffer_load_dwordx4 v12, s[44:47], s65 offen offset:3072 sc1 lds
v_mfma_f32_16x16x32_bf16 v[220:223], a[60:63], v[48:51], v[220:223]
s_add_u32 s64, s64, 32768
v_exp_f32 v19, v19
v_mfma_f32_16x16x32_bf16 v[252:255], a[60:63], v[52:55], v[252:255]
ds_read_b128 a[56:59], v3 offset:30720
ds_read_b128 a[60:63], v3 offset:31744
v_fma_f32 v109, s76, v125, v11
v_mfma_f32_16x16x32_bf16 v[72:75], a[128:131], v[48:51], v[72:75]
s_add_u32 s65, s65, 32768
v_exp_f32 v20, v20
v_mfma_f32_16x16x32_bf16 v[76:79], a[128:131], v[52:55], v[76:79]
s_add_u32 s66, s66, 32768
s_and_b32 s66, s66, 0x1ffff
v_fma_f32 v110, s76, v126, v11
v_fma_f32 v111, s76, v127, v11
L_Ue%=:
v_mfma_f32_16x16x32_bf16 v[32:35], a[0:3], a[96:99], v[96:99]
s_add_i32 s69, s69, 32
v_exp_f32 v21, v21
v_mfma_f32_16x16x32_bf16 v[36:39], a[0:3], a[112:115], v[100:103]
v_cvt_pk_bf16_f32 v48, v16, v17
v_cvt_pk_bf16_f32 v49, v18, v19
v_mfma_f32_16x16x32_bf16 v[40:43], a[16:19], a[96:99], v[104:107]
s_cmp_eq_i32 s69, 0
v_exp_f32 v22, v22
v_mfma_f32_16x16x32_bf16 v[44:47], a[16:19], a[112:115], v[108:111]
s_waitcnt vmcnt(8) lgkmcnt(0)
s_barrier
v_xor_b32 v3, 0x10000, v3
v_mfma_f32_16x16x32_bf16 v[32:35], a[4:7], a[100:103], v[32:35]
s_cselect_b32 s74, 1.0, 0
v_exp_f32 v23, v23
v_mfma_f32_16x16x32_bf16 v[36:39], a[4:7], a[116:119], v[36:39]
ds_read_b128 a[0:3], v3 offset:0
v_cvt_pk_bf16_f32 v52, v20, v21
v_mfma_f32_16x16x32_bf16 v[40:43], a[20:23], a[100:103], v[40:43]
s_cmp_gt_i32 s69, 0
s_mov_b32 s63, 28672
buffer_load_dwordx4 v[128:131], v1, s[52:55], s63 offen offset:0 sc1
v_exp_f32 v24, v24
v_mfma_f32_16x16x32_bf16 v[44:47], a[20:23], a[116:119], v[44:47]
ds_read_b128 a[16:19], v3 offset:4096
v_cvt_pk_bf16_f32 v53, v22, v23
v_mfma_f32_16x16x32_bf16 v[32:35], a[8:11], a[104:107], v[32:35]
s_cselect_b32 s75, s72, 0
v_exp_f32 v25, v25
v_mfma_f32_16x16x32_bf16 v[36:39], a[8:11], a[120:123], v[36:39]
ds_read_b128 a[4:7], v3 offset:1024
v_cvt_pk_bf16_f32 v50, v24, v25
v_add_f32 v10, s75, v8
v_mfma_f32_16x16x32_bf16 v[40:43], a[24:27], a[104:107], v[40:43]
buffer_load_dwordx4 v[132:135], v1, s[52:55], s63 offen offset:1024 sc1
v_exp_f32 v26, v26
v_mfma_f32_16x16x32_bf16 v[44:47], a[24:27], a[120:123], v[44:47]
ds_read_b128 a[20:23], v3 offset:5120
ds_read_b128 a[8:11], v3 offset:2048
v_add_f32 v11, s75, v9
v_mfma_f32_16x16x32_bf16 v[32:35], a[12:15], a[108:111], v[32:35]
v_exp_f32 v27, v27
v_mfma_f32_16x16x32_bf16 v[36:39], a[12:15], a[124:127], v[36:39]
ds_read_b128 a[24:27], v3 offset:6144
v_cvt_pk_bf16_f32 v51, v26, v27
v_fma_f32 v96, s74, v112, v10
v_mfma_f32_16x16x32_bf16 v[40:43], a[28:31], a[108:111], v[40:43]
buffer_load_dwordx4 v[136:139], v1, s[52:55], s63 offen offset:2048 sc1
v_exp_f32 v28, v28
v_mfma_f32_16x16x32_bf16 v[44:47], a[28:31], a[124:127], v[44:47]
ds_read_b128 a[12:15], v3 offset:3072
ds_read_b128 a[28:31], v3 offset:7168
v_fma_f32 v97, s74, v113, v10
v_mfma_f32_16x16x32_bf16 v[192:195], a[32:35], v[56:59], v[192:195]
buffer_load_dwordx4 v[140:143], v1, s[52:55], s63 offen offset:3072 sc1
v_exp_f32 v29, v29
v_mfma_f32_16x16x32_bf16 v[224:227], a[32:35], v[60:63], v[224:227]
ds_read_b128 a[32:35], v2 offset:16384
v_cvt_pk_bf16_f32 v54, v28, v29
v_fma_f32 v98, s74, v114, v10
v_mfma_f32_16x16x32_bf16 v[196:199], a[36:39], v[56:59], v[196:199]
v_exp_f32 v30, v30
v_mfma_f32_16x16x32_bf16 v[228:231], a[36:39], v[60:63], v[228:231]
ds_read_b128 a[36:39], v2 offset:17408
v_fma_f32 v99, s74, v115, v10
v_fma_f32 v100, s74, v116, v11
v_mfma_f32_16x16x32_bf16 v[200:203], a[40:43], v[56:59], v[200:203]
s_mov_b32 s63, 32768
)ASM"
R"ASM(buffer_load_dwordx4 v[144:147], v1, s[52:55], s63 offen offset:0 sc1
v_exp_f32 v31, v31
v_mfma_f32_16x16x32_bf16 v[232:235], a[40:43], v[60:63], v[232:235]
ds_read_b128 a[40:43], v2 offset:18432
v_cvt_pk_bf16_f32 v55, v30, v31
v_fma_f32 v101, s74, v117, v11
v_mfma_f32_16x16x32_bf16 v[204:207], a[44:47], v[56:59], v[204:207]
v_fma_f32 v102, s74, v118, v11
v_fma_f32 v103, s74, v119, v11
v_fma_f32 v104, s74, v120, v10
v_mfma_f32_16x16x32_bf16 v[236:239], a[44:47], v[60:63], v[236:239]
ds_read_b128 a[44:47], v2 offset:19456
buffer_load_dwordx4 v[148:151], v1, s[52:55], s63 offen offset:1024 sc1
v_fma_f32 v105, s74, v121, v10
v_mfma_f32_16x16x32_bf16 v[208:211], a[48:51], v[56:59], v[208:211]
v_exp_f32 v32, v32
v_mfma_f32_16x16x32_bf16 v[240:243], a[48:51], v[60:63], v[240:243]
v_fma_f32 v106, s74, v122, v10
v_fma_f32 v107, s74, v123, v10
v_mfma_f32_16x16x32_bf16 v[212:215], a[52:55], v[56:59], v[212:215]
v_exp_f32 v33, v33
v_mfma_f32_16x16x32_bf16 v[244:247], a[52:55], v[60:63], v[244:247]
ds_read_b128 a[48:51], v2 offset:20480
ds_read_b128 a[52:55], v2 offset:21504
buffer_load_dwordx4 v[152:155], v1, s[52:55], s63 offen offset:2048 sc1
v_mfma_f32_16x16x32_bf16 v[216:219], a[56:59], v[56:59], v[216:219]
v_exp_f32 v34, v34
v_mfma_f32_16x16x32_bf16 v[248:251], a[56:59], v[60:63], v[248:251]
v_fma_f32 v108, s74, v124, v11
v_fma_f32 v109, s74, v125, v11
v_mfma_f32_16x16x32_bf16 v[220:223], a[60:63], v[56:59], v[220:223]
buffer_load_dwordx4 v[156:159], v1, s[52:55], s63 offen offset:3072 sc1
v_exp_f32 v35, v35
v_mfma_f32_16x16x32_bf16 v[252:255], a[60:63], v[60:63], v[252:255]
ds_read_b128 a[56:59], v2 offset:22528
ds_read_b128 a[60:63], v2 offset:23552
v_mfma_f32_16x16x32_bf16 v[72:75], a[128:131], v[56:59], v[72:75]
v_exp_f32 v36, v36
v_mfma_f32_16x16x32_bf16 v[76:79], a[128:131], v[60:63], v[76:79]
s_waitcnt lgkmcnt(2)
v_fma_f32 v110, s74, v126, v11
v_fma_f32 v111, s74, v127, v11
v_mfma_f32_16x16x32_bf16 v[16:19], a[0:3], a[96:99], v[96:99]
s_add_i32 s69, s69, 32
s_mov_b32 s63, 36864
buffer_load_dwordx4 v[160:163], v1, s[52:55], s63 offen offset:0 sc1
v_exp_f32 v37, v37
v_mfma_f32_16x16x32_bf16 v[20:23], a[0:3], a[112:115], v[100:103]
ds_read_b128 a[0:3], v3 offset:8192
v_cvt_pk_bf16_f32 v56, v32, v33
v_mfma_f32_16x16x32_bf16 v[24:27], a[16:19], a[96:99], v[104:107]
s_cmp_eq_i32 s69, 0
v_exp_f32 v38, v38
v_mfma_f32_16x16x32_bf16 v[28:31], a[16:19], a[112:115], v[108:111]
ds_read_b128 a[16:19], v3 offset:12288
v_cvt_pk_bf16_f32 v57, v34, v35
v_mfma_f32_16x16x32_bf16 v[16:19], a[4:7], a[100:103], v[16:19]
s_cselect_b32 s76, 1.0, 0
buffer_load_dwordx4 v[164:167], v1, s[52:55], s63 offen offset:1024 sc1
v_exp_f32 v39, v39
v_mfma_f32_16x16x32_bf16 v[20:23], a[4:7], a[116:119], v[20:23]
ds_read_b128 a[4:7], v3 offset:9216
v_cvt_pk_bf16_f32 v60, v36, v37
v_mfma_f32_16x16x32_bf16 v[24:27], a[20:23], a[100:103], v[24:27]
s_cmp_gt_i32 s69, 0
v_exp_f32 v40, v40
v_mfma_f32_16x16x32_bf16 v[28:31], a[20:23], a[116:119], v[28:31]
ds_read_b128 a[20:23], v3 offset:13312
v_cvt_pk_bf16_f32 v61, v38, v39
v_mfma_f32_16x16x32_bf16 v[16:19], a[8:11], a[104:107], v[16:19]
s_cselect_b32 s77, s72, 0
buffer_load_dwordx4 v[168:171], v1, s[52:55], s63 offen offset:2048 sc1
v_exp_f32 v41, v41
v_mfma_f32_16x16x32_bf16 v[20:23], a[8:11], a[120:123], v[20:23]
ds_read_b128 a[8:11], v3 offset:10240
v_cvt_pk_bf16_f32 v58, v40, v41
v_add_f32 v10, s77, v8
v_mfma_f32_16x16x32_bf16 v[24:27], a[24:27], a[104:107], v[24:27]
v_exp_f32 v42, v42
v_mfma_f32_16x16x32_bf16 v[28:31], a[24:27], a[120:123], v[28:31]
ds_read_b128 a[24:27], v3 offset:14336
v_add_f32 v11, s77, v9
v_fma_f32 v96, s76, v112, v10
v_mfma_f32_16x16x32_bf16 v[16:19], a[12:15], a[108:111], v[16:19]
buffer_load_dwordx4 v[172:175], v1, s[52:55], s63 offen offset:3072 sc1
v_exp_f32 v43, v43
v_mfma_f32_16x16x32_bf16 v[20:23], a[12:15], a[124:127], v[20:23]
v_fma_f32 v97, s76, v113, v10
v_fma_f32 v98, s76, v114, v10
v_fma_f32 v99, s76, v115, v10
v_mfma_f32_16x16x32_bf16 v[24:27], a[28:31], a[108:111], v[24:27]
v_exp_f32 v44, v44
v_mfma_f32_16x16x32_bf16 v[28:31], a[28:31], a[124:127], v[28:31]
ds_read_b128 a[12:15], v3 offset:11264
ds_read_b128 a[28:31], v3 offset:15360
v_fma_f32 v100, s76, v116, v11
v_mfma_f32_16x16x32_bf16 v[192:195], a[32:35], v[48:51], v[192:195]
v_exp_f32 v45, v45
v_mfma_f32_16x16x32_bf16 v[224:227], a[32:35], v[52:55], v[224:227]
ds_read_b128 a[32:35], v2 offset:24576
v_cvt_pk_bf16_f32 v62, v44, v45
v_fma_f32 v101, s76, v117, v11
v_mfma_f32_16x16x32_bf16 v[196:199], a[36:39], v[48:51], v[196:199]
s_mov_b32 s63, 40960
buffer_load_dwordx4 v[176:179], v1, s[52:55], s63 offen offset:0 sc1
v_exp_f32 v46, v46
v_mfma_f32_16x16x32_bf16 v[228:231], a[36:39], v[52:55], v[228:231]
ds_read_b128 a[36:39], v2 offset:25600
v_cvt_pk_bf16_f32 v59, v42, v43
v_fma_f32 v102, s76, v118, v11
v_mfma_f32_16x16x32_bf16 v[200:203], a[40:43], v[48:51], v[200:203]
v_exp_f32 v47, v47
v_mfma_f32_16x16x32_bf16 v[232:235], a[40:43], v[52:55], v[232:235]
ds_read_b128 a[40:43], v2 offset:26624
buffer_load_dwordx4 v[180:183], v1, s[52:55], s63 offen offset:1024 sc1
v_cvt_pk_bf16_f32 v63, v46, v47
v_mfma_f32_16x16x32_bf16 v[204:207], a[44:47], v[48:51], v[204:207]
v_fma_f32 v103, s76, v119, v11
v_fma_f32 v104, s76, v120, v10
v_mfma_f32_16x16x32_bf16 v[236:239], a[44:47], v[52:55], v[236:239]
ds_read_b128 a[44:47], v2 offset:27648
v_fma_f32 v105, s76, v121, v10
v_mfma_f32_16x16x32_bf16 v[208:211], a[48:51], v[48:51], v[208:211]
v_exp_f32 v16, v16
v_mfma_f32_16x16x32_bf16 v[240:243], a[48:51], v[52:55], v[240:243]
buffer_load_dwordx4 v[184:187], v1, s[52:55], s63 offen offset:2048 sc1
v_fma_f32 v106, s76, v122, v10
v_fma_f32 v107, s76, v123, v10
v_mfma_f32_16x16x32_bf16 v[212:215], a[52:55], v[48:51], v[212:215]
v_exp_f32 v17, v17
v_mfma_f32_16x16x32_bf16 v[244:247], a[52:55], v[52:55], v[244:247]
s_waitcnt lgkmcnt(0)
ds_read_b128 a[48:51], v2 offset:28672
ds_read_b128 a[52:55], v2 offset:29696
v_mfma_f32_16x16x32_bf16 v[216:219], a[56:59], v[48:51], v[216:219]
v_exp_f32 v18, v18
v_mfma_f32_16x16x32_bf16 v[248:251], a[56:59], v[52:55], v[248:251]
buffer_load_dwordx4 v[188:191], v1, s[52:55], s63 offen offset:3072 sc1
v_fma_f32 v108, s76, v124, v11
v_fma_f32 v109, s76, v125, v11
v_mfma_f32_16x16x32_bf16 v[220:223], a[60:63], v[48:51], v[220:223]
v_exp_f32 v19, v19
v_mfma_f32_16x16x32_bf16 v[252:255], a[60:63], v[52:55], v[252:255]
ds_read_b128 a[56:59], v2 offset:30720
ds_read_b128 a[60:63], v2 offset:31744
v_mfma_f32_16x16x32_bf16 v[72:75], a[128:131], v[48:51], v[72:75]
v_exp_f32 v20, v20
v_mfma_f32_16x16x32_bf16 v[76:79], a[128:131], v[52:55], v[76:79]
v_fma_f32 v110, s76, v126, v11
v_fma_f32 v111, s76, v127, v11
v_mfma_f32_16x16x32_bf16 v[32:35], a[0:3], a[96:99], v[96:99]
v_exp_f32 v21, v21
v_mfma_f32_16x16x32_bf16 v[36:39], a[0:3], a[112:115], v[100:103]
v_cvt_pk_bf16_f32 v48, v16, v17
v_mfma_f32_16x16x32_bf16 v[40:43], a[16:19], a[96:99], v[104:107]
v_exp_f32 v22, v22
v_mfma_f32_16x16x32_bf16 v[44:47], a[16:19], a[112:115], v[108:111]
v_cvt_pk_bf16_f32 v49, v18, v19
v_mfma_f32_16x16x32_bf16 v[32:35], a[4:7], a[100:103], v[32:35]
v_exp_f32 v23, v23
v_mfma_f32_16x16x32_bf16 v[36:39], a[4:7], a[116:119], v[36:39]
v_cvt_pk_bf16_f32 v52, v20, v21
v_mfma_f32_16x16x32_bf16 v[40:43], a[20:23], a[100:103], v[40:43]
v_exp_f32 v24, v24
v_mfma_f32_16x16x32_bf16 v[44:47], a[20:23], a[116:119], v[44:47]
v_cvt_pk_bf16_f32 v53, v22, v23
v_mfma_f32_16x16x32_bf16 v[32:35], a[8:11], a[104:107], v[32:35]
v_exp_f32 v25, v25
v_mfma_f32_16x16x32_bf16 v[36:39], a[8:11], a[120:123], v[36:39]
v_cvt_pk_bf16_f32 v50, v24, v25
v_mfma_f32_16x16x32_bf16 v[40:43], a[24:27], a[104:107], v[40:43]
v_exp_f32 v26, v26
v_mfma_f32_16x16x32_bf16 v[44:47], a[24:27], a[120:123], v[44:47]
v_mfma_f32_16x16x32_bf16 v[32:35], a[12:15], a[108:111], v[32:35]
v_exp_f32 v27, v27
v_mfma_f32_16x16x32_bf16 v[36:39], a[12:15], a[124:127], v[36:39]
v_cvt_pk_bf16_f32 v51, v26, v27
v_mfma_f32_16x16x32_bf16 v[40:43], a[28:31], a[108:111], v[40:43]
v_exp_f32 v28, v28
v_mfma_f32_16x16x32_bf16 v[44:47], a[28:31], a[124:127], v[44:47]
v_mfma_f32_16x16x32_bf16 v[192:195], a[32:35], v[56:59], v[192:195]
v_exp_f32 v29, v29
v_mfma_f32_16x16x32_bf16 v[224:227], a[32:35], v[60:63], v[224:227]
ds_read_b128 a[32:35], v3 offset:16384
v_cvt_pk_bf16_f32 v54, v28, v29
v_mfma_f32_16x16x32_bf16 v[196:199], a[36:39], v[56:59], v[196:199]
v_exp_f32 v30, v30
v_mfma_f32_16x16x32_bf16 v[228:231], a[36:39], v[60:63], v[228:231]
ds_read_b128 a[36:39], v3 offset:17408
v_mfma_f32_16x16x32_bf16 v[200:203], a[40:43], v[56:59], v[200:203]
v_exp_f32 v31, v31
v_mfma_f32_16x16x32_bf16 v[232:235], a[40:43], v[60:63], v[232:235]
ds_read_b128 a[40:43], v3 offset:18432
v_cvt_pk_bf16_f32 v55, v30, v31
v_mfma_f32_16x16x32_bf16 v[204:207], a[44:47], v[56:59], v[204:207]
v_mfma_f32_16x16x32_bf16 v[236:239], a[44:47], v[60:63], v[236:239]
s_waitcnt lgkmcnt(2)
ds_read_b128 a[44:47], v3 offset:19456
v_mfma_f32_16x16x32_bf16 v[208:211], a[48:51], v[56:59], v[208:211]
v_exp_f32 v32, v32
v_mfma_f32_16x16x32_bf16 v[240:243], a[48:51], v[60:63], v[240:243]
ds_read_b128 a[48:51], v3 offset:20480
v_mfma_f32_16x16x32_bf16 v[212:215], a[52:55], v[56:59], v[212:215]
v_exp_f32 v33, v33
v_mfma_f32_16x16x32_bf16 v[244:247], a[52:55], v[60:63], v[244:247]
ds_read_b128 a[52:55], v3 offset:21504
v_mfma_f32_16x16x32_bf16 v[216:219], a[56:59], v[56:59], v[216:219]
v_exp_f32 v34, v34
v_mfma_f32_16x16x32_bf16 v[248:251], a[56:59], v[60:63], v[248:251]
ds_read_b128 a[56:59], v3 offset:22528
v_mfma_f32_16x16x32_bf16 v[220:223], a[60:63], v[56:59], v[220:223]
v_exp_f32 v35, v35
v_mfma_f32_16x16x32_bf16 v[252:255], a[60:63], v[60:63], v[252:255]
ds_read_b128 a[60:63], v3 offset:23552
v_mfma_f32_16x16x32_bf16 v[72:75], a[128:131], v[56:59], v[72:75]
v_exp_f32 v36, v36
v_mfma_f32_16x16x32_bf16 v[76:79], a[128:131], v[60:63], v[76:79]
v_mfma_f32_16x16x32_bf16 v[192:195], a[32:35], v[48:51], v[192:195]
v_exp_f32 v37, v37
v_mfma_f32_16x16x32_bf16 v[224:227], a[32:35], v[52:55], v[224:227]
s_waitcnt lgkmcnt(1)
ds_read_b128 a[32:35], v3 offset:24576
v_exp_f32 v38, v38
v_cvt_pk_bf16_f32 v56, v32, v33
v_mfma_f32_16x16x32_bf16 v[196:199], a[36:39], v[48:51], v[196:199]
v_exp_f32 v39, v39
v_mfma_f32_16x16x32_bf16 v[228:231], a[36:39], v[52:55], v[228:231]
ds_read_b128 a[36:39], v3 offset:25600
v_cvt_pk_bf16_f32 v57, v34, v35
v_mfma_f32_16x16x32_bf16 v[200:203], a[40:43], v[48:51], v[200:203]
v_exp_f32 v40, v40
v_cvt_pk_bf16_f32 v60, v36, v37
v_mfma_f32_16x16x32_bf16 v[232:235], a[40:43], v[52:55], v[232:235]
ds_read_b128 a[40:43], v3 offset:26624
v_exp_f32 v41, v41
v_mfma_f32_16x16x32_bf16 v[204:207], a[44:47], v[48:51], v[204:207]
v_cvt_pk_bf16_f32 v61, v38, v39
v_mfma_f32_16x16x32_bf16 v[236:239], a[44:47], v[52:55], v[236:239]
ds_read_b128 a[44:47], v3 offset:27648
v_exp_f32 v42, v42
v_cvt_pk_bf16_f32 v58, v40, v41
v_mfma_f32_16x16x32_bf16 v[208:211], a[48:51], v[48:51], v[208:211]
v_exp_f32 v43, v43
v_mfma_f32_16x16x32_bf16 v[240:243], a[48:51], v[52:55], v[240:243]
ds_read_b128 a[48:51], v3 offset:28672
v_mfma_f32_16x16x32_bf16 v[212:215], a[52:55], v[48:51], v[212:215]
v_exp_f32 v44, v44
v_cvt_pk_bf16_f32 v59, v42, v43
v_mfma_f32_16x16x32_bf16 v[244:247], a[52:55], v[52:55], v[244:247]
ds_read_b128 a[52:55], v3 offset:29696
v_exp_f32 v45, v45
v_mfma_f32_16x16x32_bf16 v[216:219], a[56:59], v[48:51], v[216:219]
v_mfma_f32_16x16x32_bf16 v[248:251], a[56:59], v[52:55], v[248:251]
s_waitcnt lgkmcnt(1)
ds_read_b128 a[56:59], v3 offset:30720
v_exp_f32 v46, v46
v_cvt_pk_bf16_f32 v62, v44, v45
v_mfma_f32_16x16x32_bf16 v[220:223], a[60:63], v[48:51], v[220:223]
v_exp_f32 v47, v47
v_mfma_f32_16x16x32_bf16 v[252:255], a[60:63], v[52:55], v[252:255]
ds_read_b128 a[60:63], v3 offset:31744
v_mfma_f32_16x16x32_bf16 v[72:75], a[128:131], v[48:51], v[72:75]
v_cvt_pk_bf16_f32 v63, v46, v47
v_mfma_f32_16x16x32_bf16 v[76:79], a[128:131], v[52:55], v[76:79]
v_mfma_f32_16x16x32_bf16 v[192:195], a[32:35], v[56:59], v[192:195]
s_and_b32 s62, s38, 7
s_lshr_b32 s63, s38, 3
s_lshr_b32 s65, s63, 2
v_mfma_f32_16x16x32_bf16 v[224:227], a[32:35], v[60:63], v[224:227]
s_and_b32 s63, s63, 3
s_lshl_b32 s62, s62, 2
s_add_u32 s64, s62, s63
v_mfma_f32_16x16x32_bf16 v[196:199], a[36:39], v[56:59], v[196:199]
s_lshl_b32 s67, s39, 12
s_lshl_b32 s68, s39, 15
s_mul_i32 s69, s39, 32768
v_mfma_f32_16x16x32_bf16 v[228:231], a[36:39], v[60:63], v[228:231]
s_add_u32 s48, s30, 35651584
s_addc_u32 s49, s31, 0
s_and_b32 s49, s49, 0xffff
v_mfma_f32_16x16x32_bf16 v[200:203], a[40:43], v[56:59], v[200:203]
s_mov_b32 s50, 16777216
s_mov_b32 s51, 0x20000
v_add_u32 v13, s67, v1
v_mfma_f32_16x16x32_bf16 v[232:235], a[40:43], v[60:63], v[232:235]
v_add_u32 v14, 0x10000, v13
v_add_u32 v15, 0x20000, v13
s_lshl_b32 s62, s64, 2
v_mfma_f32_16x16x32_bf16 v[204:207], a[44:47], v[56:59], v[204:207]
s_mul_i32 s62, s62, 131072
s_add_u32 s63, s39, 0
s_and_b32 s63, s63, 3
v_mfma_f32_16x16x32_bf16 v[236:239], a[44:47], v[60:63], v[236:239]
s_mul_i32 s63, s63, 131072
s_add_u32 s63, s63, s62
v_add_u32 v2, s63, v1
v_mfma_f32_16x16x32_bf16 v[208:211], a[48:51], v[56:59], v[208:211]
s_add_u32 s63, s39, 1
s_and_b32 s63, s63, 3
s_mul_i32 s63, s63, 131072
)ASM"
R"ASM(v_mfma_f32_16x16x32_bf16 v[240:243], a[48:51], v[60:63], v[240:243]
s_waitcnt lgkmcnt(0)
s_add_u32 s63, s63, s62
v_add_u32 v3, s63, v1
s_add_u32 s63, s39, 2
v_mfma_f32_16x16x32_bf16 v[212:215], a[52:55], v[56:59], v[212:215]
s_and_b32 s63, s63, 3
s_mul_i32 s63, s63, 131072
s_add_u32 s63, s63, s62
v_mfma_f32_16x16x32_bf16 v[244:247], a[52:55], v[60:63], v[244:247]
v_add_u32 v4, s63, v1
s_add_u32 s63, s39, 3
s_and_b32 s63, s63, 3
v_mfma_f32_16x16x32_bf16 v[216:219], a[56:59], v[56:59], v[216:219]
s_mul_i32 s63, s63, 131072
s_add_u32 s63, s63, s62
v_add_u32 v5, s63, v1
v_mfma_f32_16x16x32_bf16 v[248:251], a[56:59], v[60:63], v[248:251]
v_mfma_f32_16x16x32_bf16 v[220:223], a[60:63], v[56:59], v[220:223]
v_mfma_f32_16x16x32_bf16 v[252:255], a[60:63], v[60:63], v[252:255]
v_mfma_f32_16x16x32_bf16 v[72:75], a[128:131], v[56:59], v[72:75]
v_mfma_f32_16x16x32_bf16 v[76:79], a[128:131], v[60:63], v[76:79]
s_waitcnt vmcnt(0) lgkmcnt(0)
s_barrier
s_nop 5
v_rcp_f32 v24, v72
v_rcp_f32 v26, v76
v_mov_b32 v25, v24
v_mov_b32 v27, v26
v_pk_mul_f32 v[192:193], v[192:193], v[24:25]
v_pk_mul_f32 v[194:195], v[194:195], v[24:25]
v_pk_mul_f32 v[196:197], v[196:197], v[24:25]
v_pk_mul_f32 v[198:199], v[198:199], v[24:25]
v_cvt_pk_bf16_f32 v192, v192, v193
v_cvt_pk_bf16_f32 v193, v194, v195
v_cvt_pk_bf16_f32 v194, v196, v197
v_cvt_pk_bf16_f32 v195, v198, v199
v_pk_mul_f32 v[200:201], v[200:201], v[24:25]
v_pk_mul_f32 v[202:203], v[202:203], v[24:25]
v_pk_mul_f32 v[204:205], v[204:205], v[24:25]
v_pk_mul_f32 v[206:207], v[206:207], v[24:25]
v_cvt_pk_bf16_f32 v200, v200, v201
v_cvt_pk_bf16_f32 v201, v202, v203
v_cvt_pk_bf16_f32 v202, v204, v205
v_cvt_pk_bf16_f32 v203, v206, v207
s_sub_u32 s62, 15, s61
s_lshl_b32 s62, s62, 3
s_lshl_b32 s63, s39, 1
s_add_u32 s62, s62, s63
s_add_u32 s62, s62, 0
s_lshl_b32 s62, s62, 17
s_lshl_b32 s63, s60, 12
s_add_u32 s62, s62, s63
buffer_store_dwordx4 v[192:195], v1, s[56:59], s62 offen offset:0 sc1
v_mfma_f32_16x16x32_bf16 v[80:83], a[128:131], v[192:195], v[80:83]
v_pk_mul_f32 v[208:209], v[208:209], v[24:25]
v_pk_mul_f32 v[210:211], v[210:211], v[24:25]
v_pk_mul_f32 v[212:213], v[212:213], v[24:25]
v_pk_mul_f32 v[214:215], v[214:215], v[24:25]
v_cvt_pk_bf16_f32 v208, v208, v209
v_cvt_pk_bf16_f32 v209, v210, v211
v_cvt_pk_bf16_f32 v210, v212, v213
v_cvt_pk_bf16_f32 v211, v214, v215
buffer_store_dwordx4 v[200:203], v1, s[56:59], s62 offen offset:1024 sc1
v_mfma_f32_16x16x32_bf16 v[80:83], a[128:131], v[200:203], v[80:83]
v_pk_mul_f32 v[216:217], v[216:217], v[24:25]
v_pk_mul_f32 v[218:219], v[218:219], v[24:25]
v_pk_mul_f32 v[220:221], v[220:221], v[24:25]
v_pk_mul_f32 v[222:223], v[222:223], v[24:25]
v_cvt_pk_bf16_f32 v216, v216, v217
v_cvt_pk_bf16_f32 v217, v218, v219
v_cvt_pk_bf16_f32 v218, v220, v221
v_cvt_pk_bf16_f32 v219, v222, v223
buffer_store_dwordx4 v[208:211], v1, s[56:59], s62 offen offset:2048 sc1
v_mfma_f32_16x16x32_bf16 v[80:83], a[128:131], v[208:211], v[80:83]
v_pk_mul_f32 v[224:225], v[224:225], v[26:27]
v_pk_mul_f32 v[226:227], v[226:227], v[26:27]
v_pk_mul_f32 v[228:229], v[228:229], v[26:27]
v_pk_mul_f32 v[230:231], v[230:231], v[26:27]
v_cvt_pk_bf16_f32 v224, v224, v225
v_cvt_pk_bf16_f32 v225, v226, v227
v_cvt_pk_bf16_f32 v226, v228, v229
v_cvt_pk_bf16_f32 v227, v230, v231
buffer_store_dwordx4 v[216:219], v1, s[56:59], s62 offen offset:3072 sc1
v_mfma_f32_16x16x32_bf16 v[80:83], a[128:131], v[216:219], v[80:83]
v_pk_mul_f32 v[232:233], v[232:233], v[26:27]
v_pk_mul_f32 v[234:235], v[234:235], v[26:27]
v_pk_mul_f32 v[236:237], v[236:237], v[26:27]
v_pk_mul_f32 v[238:239], v[238:239], v[26:27]
v_cvt_pk_bf16_f32 v232, v232, v233
v_cvt_pk_bf16_f32 v233, v234, v235
v_cvt_pk_bf16_f32 v234, v236, v237
v_cvt_pk_bf16_f32 v235, v238, v239
s_sub_u32 s62, 15, s61
s_lshl_b32 s62, s62, 3
s_lshl_b32 s63, s39, 1
s_add_u32 s62, s62, s63
s_add_u32 s62, s62, 1
s_lshl_b32 s62, s62, 17
s_lshl_b32 s63, s60, 12
s_add_u32 s62, s62, s63
buffer_store_dwordx4 v[224:227], v1, s[56:59], s62 offen offset:0 sc1
v_mfma_f32_16x16x32_bf16 v[80:83], a[128:131], v[224:227], v[80:83]
v_pk_mul_f32 v[240:241], v[240:241], v[26:27]
v_pk_mul_f32 v[242:243], v[242:243], v[26:27]
v_pk_mul_f32 v[244:245], v[244:245], v[26:27]
v_pk_mul_f32 v[246:247], v[246:247], v[26:27]
v_cvt_pk_bf16_f32 v240, v240, v241
v_cvt_pk_bf16_f32 v241, v242, v243
v_cvt_pk_bf16_f32 v242, v244, v245
v_cvt_pk_bf16_f32 v243, v246, v247
buffer_store_dwordx4 v[232:235], v1, s[56:59], s62 offen offset:1024 sc1
v_mfma_f32_16x16x32_bf16 v[80:83], a[128:131], v[232:235], v[80:83]
v_pk_mul_f32 v[248:249], v[248:249], v[26:27]
v_pk_mul_f32 v[250:251], v[250:251], v[26:27]
v_pk_mul_f32 v[252:253], v[252:253], v[26:27]
v_pk_mul_f32 v[254:255], v[254:255], v[26:27]
v_cvt_pk_bf16_f32 v248, v248, v249
v_cvt_pk_bf16_f32 v249, v250, v251
v_cvt_pk_bf16_f32 v250, v252, v253
v_cvt_pk_bf16_f32 v251, v254, v255
buffer_store_dwordx4 v[240:243], v1, s[56:59], s62 offen offset:2048 sc1
v_mfma_f32_16x16x32_bf16 v[80:83], a[128:131], v[240:243], v[80:83]
buffer_store_dwordx4 v[248:251], v1, s[56:59], s62 offen offset:3072 sc1
v_mfma_f32_16x16x32_bf16 v[80:83], a[128:131], v[248:251], v[80:83]
s_mov_b32 s63, 53248
buffer_load_dwordx4 v[192:195], v1, s[52:55], s63 offen offset:0 sc1
buffer_load_dwordx4 v[196:199], v1, s[52:55], s63 offen offset:1024 sc1
buffer_load_dwordx4 v[200:203], v1, s[52:55], s63 offen offset:2048 sc1
buffer_load_dwordx4 v[204:207], v1, s[52:55], s63 offen offset:3072 sc1
s_mov_b32 s63, 57344
buffer_load_dwordx4 v[208:211], v1, s[52:55], s63 offen offset:0 sc1
buffer_load_dwordx4 v[212:215], v1, s[52:55], s63 offen offset:1024 sc1
buffer_load_dwordx4 v[216:219], v1, s[52:55], s63 offen offset:2048 sc1
buffer_load_dwordx4 v[220:223], v1, s[52:55], s63 offen offset:3072 sc1
s_mov_b32 s62, 0x7f7fffff
s_mov_b32 s76, 0x2b800000
s_mov_b32 s77, 0x53800000
v_cmp_nge_f32 s[74:75], v64, s76
v_cmp_nle_f32 s[78:79], |v80|, s62
s_nop 0
s_or_b64 s[74:75], s[74:75], s[78:79]
v_cmp_nle_f32 s[78:79], v64, s77
s_nop 0
s_or_b64 s[74:75], s[74:75], s[78:79]
v_cmp_nge_f32 s[78:79], v68, s76
s_nop 0
s_or_b64 s[74:75], s[74:75], s[78:79]
v_cmp_nle_f32 s[78:79], v68, s77
s_nop 0
s_or_b64 s[74:75], s[74:75], s[78:79]
v_cmp_nge_f32 s[78:79], v72, s76
s_nop 0
s_or_b64 s[74:75], s[74:75], s[78:79]
v_cmp_nle_f32 s[78:79], v72, s77
s_nop 0
s_or_b64 s[74:75], s[74:75], s[78:79]
v_cmp_nge_f32 s[78:79], v76, s76
s_nop 0
s_or_b64 s[74:75], s[74:75], s[78:79]
v_cmp_nle_f32 s[78:79], v76, s77
s_nop 0
s_or_b64 s[74:75], s[74:75], s[78:79]
s_cmp_lg_u64 s[74:75], 0
s_cselect_b32 s62, 1, 0
v_mov_b32 v20, s62
s_lshl_b32 s63, s39, 2
s_add_u32 s63, s63, 130816
v_mov_b32 v21, s63
ds_write_b32 v21, v20
s_waitcnt vmcnt(8)
s_waitcnt lgkmcnt(0)
s_barrier
v_mov_b32 v21, 130816
ds_read_b128 v[24:27], v21
s_waitcnt lgkmcnt(0)
v_or_b32 v24, v24, v25
v_or_b32 v26, v26, v27
v_or_b32 v24, v24, v26
s_nop 0
v_readfirstlane_b32 s62, v24
s_nop 0
s_andn2_b32 s62, s62, s73
s_cmp_lg_u32 s62, 0
s_cbranch_scc1 L_redo%=
v_mov_b32 v20, s33
v_mov_b32 v21, 0
s_lshl_b32 s79, s38, 7
s_add_u32 s79, s79, 98304
s_mov_b64 exec, s[34:35]
buffer_store_dword v20, v21, s[40:43], s79 offen sc1
s_mov_b64 exec, -1
s_lshr_b32 s62, s64, 1
s_sub_u32 s63, 15, s62
s_min_u32 s62, s62, s63
s_lshl_b32 s62, s62, 3
v_lshrrev_b32 v10, 4, v1
v_bfe_u32 v11, v10, 2, 3
v_and_b32 v10, 3, v10
v_lshl_add_u32 v10, v10, 6, v11
v_add_u32 v10, s62, v10
v_lshlrev_b32 v10, 7, v10
s_cmp_eq_u32 s39, 0
s_mov_b32 s74, 0xffffffff
s_cselect_b32 s74, s74, 0
s_mov_b32 s75, 0x0
s_cselect_b32 s75, s75, 0
s_mov_b64 exec, s[74:75]
s_mov_b32 s79, 98304
buffer_load_dword v11, v10, s[40:43], s79 offen sc1
s_mov_b64 exec, -1
s_mov_b32 s63, 61440
buffer_load_dwordx4 v[224:227], v1, s[52:55], s63 offen offset:0 sc1
buffer_load_dwordx4 v[228:231], v1, s[52:55], s63 offen offset:1024 sc1
buffer_load_dwordx4 v[232:235], v1, s[52:55], s63 offen offset:2048 sc1
buffer_load_dwordx4 v[236:239], v1, s[52:55], s63 offen offset:3072 sc1
s_mov_b32 s63, 65536
buffer_load_dwordx4 v[240:243], v1, s[52:55], s63 offen offset:0 sc1
buffer_load_dwordx4 v[244:247], v1, s[52:55], s63 offen offset:1024 sc1
buffer_load_dwordx4 v[248:251], v1, s[52:55], s63 offen offset:2048 sc1
buffer_load_dwordx4 v[252:255], v1, s[52:55], s63 offen offset:3072 sc1
s_mov_b32 s63, 69632
s_add_u32 m0, s67, 0
s_nop 0
buffer_load_dwordx4 v1, s[52:55], s63 offen offset:0 sc1 lds
buffer_load_dwordx4 v1, s[52:55], s63 offen offset:1024 sc1 lds
buffer_load_dwordx4 v1, s[52:55], s63 offen offset:2048 sc1 lds
buffer_load_dwordx4 v1, s[52:55], s63 offen offset:3072 sc1 lds
s_mov_b32 s63, 73728
s_add_u32 m0, s67, 16384
s_nop 0
buffer_load_dwordx4 v1, s[52:55], s63 offen offset:0 sc1 lds
buffer_load_dwordx4 v1, s[52:55], s63 offen offset:1024 sc1 lds
buffer_load_dwordx4 v1, s[52:55], s63 offen offset:2048 sc1 lds
buffer_load_dwordx4 v1, s[52:55], s63 offen offset:3072 sc1 lds
s_mov_b32 s63, 77824
s_add_u32 m0, s67, 32768
s_nop 0
buffer_load_dwordx4 v1, s[52:55], s63 offen offset:0 sc1 lds
buffer_load_dwordx4 v1, s[52:55], s63 offen offset:1024 sc1 lds
buffer_load_dwordx4 v1, s[52:55], s63 offen offset:2048 sc1 lds
buffer_load_dwordx4 v1, s[52:55], s63 offen offset:3072 sc1 lds
s_mov_b32 s63, 81920
s_add_u32 m0, s67, 49152
s_nop 0
buffer_load_dwordx4 v1, s[52:55], s63 offen offset:0 sc1 lds
buffer_load_dwordx4 v1, s[52:55], s63 offen offset:1024 sc1 lds
buffer_load_dwordx4 v1, s[52:55], s63 offen offset:2048 sc1 lds
buffer_load_dwordx4 v1, s[52:55], s63 offen offset:3072 sc1 lds
s_mov_b32 s63, 86016
s_add_u32 m0, s67, 65536
s_nop 0
buffer_load_dwordx4 v1, s[52:55], s63 offen offset:0 sc1 lds
buffer_load_dwordx4 v1, s[52:55], s63 offen offset:1024 sc1 lds
buffer_load_dwordx4 v1, s[52:55], s63 offen offset:2048 sc1 lds
buffer_load_dwordx4 v1, s[52:55], s63 offen offset:3072 sc1 lds
s_mov_b32 s63, 90112
s_add_u32 m0, s67, 81920
s_nop 0
buffer_load_dwordx4 v1, s[52:55], s63 offen offset:0 sc1 lds
buffer_load_dwordx4 v1, s[52:55], s63 offen offset:1024 sc1 lds
buffer_load_dwordx4 v1, s[52:55], s63 offen offset:2048 sc1 lds
buffer_load_dwordx4 v1, s[52:55], s63 offen offset:3072 sc1 lds
s_mov_b32 s63, 94208
s_add_u32 m0, s67, 98304
s_nop 0
buffer_load_dwordx4 v1, s[52:55], s63 offen offset:0 sc1 lds
buffer_load_dwordx4 v1, s[52:55], s63 offen offset:1024 sc1 lds
buffer_load_dwordx4 v1, s[52:55], s63 offen offset:2048 sc1 lds
buffer_load_dwordx4 v1, s[52:55], s63 offen offset:3072 sc1 lds
s_mov_b32 s63, 102400
buffer_load_dwordx4 v[16:19], v1, s[52:55], s63 offen offset:0 sc1
buffer_load_dwordx4 v[20:23], v1, s[52:55], s63 offen offset:1024 sc1
buffer_load_dwordx4 v[24:27], v1, s[52:55], s63 offen offset:2048 sc1
buffer_load_dwordx4 v[28:31], v1, s[52:55], s63 offen offset:3072 sc1
s_mov_b32 s63, 106496
buffer_load_dwordx4 v[32:35], v1, s[52:55], s63 offen offset:0 sc1
buffer_load_dwordx4 v[36:39], v1, s[52:55], s63 offen offset:1024 sc1
buffer_load_dwordx4 v[40:43], v1, s[52:55], s63 offen offset:2048 sc1
buffer_load_dwordx4 v[44:47], v1, s[52:55], s63 offen offset:3072 sc1
s_mov_b32 s63, 110592
buffer_load_dwordx4 v[48:51], v1, s[52:55], s63 offen offset:0 sc1
buffer_load_dwordx4 v[52:55], v1, s[52:55], s63 offen offset:1024 sc1
buffer_load_dwordx4 v[56:59], v1, s[52:55], s63 offen offset:2048 sc1
buffer_load_dwordx4 v[60:63], v1, s[52:55], s63 offen offset:3072 sc1
s_waitcnt vmcnt(48)
s_mov_b64 vcc, 0
s_mov_b64 exec, s[74:75]
v_cmp_ne_u32 vcc, s33, v11
s_mov_b64 exec, -1
s_nop 4
s_cbranch_vccnz L_slow3%=
L_back3%=:
s_barrier
s_add_u32 s70, s69, 0
buffer_load_dwordx4 a[0:3], v2, s[48:51], s70 offen offset:0 sc1
buffer_load_dwordx4 a[4:7], v3, s[48:51], s70 offen offset:0 sc1
buffer_load_dwordx4 a[8:11], v4, s[48:51], s70 offen offset:0 sc1
buffer_load_dwordx4 a[12:15], v5, s[48:51], s70 offen offset:0 sc1
s_add_u32 s70, s69, 0
)ASM"
R"ASM(buffer_load_dwordx4 a[16:19], v2, s[48:51], s70 offen offset:1024 sc1
buffer_load_dwordx4 a[20:23], v3, s[48:51], s70 offen offset:1024 sc1
buffer_load_dwordx4 a[24:27], v4, s[48:51], s70 offen offset:1024 sc1
buffer_load_dwordx4 a[28:31], v5, s[48:51], s70 offen offset:1024 sc1
s_add_u32 s70, s69, 0
buffer_load_dwordx4 a[32:35], v2, s[48:51], s70 offen offset:2048 sc1
buffer_load_dwordx4 a[36:39], v3, s[48:51], s70 offen offset:2048 sc1
buffer_load_dwordx4 a[40:43], v4, s[48:51], s70 offen offset:2048 sc1
buffer_load_dwordx4 a[44:47], v5, s[48:51], s70 offen offset:2048 sc1
s_add_u32 s70, s69, 0
buffer_load_dwordx4 a[48:51], v2, s[48:51], s70 offen offset:3072 sc1
buffer_load_dwordx4 a[52:55], v3, s[48:51], s70 offen offset:3072 sc1
s_waitcnt vmcnt(48)
buffer_load_dwordx4 a[56:59], v4, s[48:51], s70 offen offset:3072 sc1
buffer_load_dwordx4 a[60:63], v5, s[48:51], s70 offen offset:3072 sc1
s_add_u32 s70, s69, 4096
buffer_load_dwordx4 a[64:67], v2, s[48:51], s70 offen offset:0 sc1
buffer_load_dwordx4 a[68:71], v3, s[48:51], s70 offen offset:0 sc1
buffer_load_dwordx4 a[72:75], v4, s[48:51], s70 offen offset:0 sc1
buffer_load_dwordx4 a[76:79], v5, s[48:51], s70 offen offset:0 sc1
s_mov_b32 s63, 98304
s_add_u32 m0, s67, 114688
s_nop 0
buffer_load_dwordx4 v1, s[52:55], s63 offen offset:0 sc1 lds
buffer_load_dwordx4 v1, s[52:55], s63 offen offset:1024 sc1 lds
buffer_load_dwordx4 v1, s[52:55], s63 offen offset:2048 sc1 lds
buffer_load_dwordx4 v1, s[52:55], s63 offen offset:3072 sc1 lds
v_lshrrev_b32 v9, 4, v1
s_lshl_b32 s71, s64, 2
s_add_u32 s71, s71, s39
s_add_u32 s44, s30, 52428800
s_addc_u32 s45, s31, 0
s_and_b32 s45, s45, 0xffff
s_mov_b32 s46, 12288
s_mov_b32 s47, 0x20000
v_lshrrev_b32 v6, 4, v9
v_lshlrev_b32 v6, 4, v6
s_lshl_b32 s63, s65, 8
v_add_u32 v6, s63, v6
s_mov_b32 s72, 4096
s_mov_b32 s73, 8192
s_mov_b32 s56, s8
s_mov_b32 s57, s9
s_and_b32 s57, s57, 0xffff
s_mov_b32 s58, 2097152
s_mov_b32 s59, 0x20000
v_and_b32 v7, 15, v9
v_lshlrev_b32 v7, 10, v7
v_lshrrev_b32 v8, 4, v9
v_lshl_add_u32 v7, v8, 3, v7
s_lshl_b32 s63, s71, 14
s_lshl_b32 s62, s65, 7
s_add_u32 s63, s63, s62
s_waitcnt vmcnt(20)
v_mfma_f32_16x16x32_bf16 v[64:67], a[132:135], a[0:3], 0
s_add_u32 s70, s69, 4096
buffer_load_dwordx4 a[80:83], v2, s[48:51], s70 offen offset:1024 sc1
v_mfma_f32_16x16x32_bf16 v[80:83], a[132:135], a[4:7], 0
buffer_load_dwordx4 a[84:87], v3, s[48:51], s70 offen offset:1024 sc1
v_mfma_f32_16x16x32_bf16 v[96:99], a[132:135], a[8:11], 0
buffer_load_dwordx4 a[88:91], v4, s[48:51], s70 offen offset:1024 sc1
v_mfma_f32_16x16x32_bf16 v[112:115], a[132:135], a[12:15], 0
buffer_load_dwordx4 a[92:95], v5, s[48:51], s70 offen offset:1024 sc1
v_mfma_f32_16x16x32_bf16 v[68:71], a[136:139], a[0:3], 0
v_mfma_f32_16x16x32_bf16 v[84:87], a[136:139], a[4:7], 0
v_mfma_f32_16x16x32_bf16 v[100:103], a[136:139], a[8:11], 0
v_mfma_f32_16x16x32_bf16 v[116:119], a[136:139], a[12:15], 0
v_mfma_f32_16x16x32_bf16 v[72:75], a[140:143], a[0:3], 0
v_mfma_f32_16x16x32_bf16 v[88:91], a[140:143], a[4:7], 0
v_mfma_f32_16x16x32_bf16 v[104:107], a[140:143], a[8:11], 0
v_mfma_f32_16x16x32_bf16 v[120:123], a[140:143], a[12:15], 0
v_mfma_f32_16x16x32_bf16 v[76:79], a[144:147], a[0:3], 0
v_mfma_f32_16x16x32_bf16 v[92:95], a[144:147], a[4:7], 0
v_mfma_f32_16x16x32_bf16 v[108:111], a[144:147], a[8:11], 0
v_mfma_f32_16x16x32_bf16 v[124:127], a[144:147], a[12:15], 0
s_waitcnt vmcnt(20)
v_mfma_f32_16x16x32_bf16 v[64:67], a[148:151], a[16:19], v[64:67]
s_add_u32 s70, s69, 4096
buffer_load_dwordx4 a[0:3], v2, s[48:51], s70 offen offset:2048 sc1
v_mfma_f32_16x16x32_bf16 v[80:83], a[148:151], a[20:23], v[80:83]
buffer_load_dwordx4 a[4:7], v3, s[48:51], s70 offen offset:2048 sc1
v_mfma_f32_16x16x32_bf16 v[96:99], a[148:151], a[24:27], v[96:99]
buffer_load_dwordx4 a[8:11], v4, s[48:51], s70 offen offset:2048 sc1
v_mfma_f32_16x16x32_bf16 v[112:115], a[148:151], a[28:31], v[112:115]
buffer_load_dwordx4 a[12:15], v5, s[48:51], s70 offen offset:2048 sc1
v_mfma_f32_16x16x32_bf16 v[68:71], a[152:155], a[16:19], v[68:71]
s_mov_b32 s76, 114688
buffer_load_dwordx4 a[132:135], v1, s[52:55], s76 offen offset:0 sc1
v_mfma_f32_16x16x32_bf16 v[84:87], a[152:155], a[20:23], v[84:87]
buffer_load_dwordx4 a[136:139], v1, s[52:55], s76 offen offset:1024 sc1
v_mfma_f32_16x16x32_bf16 v[100:103], a[152:155], a[24:27], v[100:103]
buffer_load_dwordx4 a[140:143], v1, s[52:55], s76 offen offset:2048 sc1
v_mfma_f32_16x16x32_bf16 v[116:119], a[152:155], a[28:31], v[116:119]
buffer_load_dwordx4 a[144:147], v1, s[52:55], s76 offen offset:3072 sc1
v_mfma_f32_16x16x32_bf16 v[72:75], a[156:159], a[16:19], v[72:75]
v_mfma_f32_16x16x32_bf16 v[88:91], a[156:159], a[20:23], v[88:91]
v_mfma_f32_16x16x32_bf16 v[104:107], a[156:159], a[24:27], v[104:107]
v_mfma_f32_16x16x32_bf16 v[120:123], a[156:159], a[28:31], v[120:123]
v_mfma_f32_16x16x32_bf16 v[76:79], a[160:163], a[16:19], v[76:79]
v_mfma_f32_16x16x32_bf16 v[92:95], a[160:163], a[20:23], v[92:95]
v_mfma_f32_16x16x32_bf16 v[108:111], a[160:163], a[24:27], v[108:111]
v_mfma_f32_16x16x32_bf16 v[124:127], a[160:163], a[28:31], v[124:127]
s_waitcnt vmcnt(24)
v_mfma_f32_16x16x32_bf16 v[64:67], a[164:167], a[32:35], v[64:67]
s_add_u32 s70, s69, 4096
buffer_load_dwordx4 a[16:19], v2, s[48:51], s70 offen offset:3072 sc1
v_mfma_f32_16x16x32_bf16 v[80:83], a[164:167], a[36:39], v[80:83]
buffer_load_dwordx4 a[20:23], v3, s[48:51], s70 offen offset:3072 sc1
v_mfma_f32_16x16x32_bf16 v[96:99], a[164:167], a[40:43], v[96:99]
buffer_load_dwordx4 a[24:27], v4, s[48:51], s70 offen offset:3072 sc1
v_mfma_f32_16x16x32_bf16 v[112:115], a[164:167], a[44:47], v[112:115]
buffer_load_dwordx4 a[28:31], v5, s[48:51], s70 offen offset:3072 sc1
v_mfma_f32_16x16x32_bf16 v[68:71], a[168:171], a[32:35], v[68:71]
s_mov_b32 s76, 118784
buffer_load_dwordx4 a[148:151], v1, s[52:55], s76 offen offset:0 sc1
v_mfma_f32_16x16x32_bf16 v[84:87], a[168:171], a[36:39], v[84:87]
buffer_load_dwordx4 a[152:155], v1, s[52:55], s76 offen offset:1024 sc1
v_mfma_f32_16x16x32_bf16 v[100:103], a[168:171], a[40:43], v[100:103]
buffer_load_dwordx4 a[156:159], v1, s[52:55], s76 offen offset:2048 sc1
v_mfma_f32_16x16x32_bf16 v[116:119], a[168:171], a[44:47], v[116:119]
buffer_load_dwordx4 a[160:163], v1, s[52:55], s76 offen offset:3072 sc1
v_mfma_f32_16x16x32_bf16 v[72:75], a[172:175], a[32:35], v[72:75]
v_mfma_f32_16x16x32_bf16 v[88:91], a[172:175], a[36:39], v[88:91]
v_mfma_f32_16x16x32_bf16 v[104:107], a[172:175], a[40:43], v[104:107]
v_mfma_f32_16x16x32_bf16 v[120:123], a[172:175], a[44:47], v[120:123]
v_mfma_f32_16x16x32_bf16 v[76:79], a[176:179], a[32:35], v[76:79]
v_mfma_f32_16x16x32_bf16 v[92:95], a[176:179], a[36:39], v[92:95]
v_mfma_f32_16x16x32_bf16 v[108:111], a[176:179], a[40:43], v[108:111]
v_mfma_f32_16x16x32_bf16 v[124:127], a[176:179], a[44:47], v[124:127]
s_waitcnt vmcnt(28)
v_mfma_f32_16x16x32_bf16 v[64:67], a[180:183], a[48:51], v[64:67]
s_add_u32 s70, s69, 8192
buffer_load_dwordx4 a[32:35], v2, s[48:51], s70 offen offset:0 sc1
v_mfma_f32_16x16x32_bf16 v[80:83], a[180:183], a[52:55], v[80:83]
buffer_load_dwordx4 a[36:39], v3, s[48:51], s70 offen offset:0 sc1
v_mfma_f32_16x16x32_bf16 v[96:99], a[180:183], a[56:59], v[96:99]
buffer_load_dwordx4 a[40:43], v4, s[48:51], s70 offen offset:0 sc1
v_mfma_f32_16x16x32_bf16 v[112:115], a[180:183], a[60:63], v[112:115]
buffer_load_dwordx4 a[44:47], v5, s[48:51], s70 offen offset:0 sc1
v_mfma_f32_16x16x32_bf16 v[68:71], a[184:187], a[48:51], v[68:71]
s_mov_b32 s76, 122880
buffer_load_dwordx4 a[164:167], v1, s[52:55], s76 offen offset:0 sc1
v_mfma_f32_16x16x32_bf16 v[84:87], a[184:187], a[52:55], v[84:87]
buffer_load_dwordx4 a[168:171], v1, s[52:55], s76 offen offset:1024 sc1
v_mfma_f32_16x16x32_bf16 v[100:103], a[184:187], a[56:59], v[100:103]
buffer_load_dwordx4 a[172:175], v1, s[52:55], s76 offen offset:2048 sc1
v_mfma_f32_16x16x32_bf16 v[116:119], a[184:187], a[60:63], v[116:119]
buffer_load_dwordx4 a[176:179], v1, s[52:55], s76 offen offset:3072 sc1
v_mfma_f32_16x16x32_bf16 v[72:75], a[188:191], a[48:51], v[72:75]
v_mfma_f32_16x16x32_bf16 v[88:91], a[188:191], a[52:55], v[88:91]
v_mfma_f32_16x16x32_bf16 v[104:107], a[188:191], a[56:59], v[104:107]
v_mfma_f32_16x16x32_bf16 v[120:123], a[188:191], a[60:63], v[120:123]
v_mfma_f32_16x16x32_bf16 v[76:79], a[192:195], a[48:51], v[76:79]
v_mfma_f32_16x16x32_bf16 v[92:95], a[192:195], a[52:55], v[92:95]
v_mfma_f32_16x16x32_bf16 v[108:111], a[192:195], a[56:59], v[108:111]
v_mfma_f32_16x16x32_bf16 v[124:127], a[192:195], a[60:63], v[124:127]
s_waitcnt vmcnt(32)
v_mfma_f32_16x16x32_bf16 v[64:67], a[196:199], a[64:67], v[64:67]
s_add_u32 s70, s69, 8192
buffer_load_dwordx4 a[48:51], v2, s[48:51], s70 offen offset:1024 sc1
v_mfma_f32_16x16x32_bf16 v[80:83], a[196:199], a[68:71], v[80:83]
buffer_load_dwordx4 a[52:55], v3, s[48:51], s70 offen offset:1024 sc1
v_mfma_f32_16x16x32_bf16 v[96:99], a[196:199], a[72:75], v[96:99]
buffer_load_dwordx4 a[56:59], v4, s[48:51], s70 offen offset:1024 sc1
v_mfma_f32_16x16x32_bf16 v[112:115], a[196:199], a[76:79], v[112:115]
buffer_load_dwordx4 a[60:63], v5, s[48:51], s70 offen offset:1024 sc1
v_mfma_f32_16x16x32_bf16 v[68:71], a[200:203], a[64:67], v[68:71]
s_mov_b32 s76, 126976
buffer_load_dwordx4 a[180:183], v1, s[52:55], s76 offen offset:0 sc1
v_mfma_f32_16x16x32_bf16 v[84:87], a[200:203], a[68:71], v[84:87]
buffer_load_dwordx4 a[184:187], v1, s[52:55], s76 offen offset:1024 sc1
v_mfma_f32_16x16x32_bf16 v[100:103], a[200:203], a[72:75], v[100:103]
buffer_load_dwordx4 a[188:191], v1, s[52:55], s76 offen offset:2048 sc1
v_mfma_f32_16x16x32_bf16 v[116:119], a[200:203], a[76:79], v[116:119]
buffer_load_dwordx4 a[192:195], v1, s[52:55], s76 offen offset:3072 sc1
v_mfma_f32_16x16x32_bf16 v[72:75], a[204:207], a[64:67], v[72:75]
v_mfma_f32_16x16x32_bf16 v[88:91], a[204:207], a[68:71], v[88:91]
v_mfma_f32_16x16x32_bf16 v[104:107], a[204:207], a[72:75], v[104:107]
v_mfma_f32_16x16x32_bf16 v[120:123], a[204:207], a[76:79], v[120:123]
v_mfma_f32_16x16x32_bf16 v[76:79], a[208:211], a[64:67], v[76:79]
v_mfma_f32_16x16x32_bf16 v[92:95], a[208:211], a[68:71], v[92:95]
v_mfma_f32_16x16x32_bf16 v[108:111], a[208:211], a[72:75], v[108:111]
v_mfma_f32_16x16x32_bf16 v[124:127], a[208:211], a[76:79], v[124:127]
s_waitcnt vmcnt(32)
v_mfma_f32_16x16x32_bf16 v[64:67], a[212:215], a[80:83], v[64:67]
s_add_u32 s70, s69, 8192
buffer_load_dwordx4 a[64:67], v2, s[48:51], s70 offen offset:2048 sc1
v_mfma_f32_16x16x32_bf16 v[80:83], a[212:215], a[84:87], v[80:83]
buffer_load_dwordx4 a[68:71], v3, s[48:51], s70 offen offset:2048 sc1
v_mfma_f32_16x16x32_bf16 v[96:99], a[212:215], a[88:91], v[96:99]
buffer_load_dwordx4 a[72:75], v4, s[48:51], s70 offen offset:2048 sc1
v_mfma_f32_16x16x32_bf16 v[112:115], a[212:215], a[92:95], v[112:115]
buffer_load_dwordx4 a[76:79], v5, s[48:51], s70 offen offset:2048 sc1
v_mfma_f32_16x16x32_bf16 v[68:71], a[216:219], a[80:83], v[68:71]
v_mfma_f32_16x16x32_bf16 v[84:87], a[216:219], a[84:87], v[84:87]
v_mfma_f32_16x16x32_bf16 v[100:103], a[216:219], a[88:91], v[100:103]
v_mfma_f32_16x16x32_bf16 v[116:119], a[216:219], a[92:95], v[116:119]
v_mfma_f32_16x16x32_bf16 v[72:75], a[220:223], a[80:83], v[72:75]
v_mfma_f32_16x16x32_bf16 v[88:91], a[220:223], a[84:87], v[88:91]
v_mfma_f32_16x16x32_bf16 v[104:107], a[220:223], a[88:91], v[104:107]
v_mfma_f32_16x16x32_bf16 v[120:123], a[220:223], a[92:95], v[120:123]
v_mfma_f32_16x16x32_bf16 v[76:79], a[224:227], a[80:83], v[76:79]
v_mfma_f32_16x16x32_bf16 v[92:95], a[224:227], a[84:87], v[92:95]
v_mfma_f32_16x16x32_bf16 v[108:111], a[224:227], a[88:91], v[108:111]
v_mfma_f32_16x16x32_bf16 v[124:127], a[224:227], a[92:95], v[124:127]
s_waitcnt vmcnt(32)
v_mfma_f32_16x16x32_bf16 v[64:67], a[228:231], a[0:3], v[64:67]
s_add_u32 s70, s69, 8192
buffer_load_dwordx4 a[80:83], v2, s[48:51], s70 offen offset:3072 sc1
v_mfma_f32_16x16x32_bf16 v[80:83], a[228:231], a[4:7], v[80:83]
buffer_load_dwordx4 a[84:87], v3, s[48:51], s70 offen offset:3072 sc1
v_mfma_f32_16x16x32_bf16 v[96:99], a[228:231], a[8:11], v[96:99]
buffer_load_dwordx4 a[88:91], v4, s[48:51], s70 offen offset:3072 sc1
v_mfma_f32_16x16x32_bf16 v[112:115], a[228:231], a[12:15], v[112:115]
buffer_load_dwordx4 a[92:95], v5, s[48:51], s70 offen offset:3072 sc1
v_mfma_f32_16x16x32_bf16 v[68:71], a[232:235], a[0:3], v[68:71]
v_mfma_f32_16x16x32_bf16 v[84:87], a[232:235], a[4:7], v[84:87]
v_mfma_f32_16x16x32_bf16 v[100:103], a[232:235], a[8:11], v[100:103]
v_mfma_f32_16x16x32_bf16 v[116:119], a[232:235], a[12:15], v[116:119]
v_mfma_f32_16x16x32_bf16 v[72:75], a[236:239], a[0:3], v[72:75]
v_mfma_f32_16x16x32_bf16 v[88:91], a[236:239], a[4:7], v[88:91]
v_mfma_f32_16x16x32_bf16 v[104:107], a[236:239], a[8:11], v[104:107]
v_mfma_f32_16x16x32_bf16 v[120:123], a[236:239], a[12:15], v[120:123]
v_mfma_f32_16x16x32_bf16 v[76:79], a[240:243], a[0:3], v[76:79]
v_mfma_f32_16x16x32_bf16 v[92:95], a[240:243], a[4:7], v[92:95]
v_mfma_f32_16x16x32_bf16 v[108:111], a[240:243], a[8:11], v[108:111]
v_mfma_f32_16x16x32_bf16 v[124:127], a[240:243], a[12:15], v[124:127]
s_waitcnt vmcnt(28)
v_mfma_f32_16x16x32_bf16 v[64:67], v[128:131], a[16:19], v[64:67]
s_add_u32 s70, s69, 12288
buffer_load_dwordx4 a[0:3], v2, s[48:51], s70 offen offset:0 sc1
v_mfma_f32_16x16x32_bf16 v[80:83], v[128:131], a[20:23], v[80:83]
buffer_load_dwordx4 a[4:7], v3, s[48:51], s70 offen offset:0 sc1
v_mfma_f32_16x16x32_bf16 v[96:99], v[128:131], a[24:27], v[96:99]
buffer_load_dwordx4 a[8:11], v4, s[48:51], s70 offen offset:0 sc1
v_mfma_f32_16x16x32_bf16 v[112:115], v[128:131], a[28:31], v[112:115]
buffer_load_dwordx4 a[12:15], v5, s[48:51], s70 offen offset:0 sc1
v_mfma_f32_16x16x32_bf16 v[68:71], v[132:135], a[16:19], v[68:71]
v_mfma_f32_16x16x32_bf16 v[84:87], v[132:135], a[20:23], v[84:87]
v_mfma_f32_16x16x32_bf16 v[100:103], v[132:135], a[24:27], v[100:103]
v_mfma_f32_16x16x32_bf16 v[116:119], v[132:135], a[28:31], v[116:119]
v_mfma_f32_16x16x32_bf16 v[72:75], v[136:139], a[16:19], v[72:75]
v_mfma_f32_16x16x32_bf16 v[88:91], v[136:139], a[20:23], v[88:91]
v_mfma_f32_16x16x32_bf16 v[104:107], v[136:139], a[24:27], v[104:107]
v_mfma_f32_16x16x32_bf16 v[120:123], v[136:139], a[28:31], v[120:123]
v_mfma_f32_16x16x32_bf16 v[76:79], v[140:143], a[16:19], v[76:79]
v_mfma_f32_16x16x32_bf16 v[92:95], v[140:143], a[20:23], v[92:95]
v_mfma_f32_16x16x32_bf16 v[108:111], v[140:143], a[24:27], v[108:111]
v_mfma_f32_16x16x32_bf16 v[124:127], v[140:143], a[28:31], v[124:127]
s_waitcnt vmcnt(24)
v_mfma_f32_16x16x32_bf16 v[64:67], v[144:147], a[32:35], v[64:67]
s_add_u32 s70, s69, 12288
buffer_load_dwordx4 a[16:19], v2, s[48:51], s70 offen offset:1024 sc1
v_mfma_f32_16x16x32_bf16 v[80:83], v[144:147], a[36:39], v[80:83]
buffer_load_dwordx4 a[20:23], v3, s[48:51], s70 offen offset:1024 sc1
v_mfma_f32_16x16x32_bf16 v[96:99], v[144:147], a[40:43], v[96:99]
buffer_load_dwordx4 a[24:27], v4, s[48:51], s70 offen offset:1024 sc1
v_mfma_f32_16x16x32_bf16 v[112:115], v[144:147], a[44:47], v[112:115]
buffer_load_dwordx4 a[28:31], v5, s[48:51], s70 offen offset:1024 sc1
v_mfma_f32_16x16x32_bf16 v[68:71], v[148:151], a[32:35], v[68:71]
v_mfma_f32_16x16x32_bf16 v[84:87], v[148:151], a[36:39], v[84:87]
v_mfma_f32_16x16x32_bf16 v[100:103], v[148:151], a[40:43], v[100:103]
v_mfma_f32_16x16x32_bf16 v[116:119], v[148:151], a[44:47], v[116:119]
v_mfma_f32_16x16x32_bf16 v[72:75], v[152:155], a[32:35], v[72:75]
v_mfma_f32_16x16x32_bf16 v[88:91], v[152:155], a[36:39], v[88:91]
v_mfma_f32_16x16x32_bf16 v[104:107], v[152:155], a[40:43], v[104:107]
v_mfma_f32_16x16x32_bf16 v[120:123], v[152:155], a[44:47], v[120:123]
v_mfma_f32_16x16x32_bf16 v[76:79], v[156:159], a[32:35], v[76:79]
v_mfma_f32_16x16x32_bf16 v[92:95], v[156:159], a[36:39], v[92:95]
v_mfma_f32_16x16x32_bf16 v[108:111], v[156:159], a[40:43], v[108:111]
v_mfma_f32_16x16x32_bf16 v[124:127], v[156:159], a[44:47], v[124:127]
s_waitcnt vmcnt(20)
v_mfma_f32_16x16x32_bf16 v[64:67], v[160:163], a[48:51], v[64:67]
s_add_u32 s70, s69, 12288
buffer_load_dwordx4 a[32:35], v2, s[48:51], s70 offen offset:2048 sc1
v_mfma_f32_16x16x32_bf16 v[80:83], v[160:163], a[52:55], v[80:83]
buffer_load_dwordx4 a[36:39], v3, s[48:51], s70 offen offset:2048 sc1
v_mfma_f32_16x16x32_bf16 v[96:99], v[160:163], a[56:59], v[96:99]
buffer_load_dwordx4 a[40:43], v4, s[48:51], s70 offen offset:2048 sc1
v_mfma_f32_16x16x32_bf16 v[112:115], v[160:163], a[60:63], v[112:115]
buffer_load_dwordx4 a[44:47], v5, s[48:51], s70 offen offset:2048 sc1
v_mfma_f32_16x16x32_bf16 v[68:71], v[164:167], a[48:51], v[68:71]
v_mfma_f32_16x16x32_bf16 v[84:87], v[164:167], a[52:55], v[84:87]
v_mfma_f32_16x16x32_bf16 v[100:103], v[164:167], a[56:59], v[100:103]
v_mfma_f32_16x16x32_bf16 v[116:119], v[164:167], a[60:63], v[116:119]
v_mfma_f32_16x16x32_bf16 v[72:75], v[168:171], a[48:51], v[72:75]
v_mfma_f32_16x16x32_bf16 v[88:91], v[168:171], a[52:55], v[88:91]
v_mfma_f32_16x16x32_bf16 v[104:107], v[168:171], a[56:59], v[104:107]
v_mfma_f32_16x16x32_bf16 v[120:123], v[168:171], a[60:63], v[120:123]
v_mfma_f32_16x16x32_bf16 v[76:79], v[172:175], a[48:51], v[76:79]
v_mfma_f32_16x16x32_bf16 v[92:95], v[172:175], a[52:55], v[92:95]
v_mfma_f32_16x16x32_bf16 v[108:111], v[172:175], a[56:59], v[108:111]
v_mfma_f32_16x16x32_bf16 v[124:127], v[172:175], a[60:63], v[124:127]
s_waitcnt vmcnt(16)
v_mfma_f32_16x16x32_bf16 v[64:67], v[176:179], a[64:67], v[64:67]
s_add_u32 s70, s69, 12288
buffer_load_dwordx4 a[48:51], v2, s[48:51], s70 offen offset:3072 sc1
v_mfma_f32_16x16x32_bf16 v[80:83], v[176:179], a[68:71], v[80:83]
buffer_load_dwordx4 a[52:55], v3, s[48:51], s70 offen offset:3072 sc1
v_mfma_f32_16x16x32_bf16 v[96:99], v[176:179], a[72:75], v[96:99]
)ASM"
R"ASM(buffer_load_dwordx4 a[56:59], v4, s[48:51], s70 offen offset:3072 sc1
v_mfma_f32_16x16x32_bf16 v[112:115], v[176:179], a[76:79], v[112:115]
buffer_load_dwordx4 a[60:63], v5, s[48:51], s70 offen offset:3072 sc1
v_mfma_f32_16x16x32_bf16 v[68:71], v[180:183], a[64:67], v[68:71]
v_mfma_f32_16x16x32_bf16 v[84:87], v[180:183], a[68:71], v[84:87]
v_mfma_f32_16x16x32_bf16 v[100:103], v[180:183], a[72:75], v[100:103]
v_mfma_f32_16x16x32_bf16 v[116:119], v[180:183], a[76:79], v[116:119]
v_mfma_f32_16x16x32_bf16 v[72:75], v[184:187], a[64:67], v[72:75]
v_mfma_f32_16x16x32_bf16 v[88:91], v[184:187], a[68:71], v[88:91]
s_waitcnt vmcnt(16)
v_mfma_f32_16x16x32_bf16 v[104:107], v[184:187], a[72:75], v[104:107]
ds_read_b128 a[112:115], v15 offset:0
v_mfma_f32_16x16x32_bf16 v[120:123], v[184:187], a[76:79], v[120:123]
ds_read_b128 a[116:119], v15 offset:1024
v_mfma_f32_16x16x32_bf16 v[76:79], v[188:191], a[64:67], v[76:79]
ds_read_b128 a[120:123], v15 offset:2048
v_mfma_f32_16x16x32_bf16 v[92:95], v[188:191], a[68:71], v[92:95]
ds_read_b128 a[124:127], v15 offset:3072
v_mfma_f32_16x16x32_bf16 v[108:111], v[188:191], a[72:75], v[108:111]
v_mfma_f32_16x16x32_bf16 v[124:127], v[188:191], a[76:79], v[124:127]
s_waitcnt lgkmcnt(0)
v_mfma_f32_16x16x32_bf16 v[64:67], a[112:115], a[80:83], v[64:67]
s_add_u32 s70, s69, 16384
buffer_load_dwordx4 a[64:67], v2, s[48:51], s70 offen offset:0 sc1
v_mfma_f32_16x16x32_bf16 v[80:83], a[112:115], a[84:87], v[80:83]
buffer_load_dwordx4 a[68:71], v3, s[48:51], s70 offen offset:0 sc1
v_mfma_f32_16x16x32_bf16 v[96:99], a[112:115], a[88:91], v[96:99]
buffer_load_dwordx4 a[72:75], v4, s[48:51], s70 offen offset:0 sc1
v_mfma_f32_16x16x32_bf16 v[112:115], a[112:115], a[92:95], v[112:115]
buffer_load_dwordx4 a[76:79], v5, s[48:51], s70 offen offset:0 sc1
v_mfma_f32_16x16x32_bf16 v[68:71], a[116:119], a[80:83], v[68:71]
v_mfma_f32_16x16x32_bf16 v[84:87], a[116:119], a[84:87], v[84:87]
v_mfma_f32_16x16x32_bf16 v[100:103], a[116:119], a[88:91], v[100:103]
v_mfma_f32_16x16x32_bf16 v[116:119], a[116:119], a[92:95], v[116:119]
v_mfma_f32_16x16x32_bf16 v[72:75], a[120:123], a[80:83], v[72:75]
v_mfma_f32_16x16x32_bf16 v[88:91], a[120:123], a[84:87], v[88:91]
s_waitcnt vmcnt(16)
v_mfma_f32_16x16x32_bf16 v[104:107], a[120:123], a[88:91], v[104:107]
ds_read_b128 a[96:99], v15 offset:16384
v_mfma_f32_16x16x32_bf16 v[120:123], a[120:123], a[92:95], v[120:123]
ds_read_b128 a[100:103], v15 offset:17408
v_mfma_f32_16x16x32_bf16 v[76:79], a[124:127], a[80:83], v[76:79]
ds_read_b128 a[104:107], v15 offset:18432
v_mfma_f32_16x16x32_bf16 v[92:95], a[124:127], a[84:87], v[92:95]
ds_read_b128 a[108:111], v15 offset:19456
v_mfma_f32_16x16x32_bf16 v[108:111], a[124:127], a[88:91], v[108:111]
v_mfma_f32_16x16x32_bf16 v[124:127], a[124:127], a[92:95], v[124:127]
s_waitcnt lgkmcnt(0)
v_mfma_f32_16x16x32_bf16 v[64:67], a[96:99], a[0:3], v[64:67]
s_add_u32 s70, s69, 16384
buffer_load_dwordx4 a[80:83], v2, s[48:51], s70 offen offset:1024 sc1
v_mfma_f32_16x16x32_bf16 v[80:83], a[96:99], a[4:7], v[80:83]
buffer_load_dwordx4 a[84:87], v3, s[48:51], s70 offen offset:1024 sc1
v_mfma_f32_16x16x32_bf16 v[96:99], a[96:99], a[8:11], v[96:99]
buffer_load_dwordx4 a[88:91], v4, s[48:51], s70 offen offset:1024 sc1
v_mfma_f32_16x16x32_bf16 v[112:115], a[96:99], a[12:15], v[112:115]
buffer_load_dwordx4 a[92:95], v5, s[48:51], s70 offen offset:1024 sc1
v_mfma_f32_16x16x32_bf16 v[68:71], a[100:103], a[0:3], v[68:71]
v_mfma_f32_16x16x32_bf16 v[84:87], a[100:103], a[4:7], v[84:87]
v_mfma_f32_16x16x32_bf16 v[100:103], a[100:103], a[8:11], v[100:103]
v_mfma_f32_16x16x32_bf16 v[116:119], a[100:103], a[12:15], v[116:119]
v_mfma_f32_16x16x32_bf16 v[72:75], a[104:107], a[0:3], v[72:75]
v_mfma_f32_16x16x32_bf16 v[88:91], a[104:107], a[4:7], v[88:91]
v_mfma_f32_16x16x32_bf16 v[104:107], a[104:107], a[8:11], v[104:107]
v_mfma_f32_16x16x32_bf16 v[120:123], a[104:107], a[12:15], v[120:123]
v_mfma_f32_16x16x32_bf16 v[76:79], a[108:111], a[0:3], v[76:79]
v_mfma_f32_16x16x32_bf16 v[92:95], a[108:111], a[4:7], v[92:95]
v_mfma_f32_16x16x32_bf16 v[108:111], a[108:111], a[8:11], v[108:111]
v_mfma_f32_16x16x32_bf16 v[124:127], a[108:111], a[12:15], v[124:127]
s_waitcnt vmcnt(16)
v_mfma_f32_16x16x32_bf16 v[64:67], v[192:195], a[16:19], v[64:67]
s_add_u32 s70, s69, 16384
buffer_load_dwordx4 a[0:3], v2, s[48:51], s70 offen offset:2048 sc1
v_mfma_f32_16x16x32_bf16 v[80:83], v[192:195], a[20:23], v[80:83]
buffer_load_dwordx4 a[4:7], v3, s[48:51], s70 offen offset:2048 sc1
v_mfma_f32_16x16x32_bf16 v[96:99], v[192:195], a[24:27], v[96:99]
buffer_load_dwordx4 a[8:11], v4, s[48:51], s70 offen offset:2048 sc1
v_mfma_f32_16x16x32_bf16 v[112:115], v[192:195], a[28:31], v[112:115]
buffer_load_dwordx4 a[12:15], v5, s[48:51], s70 offen offset:2048 sc1
v_mfma_f32_16x16x32_bf16 v[68:71], v[196:199], a[16:19], v[68:71]
v_mfma_f32_16x16x32_bf16 v[84:87], v[196:199], a[20:23], v[84:87]
v_mfma_f32_16x16x32_bf16 v[100:103], v[196:199], a[24:27], v[100:103]
v_mfma_f32_16x16x32_bf16 v[116:119], v[196:199], a[28:31], v[116:119]
v_mfma_f32_16x16x32_bf16 v[72:75], v[200:203], a[16:19], v[72:75]
v_mfma_f32_16x16x32_bf16 v[88:91], v[200:203], a[20:23], v[88:91]
v_mfma_f32_16x16x32_bf16 v[104:107], v[200:203], a[24:27], v[104:107]
v_mfma_f32_16x16x32_bf16 v[120:123], v[200:203], a[28:31], v[120:123]
v_mfma_f32_16x16x32_bf16 v[76:79], v[204:207], a[16:19], v[76:79]
v_mfma_f32_16x16x32_bf16 v[92:95], v[204:207], a[20:23], v[92:95]
v_mfma_f32_16x16x32_bf16 v[108:111], v[204:207], a[24:27], v[108:111]
v_mfma_f32_16x16x32_bf16 v[124:127], v[204:207], a[28:31], v[124:127]
s_waitcnt vmcnt(16)
v_mfma_f32_16x16x32_bf16 v[64:67], v[208:211], a[32:35], v[64:67]
s_add_u32 s70, s69, 16384
buffer_load_dwordx4 a[16:19], v2, s[48:51], s70 offen offset:3072 sc1
v_mfma_f32_16x16x32_bf16 v[80:83], v[208:211], a[36:39], v[80:83]
buffer_load_dwordx4 a[20:23], v3, s[48:51], s70 offen offset:3072 sc1
v_mfma_f32_16x16x32_bf16 v[96:99], v[208:211], a[40:43], v[96:99]
buffer_load_dwordx4 a[24:27], v4, s[48:51], s70 offen offset:3072 sc1
v_mfma_f32_16x16x32_bf16 v[112:115], v[208:211], a[44:47], v[112:115]
buffer_load_dwordx4 a[28:31], v5, s[48:51], s70 offen offset:3072 sc1
v_mfma_f32_16x16x32_bf16 v[68:71], v[212:215], a[32:35], v[68:71]
v_mfma_f32_16x16x32_bf16 v[84:87], v[212:215], a[36:39], v[84:87]
v_mfma_f32_16x16x32_bf16 v[100:103], v[212:215], a[40:43], v[100:103]
v_mfma_f32_16x16x32_bf16 v[116:119], v[212:215], a[44:47], v[116:119]
v_mfma_f32_16x16x32_bf16 v[72:75], v[216:219], a[32:35], v[72:75]
v_mfma_f32_16x16x32_bf16 v[88:91], v[216:219], a[36:39], v[88:91]
v_mfma_f32_16x16x32_bf16 v[104:107], v[216:219], a[40:43], v[104:107]
v_mfma_f32_16x16x32_bf16 v[120:123], v[216:219], a[44:47], v[120:123]
v_mfma_f32_16x16x32_bf16 v[76:79], v[220:223], a[32:35], v[76:79]
v_mfma_f32_16x16x32_bf16 v[92:95], v[220:223], a[36:39], v[92:95]
v_mfma_f32_16x16x32_bf16 v[108:111], v[220:223], a[40:43], v[108:111]
v_mfma_f32_16x16x32_bf16 v[124:127], v[220:223], a[44:47], v[124:127]
s_waitcnt vmcnt(16)
v_mfma_f32_16x16x32_bf16 v[64:67], v[224:227], a[48:51], v[64:67]
s_add_u32 s70, s69, 20480
buffer_load_dwordx4 a[32:35], v2, s[48:51], s70 offen offset:0 sc1
v_mfma_f32_16x16x32_bf16 v[80:83], v[224:227], a[52:55], v[80:83]
buffer_load_dwordx4 a[36:39], v3, s[48:51], s70 offen offset:0 sc1
v_mfma_f32_16x16x32_bf16 v[96:99], v[224:227], a[56:59], v[96:99]
buffer_load_dwordx4 a[40:43], v4, s[48:51], s70 offen offset:0 sc1
v_mfma_f32_16x16x32_bf16 v[112:115], v[224:227], a[60:63], v[112:115]
buffer_load_dwordx4 a[44:47], v5, s[48:51], s70 offen offset:0 sc1
v_mfma_f32_16x16x32_bf16 v[68:71], v[228:231], a[48:51], v[68:71]
v_mfma_f32_16x16x32_bf16 v[84:87], v[228:231], a[52:55], v[84:87]
v_mfma_f32_16x16x32_bf16 v[100:103], v[228:231], a[56:59], v[100:103]
v_mfma_f32_16x16x32_bf16 v[116:119], v[228:231], a[60:63], v[116:119]
v_mfma_f32_16x16x32_bf16 v[72:75], v[232:235], a[48:51], v[72:75]
v_mfma_f32_16x16x32_bf16 v[88:91], v[232:235], a[52:55], v[88:91]
v_mfma_f32_16x16x32_bf16 v[104:107], v[232:235], a[56:59], v[104:107]
v_mfma_f32_16x16x32_bf16 v[120:123], v[232:235], a[60:63], v[120:123]
v_mfma_f32_16x16x32_bf16 v[76:79], v[236:239], a[48:51], v[76:79]
v_mfma_f32_16x16x32_bf16 v[92:95], v[236:239], a[52:55], v[92:95]
v_mfma_f32_16x16x32_bf16 v[108:111], v[236:239], a[56:59], v[108:111]
v_mfma_f32_16x16x32_bf16 v[124:127], v[236:239], a[60:63], v[124:127]
s_waitcnt vmcnt(16)
v_mfma_f32_16x16x32_bf16 v[64:67], v[240:243], a[64:67], v[64:67]
s_add_u32 s70, s69, 20480
buffer_load_dwordx4 a[48:51], v2, s[48:51], s70 offen offset:1024 sc1
v_mfma_f32_16x16x32_bf16 v[80:83], v[240:243], a[68:71], v[80:83]
buffer_load_dwordx4 a[52:55], v3, s[48:51], s70 offen offset:1024 sc1
v_mfma_f32_16x16x32_bf16 v[96:99], v[240:243], a[72:75], v[96:99]
buffer_load_dwordx4 a[56:59], v4, s[48:51], s70 offen offset:1024 sc1
v_mfma_f32_16x16x32_bf16 v[112:115], v[240:243], a[76:79], v[112:115]
buffer_load_dwordx4 a[60:63], v5, s[48:51], s70 offen offset:1024 sc1
v_mfma_f32_16x16x32_bf16 v[68:71], v[244:247], a[64:67], v[68:71]
v_mfma_f32_16x16x32_bf16 v[84:87], v[244:247], a[68:71], v[84:87]
v_mfma_f32_16x16x32_bf16 v[100:103], v[244:247], a[72:75], v[100:103]
v_mfma_f32_16x16x32_bf16 v[116:119], v[244:247], a[76:79], v[116:119]
v_mfma_f32_16x16x32_bf16 v[72:75], v[248:251], a[64:67], v[72:75]
v_mfma_f32_16x16x32_bf16 v[88:91], v[248:251], a[68:71], v[88:91]
s_waitcnt vmcnt(16)
v_mfma_f32_16x16x32_bf16 v[104:107], v[248:251], a[72:75], v[104:107]
ds_read_b128 a[112:115], v13 offset:0
v_mfma_f32_16x16x32_bf16 v[120:123], v[248:251], a[76:79], v[120:123]
ds_read_b128 a[116:119], v13 offset:1024
v_mfma_f32_16x16x32_bf16 v[76:79], v[252:255], a[64:67], v[76:79]
ds_read_b128 a[120:123], v13 offset:2048
v_mfma_f32_16x16x32_bf16 v[92:95], v[252:255], a[68:71], v[92:95]
ds_read_b128 a[124:127], v13 offset:3072
v_mfma_f32_16x16x32_bf16 v[108:111], v[252:255], a[72:75], v[108:111]
v_mfma_f32_16x16x32_bf16 v[124:127], v[252:255], a[76:79], v[124:127]
s_waitcnt lgkmcnt(0)
v_mfma_f32_16x16x32_bf16 v[64:67], a[112:115], a[80:83], v[64:67]
s_add_u32 s70, s69, 20480
buffer_load_dwordx4 a[64:67], v2, s[48:51], s70 offen offset:2048 sc1
v_mfma_f32_16x16x32_bf16 v[80:83], a[112:115], a[84:87], v[80:83]
buffer_load_dwordx4 a[68:71], v3, s[48:51], s70 offen offset:2048 sc1
v_mfma_f32_16x16x32_bf16 v[96:99], a[112:115], a[88:91], v[96:99]
buffer_load_dwordx4 a[72:75], v4, s[48:51], s70 offen offset:2048 sc1
v_mfma_f32_16x16x32_bf16 v[112:115], a[112:115], a[92:95], v[112:115]
buffer_load_dwordx4 a[76:79], v5, s[48:51], s70 offen offset:2048 sc1
v_mfma_f32_16x16x32_bf16 v[68:71], a[116:119], a[80:83], v[68:71]
v_mfma_f32_16x16x32_bf16 v[84:87], a[116:119], a[84:87], v[84:87]
v_mfma_f32_16x16x32_bf16 v[100:103], a[116:119], a[88:91], v[100:103]
v_mfma_f32_16x16x32_bf16 v[116:119], a[116:119], a[92:95], v[116:119]
v_mfma_f32_16x16x32_bf16 v[72:75], a[120:123], a[80:83], v[72:75]
v_mfma_f32_16x16x32_bf16 v[88:91], a[120:123], a[84:87], v[88:91]
s_waitcnt vmcnt(16)
v_mfma_f32_16x16x32_bf16 v[104:107], a[120:123], a[88:91], v[104:107]
ds_read_b128 a[96:99], v13 offset:16384
v_mfma_f32_16x16x32_bf16 v[120:123], a[120:123], a[92:95], v[120:123]
ds_read_b128 a[100:103], v13 offset:17408
v_mfma_f32_16x16x32_bf16 v[76:79], a[124:127], a[80:83], v[76:79]
ds_read_b128 a[104:107], v13 offset:18432
v_mfma_f32_16x16x32_bf16 v[92:95], a[124:127], a[84:87], v[92:95]
ds_read_b128 a[108:111], v13 offset:19456
v_mfma_f32_16x16x32_bf16 v[108:111], a[124:127], a[88:91], v[108:111]
v_mfma_f32_16x16x32_bf16 v[124:127], a[124:127], a[92:95], v[124:127]
s_waitcnt lgkmcnt(0)
v_mfma_f32_16x16x32_bf16 v[64:67], a[96:99], a[0:3], v[64:67]
s_add_u32 s70, s69, 20480
buffer_load_dwordx4 a[80:83], v2, s[48:51], s70 offen offset:3072 sc1
v_mfma_f32_16x16x32_bf16 v[80:83], a[96:99], a[4:7], v[80:83]
buffer_load_dwordx4 a[84:87], v3, s[48:51], s70 offen offset:3072 sc1
v_mfma_f32_16x16x32_bf16 v[96:99], a[96:99], a[8:11], v[96:99]
buffer_load_dwordx4 a[88:91], v4, s[48:51], s70 offen offset:3072 sc1
v_mfma_f32_16x16x32_bf16 v[112:115], a[96:99], a[12:15], v[112:115]
buffer_load_dwordx4 a[92:95], v5, s[48:51], s70 offen offset:3072 sc1
v_mfma_f32_16x16x32_bf16 v[68:71], a[100:103], a[0:3], v[68:71]
v_mfma_f32_16x16x32_bf16 v[84:87], a[100:103], a[4:7], v[84:87]
v_mfma_f32_16x16x32_bf16 v[100:103], a[100:103], a[8:11], v[100:103]
v_mfma_f32_16x16x32_bf16 v[116:119], a[100:103], a[12:15], v[116:119]
v_mfma_f32_16x16x32_bf16 v[72:75], a[104:107], a[0:3], v[72:75]
v_mfma_f32_16x16x32_bf16 v[88:91], a[104:107], a[4:7], v[88:91]
s_waitcnt vmcnt(16)
v_mfma_f32_16x16x32_bf16 v[104:107], a[104:107], a[8:11], v[104:107]
ds_read_b128 a[112:115], v13 offset:32768
v_mfma_f32_16x16x32_bf16 v[120:123], a[104:107], a[12:15], v[120:123]
ds_read_b128 a[116:119], v13 offset:33792
v_mfma_f32_16x16x32_bf16 v[76:79], a[108:111], a[0:3], v[76:79]
ds_read_b128 a[120:123], v13 offset:34816
v_mfma_f32_16x16x32_bf16 v[92:95], a[108:111], a[4:7], v[92:95]
ds_read_b128 a[124:127], v13 offset:35840
v_mfma_f32_16x16x32_bf16 v[108:111], a[108:111], a[8:11], v[108:111]
v_mfma_f32_16x16x32_bf16 v[124:127], a[108:111], a[12:15], v[124:127]
s_waitcnt lgkmcnt(0)
v_mfma_f32_16x16x32_bf16 v[64:67], a[112:115], a[16:19], v[64:67]
s_add_u32 s70, s69, 24576
buffer_load_dwordx4 a[0:3], v2, s[48:51], s70 offen offset:0 sc1
v_mfma_f32_16x16x32_bf16 v[80:83], a[112:115], a[20:23], v[80:83]
buffer_load_dwordx4 a[4:7], v3, s[48:51], s70 offen offset:0 sc1
v_mfma_f32_16x16x32_bf16 v[96:99], a[112:115], a[24:27], v[96:99]
buffer_load_dwordx4 a[8:11], v4, s[48:51], s70 offen offset:0 sc1
v_mfma_f32_16x16x32_bf16 v[112:115], a[112:115], a[28:31], v[112:115]
buffer_load_dwordx4 a[12:15], v5, s[48:51], s70 offen offset:0 sc1
v_mfma_f32_16x16x32_bf16 v[68:71], a[116:119], a[16:19], v[68:71]
v_mfma_f32_16x16x32_bf16 v[84:87], a[116:119], a[20:23], v[84:87]
v_mfma_f32_16x16x32_bf16 v[100:103], a[116:119], a[24:27], v[100:103]
v_mfma_f32_16x16x32_bf16 v[116:119], a[116:119], a[28:31], v[116:119]
v_mfma_f32_16x16x32_bf16 v[72:75], a[120:123], a[16:19], v[72:75]
v_mfma_f32_16x16x32_bf16 v[88:91], a[120:123], a[20:23], v[88:91]
s_waitcnt vmcnt(16)
v_mfma_f32_16x16x32_bf16 v[104:107], a[120:123], a[24:27], v[104:107]
ds_read_b128 a[96:99], v13 offset:49152
v_mfma_f32_16x16x32_bf16 v[120:123], a[120:123], a[28:31], v[120:123]
ds_read_b128 a[100:103], v13 offset:50176
v_mfma_f32_16x16x32_bf16 v[76:79], a[124:127], a[16:19], v[76:79]
ds_read_b128 a[104:107], v13 offset:51200
v_mfma_f32_16x16x32_bf16 v[92:95], a[124:127], a[20:23], v[92:95]
ds_read_b128 a[108:111], v13 offset:52224
v_mfma_f32_16x16x32_bf16 v[108:111], a[124:127], a[24:27], v[108:111]
v_mfma_f32_16x16x32_bf16 v[124:127], a[124:127], a[28:31], v[124:127]
s_waitcnt lgkmcnt(0)
v_mfma_f32_16x16x32_bf16 v[64:67], a[96:99], a[32:35], v[64:67]
s_add_u32 s70, s69, 24576
buffer_load_dwordx4 a[16:19], v2, s[48:51], s70 offen offset:1024 sc1
v_mfma_f32_16x16x32_bf16 v[80:83], a[96:99], a[36:39], v[80:83]
buffer_load_dwordx4 a[20:23], v3, s[48:51], s70 offen offset:1024 sc1
v_mfma_f32_16x16x32_bf16 v[96:99], a[96:99], a[40:43], v[96:99]
buffer_load_dwordx4 a[24:27], v4, s[48:51], s70 offen offset:1024 sc1
v_mfma_f32_16x16x32_bf16 v[112:115], a[96:99], a[44:47], v[112:115]
buffer_load_dwordx4 a[28:31], v5, s[48:51], s70 offen offset:1024 sc1
v_mfma_f32_16x16x32_bf16 v[68:71], a[100:103], a[32:35], v[68:71]
v_mfma_f32_16x16x32_bf16 v[84:87], a[100:103], a[36:39], v[84:87]
v_mfma_f32_16x16x32_bf16 v[100:103], a[100:103], a[40:43], v[100:103]
v_mfma_f32_16x16x32_bf16 v[116:119], a[100:103], a[44:47], v[116:119]
v_mfma_f32_16x16x32_bf16 v[72:75], a[104:107], a[32:35], v[72:75]
v_mfma_f32_16x16x32_bf16 v[88:91], a[104:107], a[36:39], v[88:91]
s_waitcnt vmcnt(16)
v_mfma_f32_16x16x32_bf16 v[104:107], a[104:107], a[40:43], v[104:107]
ds_read_b128 a[112:115], v14 offset:0
v_mfma_f32_16x16x32_bf16 v[120:123], a[104:107], a[44:47], v[120:123]
ds_read_b128 a[116:119], v14 offset:1024
v_mfma_f32_16x16x32_bf16 v[76:79], a[108:111], a[32:35], v[76:79]
ds_read_b128 a[120:123], v14 offset:2048
v_mfma_f32_16x16x32_bf16 v[92:95], a[108:111], a[36:39], v[92:95]
ds_read_b128 a[124:127], v14 offset:3072
v_mfma_f32_16x16x32_bf16 v[108:111], a[108:111], a[40:43], v[108:111]
v_mfma_f32_16x16x32_bf16 v[124:127], a[108:111], a[44:47], v[124:127]
s_waitcnt lgkmcnt(0)
v_mfma_f32_16x16x32_bf16 v[64:67], a[112:115], a[48:51], v[64:67]
s_add_u32 s70, s69, 24576
buffer_load_dwordx4 a[32:35], v2, s[48:51], s70 offen offset:2048 sc1
v_mfma_f32_16x16x32_bf16 v[80:83], a[112:115], a[52:55], v[80:83]
buffer_load_dwordx4 a[36:39], v3, s[48:51], s70 offen offset:2048 sc1
v_mfma_f32_16x16x32_bf16 v[96:99], a[112:115], a[56:59], v[96:99]
buffer_load_dwordx4 a[40:43], v4, s[48:51], s70 offen offset:2048 sc1
v_mfma_f32_16x16x32_bf16 v[112:115], a[112:115], a[60:63], v[112:115]
buffer_load_dwordx4 a[44:47], v5, s[48:51], s70 offen offset:2048 sc1
v_mfma_f32_16x16x32_bf16 v[68:71], a[116:119], a[48:51], v[68:71]
v_mfma_f32_16x16x32_bf16 v[84:87], a[116:119], a[52:55], v[84:87]
v_mfma_f32_16x16x32_bf16 v[100:103], a[116:119], a[56:59], v[100:103]
v_mfma_f32_16x16x32_bf16 v[116:119], a[116:119], a[60:63], v[116:119]
v_mfma_f32_16x16x32_bf16 v[72:75], a[120:123], a[48:51], v[72:75]
v_mfma_f32_16x16x32_bf16 v[88:91], a[120:123], a[52:55], v[88:91]
s_waitcnt vmcnt(16)
v_mfma_f32_16x16x32_bf16 v[104:107], a[120:123], a[56:59], v[104:107]
ds_read_b128 a[96:99], v14 offset:16384
v_mfma_f32_16x16x32_bf16 v[120:123], a[120:123], a[60:63], v[120:123]
ds_read_b128 a[100:103], v14 offset:17408
v_mfma_f32_16x16x32_bf16 v[76:79], a[124:127], a[48:51], v[76:79]
ds_read_b128 a[104:107], v14 offset:18432
v_mfma_f32_16x16x32_bf16 v[92:95], a[124:127], a[52:55], v[92:95]
ds_read_b128 a[108:111], v14 offset:19456
v_mfma_f32_16x16x32_bf16 v[108:111], a[124:127], a[56:59], v[108:111]
v_mfma_f32_16x16x32_bf16 v[124:127], a[124:127], a[60:63], v[124:127]
s_waitcnt lgkmcnt(0)
v_mfma_f32_16x16x32_bf16 v[64:67], a[96:99], a[64:67], v[64:67]
s_add_u32 s70, s69, 24576
)ASM"
R"ASM(buffer_load_dwordx4 a[48:51], v2, s[48:51], s70 offen offset:3072 sc1
v_mfma_f32_16x16x32_bf16 v[80:83], a[96:99], a[68:71], v[80:83]
buffer_load_dwordx4 a[52:55], v3, s[48:51], s70 offen offset:3072 sc1
v_mfma_f32_16x16x32_bf16 v[96:99], a[96:99], a[72:75], v[96:99]
buffer_load_dwordx4 a[56:59], v4, s[48:51], s70 offen offset:3072 sc1
v_mfma_f32_16x16x32_bf16 v[112:115], a[96:99], a[76:79], v[112:115]
buffer_load_dwordx4 a[60:63], v5, s[48:51], s70 offen offset:3072 sc1
v_mfma_f32_16x16x32_bf16 v[68:71], a[100:103], a[64:67], v[68:71]
v_mfma_f32_16x16x32_bf16 v[84:87], a[100:103], a[68:71], v[84:87]
v_mfma_f32_16x16x32_bf16 v[100:103], a[100:103], a[72:75], v[100:103]
v_mfma_f32_16x16x32_bf16 v[116:119], a[100:103], a[76:79], v[116:119]
v_mfma_f32_16x16x32_bf16 v[72:75], a[104:107], a[64:67], v[72:75]
v_mfma_f32_16x16x32_bf16 v[88:91], a[104:107], a[68:71], v[88:91]
s_waitcnt vmcnt(16)
v_mfma_f32_16x16x32_bf16 v[104:107], a[104:107], a[72:75], v[104:107]
ds_read_b128 a[112:115], v14 offset:32768
v_mfma_f32_16x16x32_bf16 v[120:123], a[104:107], a[76:79], v[120:123]
ds_read_b128 a[116:119], v14 offset:33792
v_mfma_f32_16x16x32_bf16 v[76:79], a[108:111], a[64:67], v[76:79]
ds_read_b128 a[120:123], v14 offset:34816
v_mfma_f32_16x16x32_bf16 v[92:95], a[108:111], a[68:71], v[92:95]
ds_read_b128 a[124:127], v14 offset:35840
v_mfma_f32_16x16x32_bf16 v[108:111], a[108:111], a[72:75], v[108:111]
v_mfma_f32_16x16x32_bf16 v[124:127], a[108:111], a[76:79], v[124:127]
s_waitcnt lgkmcnt(0)
v_mfma_f32_16x16x32_bf16 v[64:67], a[112:115], a[80:83], v[64:67]
s_add_u32 s70, s69, 28672
buffer_load_dwordx4 a[64:67], v2, s[48:51], s70 offen offset:0 sc1
v_mfma_f32_16x16x32_bf16 v[80:83], a[112:115], a[84:87], v[80:83]
buffer_load_dwordx4 a[68:71], v3, s[48:51], s70 offen offset:0 sc1
v_mfma_f32_16x16x32_bf16 v[96:99], a[112:115], a[88:91], v[96:99]
buffer_load_dwordx4 a[72:75], v4, s[48:51], s70 offen offset:0 sc1
v_mfma_f32_16x16x32_bf16 v[112:115], a[112:115], a[92:95], v[112:115]
buffer_load_dwordx4 a[76:79], v5, s[48:51], s70 offen offset:0 sc1
v_mfma_f32_16x16x32_bf16 v[68:71], a[116:119], a[80:83], v[68:71]
v_mfma_f32_16x16x32_bf16 v[84:87], a[116:119], a[84:87], v[84:87]
v_mfma_f32_16x16x32_bf16 v[100:103], a[116:119], a[88:91], v[100:103]
v_mfma_f32_16x16x32_bf16 v[116:119], a[116:119], a[92:95], v[116:119]
v_mfma_f32_16x16x32_bf16 v[72:75], a[120:123], a[80:83], v[72:75]
v_mfma_f32_16x16x32_bf16 v[88:91], a[120:123], a[84:87], v[88:91]
s_waitcnt vmcnt(16)
v_mfma_f32_16x16x32_bf16 v[104:107], a[120:123], a[88:91], v[104:107]
ds_read_b128 a[96:99], v14 offset:49152
v_mfma_f32_16x16x32_bf16 v[120:123], a[120:123], a[92:95], v[120:123]
ds_read_b128 a[100:103], v14 offset:50176
v_mfma_f32_16x16x32_bf16 v[76:79], a[124:127], a[80:83], v[76:79]
ds_read_b128 a[104:107], v14 offset:51200
v_mfma_f32_16x16x32_bf16 v[92:95], a[124:127], a[84:87], v[92:95]
ds_read_b128 a[108:111], v14 offset:52224
v_mfma_f32_16x16x32_bf16 v[108:111], a[124:127], a[88:91], v[108:111]
v_mfma_f32_16x16x32_bf16 v[124:127], a[124:127], a[92:95], v[124:127]
s_waitcnt lgkmcnt(0)
v_mfma_f32_16x16x32_bf16 v[64:67], a[96:99], a[0:3], v[64:67]
s_add_u32 s70, s69, 28672
buffer_load_dwordx4 a[80:83], v2, s[48:51], s70 offen offset:1024 sc1
v_mfma_f32_16x16x32_bf16 v[80:83], a[96:99], a[4:7], v[80:83]
buffer_load_dwordx4 a[84:87], v3, s[48:51], s70 offen offset:1024 sc1
v_mfma_f32_16x16x32_bf16 v[96:99], a[96:99], a[8:11], v[96:99]
buffer_load_dwordx4 a[88:91], v4, s[48:51], s70 offen offset:1024 sc1
v_mfma_f32_16x16x32_bf16 v[112:115], a[96:99], a[12:15], v[112:115]
buffer_load_dwordx4 a[92:95], v5, s[48:51], s70 offen offset:1024 sc1
v_mfma_f32_16x16x32_bf16 v[68:71], a[100:103], a[0:3], v[68:71]
v_mfma_f32_16x16x32_bf16 v[84:87], a[100:103], a[4:7], v[84:87]
v_mfma_f32_16x16x32_bf16 v[100:103], a[100:103], a[8:11], v[100:103]
v_mfma_f32_16x16x32_bf16 v[116:119], a[100:103], a[12:15], v[116:119]
v_mfma_f32_16x16x32_bf16 v[72:75], a[104:107], a[0:3], v[72:75]
v_mfma_f32_16x16x32_bf16 v[88:91], a[104:107], a[4:7], v[88:91]
v_mfma_f32_16x16x32_bf16 v[104:107], a[104:107], a[8:11], v[104:107]
v_mfma_f32_16x16x32_bf16 v[120:123], a[104:107], a[12:15], v[120:123]
v_mfma_f32_16x16x32_bf16 v[76:79], a[108:111], a[0:3], v[76:79]
v_mfma_f32_16x16x32_bf16 v[92:95], a[108:111], a[4:7], v[92:95]
v_mfma_f32_16x16x32_bf16 v[108:111], a[108:111], a[8:11], v[108:111]
v_mfma_f32_16x16x32_bf16 v[124:127], a[108:111], a[12:15], v[124:127]
s_waitcnt vmcnt(16)
v_mfma_f32_16x16x32_bf16 v[64:67], v[16:19], a[16:19], v[64:67]
s_add_u32 s70, s69, 28672
buffer_load_dwordx4 a[0:3], v2, s[48:51], s70 offen offset:2048 sc1
v_mfma_f32_16x16x32_bf16 v[80:83], v[16:19], a[20:23], v[80:83]
buffer_load_dwordx4 a[4:7], v3, s[48:51], s70 offen offset:2048 sc1
v_mfma_f32_16x16x32_bf16 v[96:99], v[16:19], a[24:27], v[96:99]
buffer_load_dwordx4 a[8:11], v4, s[48:51], s70 offen offset:2048 sc1
v_mfma_f32_16x16x32_bf16 v[112:115], v[16:19], a[28:31], v[112:115]
buffer_load_dwordx4 a[12:15], v5, s[48:51], s70 offen offset:2048 sc1
v_mfma_f32_16x16x32_bf16 v[68:71], v[20:23], a[16:19], v[68:71]
v_mfma_f32_16x16x32_bf16 v[84:87], v[20:23], a[20:23], v[84:87]
v_mfma_f32_16x16x32_bf16 v[100:103], v[20:23], a[24:27], v[100:103]
v_mfma_f32_16x16x32_bf16 v[116:119], v[20:23], a[28:31], v[116:119]
v_mfma_f32_16x16x32_bf16 v[72:75], v[24:27], a[16:19], v[72:75]
v_mfma_f32_16x16x32_bf16 v[88:91], v[24:27], a[20:23], v[88:91]
v_mfma_f32_16x16x32_bf16 v[104:107], v[24:27], a[24:27], v[104:107]
v_mfma_f32_16x16x32_bf16 v[120:123], v[24:27], a[28:31], v[120:123]
v_mfma_f32_16x16x32_bf16 v[76:79], v[28:31], a[16:19], v[76:79]
v_mfma_f32_16x16x32_bf16 v[92:95], v[28:31], a[20:23], v[92:95]
v_mfma_f32_16x16x32_bf16 v[108:111], v[28:31], a[24:27], v[108:111]
v_mfma_f32_16x16x32_bf16 v[124:127], v[28:31], a[28:31], v[124:127]
s_waitcnt vmcnt(16)
v_mfma_f32_16x16x32_bf16 v[64:67], v[32:35], a[32:35], v[64:67]
s_add_u32 s70, s69, 28672
buffer_load_dwordx4 a[16:19], v2, s[48:51], s70 offen offset:3072 sc1
v_mfma_f32_16x16x32_bf16 v[80:83], v[32:35], a[36:39], v[80:83]
buffer_load_dwordx4 a[20:23], v3, s[48:51], s70 offen offset:3072 sc1
v_mfma_f32_16x16x32_bf16 v[96:99], v[32:35], a[40:43], v[96:99]
buffer_load_dwordx4 a[24:27], v4, s[48:51], s70 offen offset:3072 sc1
v_mfma_f32_16x16x32_bf16 v[112:115], v[32:35], a[44:47], v[112:115]
buffer_load_dwordx4 a[28:31], v5, s[48:51], s70 offen offset:3072 sc1
v_mfma_f32_16x16x32_bf16 v[68:71], v[36:39], a[32:35], v[68:71]
v_mfma_f32_16x16x32_bf16 v[84:87], v[36:39], a[36:39], v[84:87]
v_mfma_f32_16x16x32_bf16 v[100:103], v[36:39], a[40:43], v[100:103]
v_mfma_f32_16x16x32_bf16 v[116:119], v[36:39], a[44:47], v[116:119]
v_mfma_f32_16x16x32_bf16 v[72:75], v[40:43], a[32:35], v[72:75]
v_mfma_f32_16x16x32_bf16 v[88:91], v[40:43], a[36:39], v[88:91]
v_mfma_f32_16x16x32_bf16 v[104:107], v[40:43], a[40:43], v[104:107]
v_mfma_f32_16x16x32_bf16 v[120:123], v[40:43], a[44:47], v[120:123]
v_mfma_f32_16x16x32_bf16 v[76:79], v[44:47], a[32:35], v[76:79]
v_mfma_f32_16x16x32_bf16 v[92:95], v[44:47], a[36:39], v[92:95]
v_mfma_f32_16x16x32_bf16 v[108:111], v[44:47], a[40:43], v[108:111]
v_mfma_f32_16x16x32_bf16 v[124:127], v[44:47], a[44:47], v[124:127]
s_waitcnt vmcnt(16)
v_mfma_f32_16x16x32_bf16 v[64:67], v[48:51], a[48:51], v[64:67]
buffer_load_dwordx4 v[176:179], v6, s[44:47], s72 offen offset:0 sc1
v_mfma_f32_16x16x32_bf16 v[80:83], v[48:51], a[52:55], v[80:83]
buffer_load_dwordx4 v[180:183], v6, s[44:47], s72 offen offset:64 sc1
v_mfma_f32_16x16x32_bf16 v[96:99], v[48:51], a[56:59], v[96:99]
buffer_load_dwordx4 v[184:187], v6, s[44:47], s72 offen offset:128 sc1
v_mfma_f32_16x16x32_bf16 v[112:115], v[48:51], a[60:63], v[112:115]
buffer_load_dwordx4 v[188:191], v6, s[44:47], s72 offen offset:192 sc1
v_mfma_f32_16x16x32_bf16 v[68:71], v[52:55], a[48:51], v[68:71]
v_mfma_f32_16x16x32_bf16 v[84:87], v[52:55], a[52:55], v[84:87]
v_mfma_f32_16x16x32_bf16 v[100:103], v[52:55], a[56:59], v[100:103]
v_mfma_f32_16x16x32_bf16 v[116:119], v[52:55], a[60:63], v[116:119]
v_mfma_f32_16x16x32_bf16 v[72:75], v[56:59], a[48:51], v[72:75]
v_mfma_f32_16x16x32_bf16 v[88:91], v[56:59], a[52:55], v[88:91]
v_mfma_f32_16x16x32_bf16 v[104:107], v[56:59], a[56:59], v[104:107]
v_mfma_f32_16x16x32_bf16 v[120:123], v[56:59], a[60:63], v[120:123]
v_mfma_f32_16x16x32_bf16 v[76:79], v[60:63], a[48:51], v[76:79]
v_mfma_f32_16x16x32_bf16 v[92:95], v[60:63], a[52:55], v[92:95]
v_mfma_f32_16x16x32_bf16 v[108:111], v[60:63], a[56:59], v[108:111]
v_mfma_f32_16x16x32_bf16 v[124:127], v[60:63], a[60:63], v[124:127]
s_waitcnt vmcnt(16)
v_mfma_f32_16x16x32_bf16 v[64:67], a[132:135], a[64:67], v[64:67]
buffer_load_dwordx4 v[160:163], v6, s[44:47], s73 offen offset:0 sc1
v_mfma_f32_16x16x32_bf16 v[80:83], a[132:135], a[68:71], v[80:83]
buffer_load_dwordx4 v[164:167], v6, s[44:47], s73 offen offset:64 sc1
v_mfma_f32_16x16x32_bf16 v[96:99], a[132:135], a[72:75], v[96:99]
buffer_load_dwordx4 v[168:171], v6, s[44:47], s73 offen offset:128 sc1
v_mfma_f32_16x16x32_bf16 v[112:115], a[132:135], a[76:79], v[112:115]
buffer_load_dwordx4 v[172:175], v6, s[44:47], s73 offen offset:192 sc1
v_mfma_f32_16x16x32_bf16 v[68:71], a[136:139], a[64:67], v[68:71]
v_mfma_f32_16x16x32_bf16 v[84:87], a[136:139], a[68:71], v[84:87]
v_mfma_f32_16x16x32_bf16 v[100:103], a[136:139], a[72:75], v[100:103]
v_mfma_f32_16x16x32_bf16 v[116:119], a[136:139], a[76:79], v[116:119]
v_mfma_f32_16x16x32_bf16 v[72:75], a[140:143], a[64:67], v[72:75]
v_mfma_f32_16x16x32_bf16 v[88:91], a[140:143], a[68:71], v[88:91]
v_mfma_f32_16x16x32_bf16 v[104:107], a[140:143], a[72:75], v[104:107]
v_mfma_f32_16x16x32_bf16 v[120:123], a[140:143], a[76:79], v[120:123]
v_mfma_f32_16x16x32_bf16 v[76:79], a[144:147], a[64:67], v[76:79]
v_mfma_f32_16x16x32_bf16 v[92:95], a[144:147], a[68:71], v[92:95]
v_mfma_f32_16x16x32_bf16 v[108:111], a[144:147], a[72:75], v[108:111]
v_mfma_f32_16x16x32_bf16 v[124:127], a[144:147], a[76:79], v[124:127]
s_waitcnt vmcnt(16)
v_mfma_f32_16x16x32_bf16 v[64:67], a[148:151], a[80:83], v[64:67]
buffer_load_dwordx2 v[192:193], v7, s[56:59], s63 offen offset:0
v_mfma_f32_16x16x32_bf16 v[80:83], a[148:151], a[84:87], v[80:83]
buffer_load_dwordx2 v[194:195], v7, s[56:59], s63 offen offset:32
v_mfma_f32_16x16x32_bf16 v[96:99], a[148:151], a[88:91], v[96:99]
buffer_load_dwordx2 v[196:197], v7, s[56:59], s63 offen offset:64
v_mfma_f32_16x16x32_bf16 v[112:115], a[148:151], a[92:95], v[112:115]
buffer_load_dwordx2 v[198:199], v7, s[56:59], s63 offen offset:96
v_mfma_f32_16x16x32_bf16 v[68:71], a[152:155], a[80:83], v[68:71]
v_mfma_f32_16x16x32_bf16 v[84:87], a[152:155], a[84:87], v[84:87]
v_mfma_f32_16x16x32_bf16 v[100:103], a[152:155], a[88:91], v[100:103]
v_mfma_f32_16x16x32_bf16 v[116:119], a[152:155], a[92:95], v[116:119]
v_mfma_f32_16x16x32_bf16 v[72:75], a[156:159], a[80:83], v[72:75]
v_mfma_f32_16x16x32_bf16 v[88:91], a[156:159], a[84:87], v[88:91]
v_mfma_f32_16x16x32_bf16 v[104:107], a[156:159], a[88:91], v[104:107]
v_mfma_f32_16x16x32_bf16 v[120:123], a[156:159], a[92:95], v[120:123]
v_mfma_f32_16x16x32_bf16 v[76:79], a[160:163], a[80:83], v[76:79]
v_mfma_f32_16x16x32_bf16 v[92:95], a[160:163], a[84:87], v[92:95]
v_mfma_f32_16x16x32_bf16 v[108:111], a[160:163], a[88:91], v[108:111]
v_mfma_f32_16x16x32_bf16 v[124:127], a[160:163], a[92:95], v[124:127]
s_waitcnt vmcnt(16)
v_mfma_f32_16x16x32_bf16 v[64:67], a[164:167], a[0:3], v[64:67]
v_mfma_f32_16x16x32_bf16 v[80:83], a[164:167], a[4:7], v[80:83]
v_mfma_f32_16x16x32_bf16 v[96:99], a[164:167], a[8:11], v[96:99]
v_mfma_f32_16x16x32_bf16 v[112:115], a[164:167], a[12:15], v[112:115]
v_mfma_f32_16x16x32_bf16 v[68:71], a[168:171], a[0:3], v[68:71]
v_mfma_f32_16x16x32_bf16 v[84:87], a[168:171], a[4:7], v[84:87]
v_mfma_f32_16x16x32_bf16 v[100:103], a[168:171], a[8:11], v[100:103]
v_mfma_f32_16x16x32_bf16 v[116:119], a[168:171], a[12:15], v[116:119]
v_mfma_f32_16x16x32_bf16 v[72:75], a[172:175], a[0:3], v[72:75]
v_mfma_f32_16x16x32_bf16 v[88:91], a[172:175], a[4:7], v[88:91]
v_mfma_f32_16x16x32_bf16 v[104:107], a[172:175], a[8:11], v[104:107]
v_mfma_f32_16x16x32_bf16 v[120:123], a[172:175], a[12:15], v[120:123]
v_mfma_f32_16x16x32_bf16 v[76:79], a[176:179], a[0:3], v[76:79]
v_mfma_f32_16x16x32_bf16 v[92:95], a[176:179], a[4:7], v[92:95]
v_mfma_f32_16x16x32_bf16 v[108:111], a[176:179], a[8:11], v[108:111]
v_mfma_f32_16x16x32_bf16 v[124:127], a[176:179], a[12:15], v[124:127]
s_waitcnt vmcnt(12)
v_mfma_f32_16x16x32_bf16 v[64:67], a[180:183], a[16:19], v[64:67]
s_add_u32 s62, s39, 1
v_mfma_f32_16x16x32_bf16 v[80:83], a[180:183], a[20:23], v[80:83]
s_and_b32 s62, s62, 3
v_mfma_f32_16x16x32_bf16 v[96:99], a[180:183], a[24:27], v[96:99]
s_lshl_b32 s62, s62, 15
v_mfma_f32_16x16x32_bf16 v[112:115], a[180:183], a[28:31], v[112:115]
s_add_u32 s62, s62, s67
v_mfma_f32_16x16x32_bf16 v[68:71], a[184:187], a[16:19], v[68:71]
v_add_u32 v3, s62, v1
v_mfma_f32_16x16x32_bf16 v[84:87], a[184:187], a[20:23], v[84:87]
s_add_u32 s62, s39, 2
v_mfma_f32_16x16x32_bf16 v[100:103], a[184:187], a[24:27], v[100:103]
s_and_b32 s62, s62, 3
v_mfma_f32_16x16x32_bf16 v[116:119], a[184:187], a[28:31], v[116:119]
s_lshl_b32 s62, s62, 15
v_mfma_f32_16x16x32_bf16 v[72:75], a[188:191], a[16:19], v[72:75]
s_add_u32 s62, s62, s67
v_mfma_f32_16x16x32_bf16 v[88:91], a[188:191], a[20:23], v[88:91]
v_add_u32 v4, s62, v1
v_mfma_f32_16x16x32_bf16 v[104:107], a[188:191], a[24:27], v[104:107]
s_add_u32 s62, s39, 3
v_mfma_f32_16x16x32_bf16 v[120:123], a[188:191], a[28:31], v[120:123]
s_and_b32 s62, s62, 3
v_mfma_f32_16x16x32_bf16 v[76:79], a[192:195], a[16:19], v[76:79]
s_lshl_b32 s62, s62, 15
v_mfma_f32_16x16x32_bf16 v[92:95], a[192:195], a[20:23], v[92:95]
s_add_u32 s62, s62, s67
v_mfma_f32_16x16x32_bf16 v[108:111], a[192:195], a[24:27], v[108:111]
v_add_u32 v5, s62, v1
v_mfma_f32_16x16x32_bf16 v[124:127], a[192:195], a[28:31], v[124:127]
s_nop 7
ds_write_b128 v3, v[80:83] offset:0
ds_write_b128 v3, v[84:87] offset:1024
ds_write_b128 v3, v[88:91] offset:2048
ds_write_b128 v3, v[92:95] offset:3072
ds_write_b128 v4, v[96:99] offset:0
ds_write_b128 v4, v[100:103] offset:1024
ds_write_b128 v4, v[104:107] offset:2048
ds_write_b128 v4, v[108:111] offset:3072
ds_write_b128 v5, v[112:115] offset:0
ds_write_b128 v5, v[116:119] offset:1024
ds_write_b128 v5, v[120:123] offset:2048
ds_write_b128 v5, v[124:127] offset:3072
s_add_u32 s62, s39, 1
s_and_b32 s62, s62, 3
s_lshl_b32 s62, s62, 12
s_add_u32 s62, s62, s68
v_add_u32 v3, s62, v1
s_add_u32 s62, s39, 2
s_and_b32 s62, s62, 3
s_lshl_b32 s62, s62, 12
s_add_u32 s62, s62, s68
v_add_u32 v4, s62, v1
s_add_u32 s62, s39, 3
s_and_b32 s62, s62, 3
s_lshl_b32 s62, s62, 12
s_add_u32 s62, s62, s68
v_add_u32 v5, s62, v1
s_waitcnt vmcnt(0) lgkmcnt(0)
s_barrier
ds_read_b128 v[80:83], v3 offset:0
ds_read_b128 v[84:87], v3 offset:1024
ds_read_b128 v[88:91], v3 offset:2048
ds_read_b128 v[92:95], v3 offset:3072
ds_read_b128 v[96:99], v4 offset:0
ds_read_b128 v[100:103], v4 offset:1024
ds_read_b128 v[104:107], v4 offset:2048
ds_read_b128 v[108:111], v4 offset:3072
ds_read_b128 v[112:115], v5 offset:0
ds_read_b128 v[116:119], v5 offset:1024
ds_read_b128 v[120:123], v5 offset:2048
ds_read_b128 v[124:127], v5 offset:3072
s_waitcnt lgkmcnt(8)
v_add_f32 v64, v64, v80
v_add_f32 v65, v65, v81
v_add_f32 v66, v66, v82
v_add_f32 v67, v67, v83
v_add_f32 v68, v68, v84
v_add_f32 v69, v69, v85
v_add_f32 v70, v70, v86
v_add_f32 v71, v71, v87
v_add_f32 v72, v72, v88
v_add_f32 v73, v73, v89
v_add_f32 v74, v74, v90
v_add_f32 v75, v75, v91
v_add_f32 v76, v76, v92
v_add_f32 v77, v77, v93
v_add_f32 v78, v78, v94
v_add_f32 v79, v79, v95
s_waitcnt lgkmcnt(0)
v_add_f32 v96, v96, v112
v_add_f32 v97, v97, v113
v_add_f32 v98, v98, v114
v_add_f32 v99, v99, v115
v_add_f32 v100, v100, v116
v_add_f32 v101, v101, v117
v_add_f32 v102, v102, v118
v_add_f32 v103, v103, v119
v_add_f32 v104, v104, v120
v_add_f32 v105, v105, v121
v_add_f32 v106, v106, v122
)ASM"
R"ASM(v_add_f32 v107, v107, v123
v_add_f32 v108, v108, v124
v_add_f32 v109, v109, v125
v_add_f32 v110, v110, v126
v_add_f32 v111, v111, v127
v_add_f32 v64, v64, v96
v_add_f32 v65, v65, v97
v_add_f32 v66, v66, v98
v_add_f32 v67, v67, v99
v_add_f32 v68, v68, v100
v_add_f32 v69, v69, v101
v_add_f32 v70, v70, v102
v_add_f32 v71, v71, v103
v_add_f32 v72, v72, v104
v_add_f32 v73, v73, v105
v_add_f32 v74, v74, v106
v_add_f32 v75, v75, v107
v_add_f32 v76, v76, v108
v_add_f32 v77, v77, v109
v_add_f32 v78, v78, v110
v_add_f32 v79, v79, v111
v_lshlrev_b32 v240, 16, v192
v_and_b32 v241, 0xffff0000, v192
v_lshlrev_b32 v242, 16, v193
v_and_b32 v243, 0xffff0000, v193
v_lshlrev_b32 v244, 16, v194
v_and_b32 v245, 0xffff0000, v194
v_lshlrev_b32 v246, 16, v195
v_and_b32 v247, 0xffff0000, v195
v_lshlrev_b32 v248, 16, v196
v_and_b32 v249, 0xffff0000, v196
v_lshlrev_b32 v250, 16, v197
v_and_b32 v251, 0xffff0000, v197
v_lshlrev_b32 v252, 16, v198
v_and_b32 v253, 0xffff0000, v198
v_lshlrev_b32 v254, 16, v199
v_and_b32 v255, 0xffff0000, v199
v_fmac_f32 v240, v176, v64
v_fmac_f32 v241, v177, v65
v_fmac_f32 v242, v178, v66
v_fmac_f32 v243, v179, v67
v_fmac_f32 v244, v180, v68
v_fmac_f32 v245, v181, v69
v_fmac_f32 v246, v182, v70
v_fmac_f32 v247, v183, v71
v_fmac_f32 v248, v184, v72
v_fmac_f32 v249, v185, v73
v_fmac_f32 v250, v186, v74
v_fmac_f32 v251, v187, v75
v_fmac_f32 v252, v188, v76
v_fmac_f32 v253, v189, v77
v_fmac_f32 v254, v190, v78
v_fmac_f32 v255, v191, v79
v_mul_f32 v96, v240, v160
v_mul_f32 v97, v241, v161
v_mul_f32 v98, v242, v162
v_mul_f32 v99, v243, v163
v_mul_f32 v100, v244, v164
v_mul_f32 v101, v245, v165
v_mul_f32 v102, v246, v166
v_mul_f32 v103, v247, v167
v_mul_f32 v104, v248, v168
v_mul_f32 v105, v249, v169
v_mul_f32 v106, v250, v170
v_mul_f32 v107, v251, v171
v_mul_f32 v108, v252, v172
v_mul_f32 v109, v253, v173
v_mul_f32 v110, v254, v174
v_mul_f32 v111, v255, v175
v_cvt_pk_bf16_f32 v112, v96, v97
v_cvt_pk_bf16_f32 v113, v98, v99
v_cvt_pk_bf16_f32 v114, v100, v101
v_cvt_pk_bf16_f32 v115, v102, v103
v_cvt_pk_bf16_f32 v116, v104, v105
v_cvt_pk_bf16_f32 v117, v106, v107
v_cvt_pk_bf16_f32 v118, v108, v109
v_cvt_pk_bf16_f32 v119, v110, v111
s_add_u32 s44, s30, 57671680
s_addc_u32 s45, s31, 0
s_and_b32 s45, s45, 0xffff
s_mov_b32 s46, 2097152
s_mov_b32 s47, 0x20000
s_lshl_b32 s63, s71, 14
s_lshl_b32 s62, s65, 11
s_add_u32 s63, s63, s62
buffer_store_dwordx4 v[112:115], v1, s[44:47], s63 offen offset:0 sc1
buffer_store_dwordx4 v[116:119], v1, s[44:47], s63 offen offset:1024 sc1
v_add_f32 v12, v240, v241
v_add_f32 v12, v12, v242
v_add_f32 v12, v12, v243
v_add_f32 v12, v12, v244
v_add_f32 v12, v12, v245
v_add_f32 v12, v12, v246
v_add_f32 v12, v12, v247
v_add_f32 v12, v12, v248
v_add_f32 v12, v12, v249
v_add_f32 v12, v12, v250
v_add_f32 v12, v12, v251
v_add_f32 v12, v12, v252
v_add_f32 v12, v12, v253
v_add_f32 v12, v12, v254
v_add_f32 v12, v12, v255
v_mov_b32 v14, v12
s_nop 1
v_permlane32_swap_b32 v12, v14
s_nop 0
v_add_f32 v12, v12, v14
v_mov_b32 v14, v12
s_nop 1
v_permlane16_swap_b32 v12, v14
s_nop 0
v_add_f32 v12, v12, v14
v_mul_f32 v12, 0x3c800000, v12
v_sub_f32 v14, v240, v12
v_mul_f32 v15, v14, v14
v_sub_f32 v14, v241, v12
v_fmac_f32 v15, v14, v14
v_sub_f32 v14, v242, v12
v_fmac_f32 v15, v14, v14
v_sub_f32 v14, v243, v12
v_fmac_f32 v15, v14, v14
v_sub_f32 v14, v244, v12
v_fmac_f32 v15, v14, v14
v_sub_f32 v14, v245, v12
v_fmac_f32 v15, v14, v14
v_sub_f32 v14, v246, v12
v_fmac_f32 v15, v14, v14
v_sub_f32 v14, v247, v12
v_fmac_f32 v15, v14, v14
v_sub_f32 v14, v248, v12
v_fmac_f32 v15, v14, v14
v_sub_f32 v14, v249, v12
v_fmac_f32 v15, v14, v14
v_sub_f32 v14, v250, v12
v_fmac_f32 v15, v14, v14
v_sub_f32 v14, v251, v12
v_fmac_f32 v15, v14, v14
v_sub_f32 v14, v252, v12
v_fmac_f32 v15, v14, v14
v_sub_f32 v14, v253, v12
v_fmac_f32 v15, v14, v14
v_sub_f32 v14, v254, v12
v_fmac_f32 v15, v14, v14
v_sub_f32 v14, v255, v12
v_fmac_f32 v15, v14, v14
v_mov_b32 v14, v15
s_nop 1
v_permlane32_swap_b32 v15, v14
s_nop 0
v_add_f32 v15, v15, v14
v_mov_b32 v14, v15
s_nop 1
v_permlane16_swap_b32 v15, v14
s_nop 0
v_add_f32 v15, v15, v14
v_mov_b32 v13, v15
s_add_u32 s56, s30, 67108864
s_addc_u32 s57, s31, 0
s_and_b32 s57, s57, 0xffff
s_mov_b32 s58, 131072
s_mov_b32 s59, 0x20000
v_lshrrev_b32 v16, 4, v1
v_and_b32 v16, 15, v16
v_lshlrev_b32 v16, 6, v16
s_lshl_b32 s63, s71, 10
s_lshl_b32 s62, s65, 3
s_add_u32 s63, s63, s62
s_mov_b64 exec, 0xffff
buffer_store_dwordx2 v[12:13], v16, s[56:59], s63 offen sc1
s_mov_b64 exec, -1
s_lshr_b32 s63, s38, 3
s_cmp_ge_u32 s63, 28
s_cbranch_scc1 L_skip%=
s_lshr_b32 s63, s38, 3
s_and_b32 s62, s38, 7
s_mul_i32 s70, s63, 37
s_lshr_b32 s70, s70, 8
s_mul_i32 s76, s70, 7
s_sub_u32 s77, s63, s76
s_and_b32 s63, s62, 1
s_mul_i32 s63, s63, 7
s_add_u32 s77, s77, s63
s_lshr_b32 s62, s62, 1
s_lshl_b32 s62, s62, 2
s_add_u32 s76, s62, s70
v_lshrrev_b32 v9, 4, v1
v_lshrrev_b32 v6, 4, v9
v_mul_u32_u24 v7, 3584, v6
v_lshlrev_b32 v6, 1, v6
v_and_b32 v8, 15, v9
v_xor_b32 v8, v6, v8
v_lshl_add_u32 v2, v8, 4, v7
v_xor_b32 v8, 8, v8
v_lshl_add_u32 v3, v8, 4, v7
s_lshl_b32 s62, s77, 8
v_add_u32 v2, s62, v2
v_add_u32 v3, s62, v3
s_mov_b32 s48, s20
s_mov_b32 s49, s21
s_and_b32 s49, s49, 0xffff
s_mov_b32 s50, 1835008
s_mov_b32 s51, 0x20000
s_mul_i32 s70, s39, 28672
s_lshl_b32 s72, s39, 11
s_add_u32 s62, s70, 0
s_add_u32 m0, s72, 16384
s_nop 0
buffer_load_dwordx4 v2, s[48:51], s62 offen lds
s_add_u32 s62, s70, 14336
s_add_u32 m0, s72, 17408
s_nop 0
buffer_load_dwordx4 v3, s[48:51], s62 offen lds
s_add_u32 s62, s70, 114688
s_add_u32 m0, s72, 24576
s_nop 0
buffer_load_dwordx4 v2, s[48:51], s62 offen lds
s_add_u32 s62, s70, 129024
s_add_u32 m0, s72, 25600
s_nop 0
buffer_load_dwordx4 v3, s[48:51], s62 offen lds
s_add_u32 s62, s70, 229376
s_add_u32 m0, s72, 49152
s_nop 0
buffer_load_dwordx4 v2, s[48:51], s62 offen lds
s_add_u32 s62, s70, 243712
s_add_u32 m0, s72, 50176
s_nop 0
buffer_load_dwordx4 v3, s[48:51], s62 offen lds
s_add_u32 s62, s70, 344064
s_add_u32 m0, s72, 57344
s_nop 0
buffer_load_dwordx4 v2, s[48:51], s62 offen lds
s_add_u32 s62, s70, 358400
s_add_u32 m0, s72, 58368
s_nop 0
buffer_load_dwordx4 v3, s[48:51], s62 offen lds
s_add_u32 s62, s70, 458752
s_add_u32 m0, s72, 81920
s_nop 0
buffer_load_dwordx4 v2, s[48:51], s62 offen lds
s_add_u32 s62, s70, 473088
s_add_u32 m0, s72, 82944
s_nop 0
buffer_load_dwordx4 v3, s[48:51], s62 offen lds
s_add_u32 s62, s70, 573440
s_add_u32 m0, s72, 90112
s_nop 0
buffer_load_dwordx4 v2, s[48:51], s62 offen lds
s_add_u32 s62, s70, 587776
s_add_u32 m0, s72, 91136
s_nop 0
buffer_load_dwordx4 v3, s[48:51], s62 offen lds
s_add_u32 s62, s70, 688128
s_add_u32 m0, s72, 114688
s_nop 0
buffer_load_dwordx4 v2, s[48:51], s62 offen lds
s_add_u32 s62, s70, 702464
s_add_u32 m0, s72, 115712
s_nop 0
buffer_load_dwordx4 v3, s[48:51], s62 offen lds
s_add_u32 s62, s70, 802816
s_add_u32 m0, s72, 122880
s_nop 0
buffer_load_dwordx4 v2, s[48:51], s62 offen lds
s_add_u32 s62, s70, 817152
s_add_u32 m0, s72, 123904
s_nop 0
buffer_load_dwordx4 v3, s[48:51], s62 offen lds
s_waitcnt vmcnt(16)
s_barrier
v_mov_b32 v20, s33
v_mov_b32 v21, 0
s_lshl_b32 s79, s38, 7
s_add_u32 s79, s79, 131072
s_mov_b64 exec, s[34:35]
buffer_store_dword v20, v21, s[40:43], s79 offen sc1
s_mov_b64 exec, -1
s_add_u32 s52, s30, 67633152
s_addc_u32 s53, s31, 0
s_and_b32 s53, s53, 0xffff
s_mov_b32 s54, 16384
s_mov_b32 s55, 0x20000
s_mov_b32 exec_lo, 0x00030003
s_mov_b32 exec_hi, 0x00030003
s_mov_b32 s63, 0
buffer_load_dwordx4 a[128:131], v1, s[52:55], s63 offen offset:0 sc1
buffer_load_dwordx4 a[132:135], v1, s[52:55], s63 offen offset:1024 sc1
buffer_load_dwordx4 a[136:139], v1, s[52:55], s63 offen offset:2048 sc1
buffer_load_dwordx4 a[140:143], v1, s[52:55], s63 offen offset:3072 sc1
s_mov_b32 s63, 4096
buffer_load_dwordx4 a[144:147], v1, s[52:55], s63 offen offset:0 sc1
buffer_load_dwordx4 a[148:151], v1, s[52:55], s63 offen offset:1024 sc1
buffer_load_dwordx4 a[152:155], v1, s[52:55], s63 offen offset:2048 sc1
buffer_load_dwordx4 a[156:159], v1, s[52:55], s63 offen offset:3072 sc1
s_mov_b32 s63, 8192
buffer_load_dwordx4 a[160:163], v1, s[52:55], s63 offen offset:0 sc1
buffer_load_dwordx4 a[164:167], v1, s[52:55], s63 offen offset:1024 sc1
buffer_load_dwordx4 a[168:171], v1, s[52:55], s63 offen offset:2048 sc1
buffer_load_dwordx4 a[172:175], v1, s[52:55], s63 offen offset:3072 sc1
s_mov_b32 s63, 12288
)ASM"
R"ASM(buffer_load_dwordx4 a[176:179], v1, s[52:55], s63 offen offset:0 sc1
buffer_load_dwordx4 a[180:183], v1, s[52:55], s63 offen offset:1024 sc1
buffer_load_dwordx4 a[184:187], v1, s[52:55], s63 offen offset:2048 sc1
buffer_load_dwordx4 a[188:191], v1, s[52:55], s63 offen offset:3072 sc1
s_mov_b64 exec, -1
s_lshl_b32 s62, s76, 1
s_and_b32 s63, s62, 3
s_lshl_b32 s63, s63, 3
s_lshr_b32 s62, s62, 2
s_add_u32 s62, s62, s63
v_lshrrev_b32 v10, 4, v1
v_bfe_u32 v11, v10, 3, 1
v_and_b32 v10, 7, v10
v_lshlrev_b32 v10, 5, v10
v_lshl_add_u32 v10, v11, 3, v10
v_add_u32 v10, s62, v10
v_lshlrev_b32 v10, 7, v10
s_cmp_eq_u32 s39, 0
s_mov_b32 s74, 0xffff
s_cselect_b32 s74, s74, 0
s_mov_b32 s75, 0x0
s_cselect_b32 s75, s75, 0
s_mov_b64 exec, s[74:75]
s_mov_b32 s79, 131072
buffer_load_dword v11, v10, s[40:43], s79 offen sc1
s_mov_b64 exec, -1
s_add_u32 s62, s70, 917504
s_add_u32 m0, s72, 131072
s_nop 0
buffer_load_dwordx4 v2, s[48:51], s62 offen lds
s_add_u32 s62, s70, 931840
s_add_u32 m0, s72, 132096
s_nop 0
buffer_load_dwordx4 v3, s[48:51], s62 offen lds
s_add_u32 s62, s70, 1032192
s_add_u32 m0, s72, 139264
s_nop 0
buffer_load_dwordx4 v2, s[48:51], s62 offen lds
s_add_u32 s62, s70, 1046528
s_add_u32 m0, s72, 140288
s_nop 0
buffer_load_dwordx4 v3, s[48:51], s62 offen lds
s_add_u32 s62, s70, 1146880
s_add_u32 m0, s72, 147456
s_nop 0
buffer_load_dwordx4 v2, s[48:51], s62 offen lds
s_add_u32 s62, s70, 1161216
s_add_u32 m0, s72, 148480
s_nop 0
buffer_load_dwordx4 v3, s[48:51], s62 offen lds
s_add_u32 s62, s70, 1261568
s_add_u32 m0, s72, 155648
s_nop 0
buffer_load_dwordx4 v2, s[48:51], s62 offen lds
s_add_u32 s62, s70, 1275904
s_add_u32 m0, s72, 156672
s_nop 0
buffer_load_dwordx4 v3, s[48:51], s62 offen lds
s_add_u32 s62, s70, 1376256
s_add_u32 m0, s72, 0
s_nop 0
buffer_load_dwordx4 v2, s[48:51], s62 offen lds
s_add_u32 s62, s70, 1390592
s_add_u32 m0, s72, 1024
s_nop 0
buffer_load_dwordx4 v3, s[48:51], s62 offen lds
s_add_u32 s62, s70, 1490944
s_add_u32 m0, s72, 8192
s_nop 0
buffer_load_dwordx4 v2, s[48:51], s62 offen lds
s_add_u32 s62, s70, 1505280
s_add_u32 m0, s72, 9216
s_nop 0
buffer_load_dwordx4 v3, s[48:51], s62 offen lds
s_add_u32 s62, s70, 1605632
s_add_u32 m0, s72, 32768
s_nop 0
buffer_load_dwordx4 v2, s[48:51], s62 offen lds
s_add_u32 s62, s70, 1619968
s_add_u32 m0, s72, 33792
s_nop 0
buffer_load_dwordx4 v3, s[48:51], s62 offen lds
s_add_u32 s62, s70, 1720320
s_add_u32 m0, s72, 40960
s_nop 0
buffer_load_dwordx4 v2, s[48:51], s62 offen lds
s_add_u32 s62, s70, 1734656
s_add_u32 m0, s72, 41984
s_nop 0
buffer_load_dwordx4 v3, s[48:51], s62 offen lds
s_waitcnt vmcnt(16)
s_mov_b64 vcc, 0
s_mov_b64 exec, s[74:75]
v_cmp_ne_u32 vcc, s33, v11
s_mov_b64 exec, -1
s_nop 4
s_cbranch_vccnz L_slow4%=
L_back4%=:
s_barrier
v_lshrrev_b32 v26, 4, v1
s_lshl_b32 s71, s76, 3
s_lshl_b32 s62, s39, 1
s_add_u32 s71, s71, s62
s_lshl_b32 s62, s71, 14
v_add_u32 v18, s62, v1
v_add_u32 v19, 16384, v18
s_add_u32 s44, s30, 57671680
s_addc_u32 s45, s31, 0
s_and_b32 s45, s45, 0xffff
s_mov_b32 s46, 2097152
s_mov_b32 s47, 0x20000
s_add_u32 s56, s30, 67108864
s_addc_u32 s57, s31, 0
s_and_b32 s57, s57, 0xffff
s_mov_b32 s58, 131072
s_mov_b32 s59, 0x20000
v_and_b32 v27, 15, v26
v_lshlrev_b32 v27, 6, v27
v_lshrrev_b32 v28, 4, v26
v_lshl_add_u32 v27, v28, 4, v27
s_lshl_b32 s63, s71, 10
buffer_load_dwordx4 v[128:131], v27, s[56:59], s63 offen offset:0 sc1
buffer_load_dwordx4 v[132:135], v27, s[56:59], s63 offen offset:1024 sc1
s_mov_b32 s63, 0
buffer_load_dwordx4 a[0:3], v18, s[44:47], s63 offen offset:0 sc1
buffer_load_dwordx4 a[64:67], v19, s[44:47], s63 offen offset:0 sc1
buffer_load_dwordx4 a[4:7], v18, s[44:47], s63 offen offset:1024 sc1
buffer_load_dwordx4 a[68:71], v19, s[44:47], s63 offen offset:1024 sc1
buffer_load_dwordx4 a[8:11], v18, s[44:47], s63 offen offset:2048 sc1
buffer_load_dwordx4 a[72:75], v19, s[44:47], s63 offen offset:2048 sc1
buffer_load_dwordx4 a[12:15], v18, s[44:47], s63 offen offset:3072 sc1
buffer_load_dwordx4 a[76:79], v19, s[44:47], s63 offen offset:3072 sc1
v_lshrrev_b32 v27, 2, v26
v_and_b32 v28, 7, v27
v_lshlrev_b32 v28, 5, v28
v_lshl_add_u32 v27, v27, 8, v28
v_and_b32 v28, 3, v26
v_lshl_add_u32 v4, v28, 3, v27
s_lshl_b32 s62, s39, 6
v_xor_b32 v5, s62, v4
v_add_u32 v6, 0x10000, v5
v_add_u32 v7, 0x20000, v5
v_xor_b32 v8, 32, v5
v_add_u32 v9, 0x10000, v8
v_add_u32 v10, 0x20000, v8
s_waitcnt vmcnt(10)
s_barrier
ds_read_b64_tr_b16 a[192:193], v5 offset:16384
ds_read_b64_tr_b16 a[194:195], v5 offset:20480
s_mov_b32 s63, 4096
buffer_load_dwordx4 a[16:19], v18, s[44:47], s63 offen offset:0 sc1
ds_read_b64_tr_b16 a[196:197], v5 offset:24576
ds_read_b64_tr_b16 a[198:199], v5 offset:28672
buffer_load_dwordx4 a[80:83], v19, s[44:47], s63 offen offset:0 sc1
ds_read_b64_tr_b16 a[200:201], v5 offset:49152
ds_read_b64_tr_b16 a[202:203], v5 offset:53248
s_waitcnt lgkmcnt(4)
v_mfma_f32_16x16x32_bf16 v[32:35], a[192:195], a[128:131], 0
buffer_load_dwordx4 a[20:23], v18, s[44:47], s63 offen offset:1024 sc1
ds_read_b64_tr_b16 a[204:205], v5 offset:57344
ds_read_b64_tr_b16 a[206:207], v5 offset:61440
s_waitcnt lgkmcnt(4)
v_mfma_f32_16x16x32_bf16 v[32:35], a[196:199], a[132:135], v[32:35]
buffer_load_dwordx4 a[84:87], v19, s[44:47], s63 offen offset:1024 sc1
ds_read_b64_tr_b16 a[192:193], v6 offset:16384
ds_read_b64_tr_b16 a[194:195], v6 offset:20480
s_waitcnt lgkmcnt(4)
v_mfma_f32_16x16x32_bf16 v[32:35], a[200:203], a[136:139], v[32:35]
buffer_load_dwordx4 a[24:27], v18, s[44:47], s63 offen offset:2048 sc1
ds_read_b64_tr_b16 a[196:197], v6 offset:24576
ds_read_b64_tr_b16 a[198:199], v6 offset:28672
s_waitcnt lgkmcnt(4)
v_mfma_f32_16x16x32_bf16 v[32:35], a[204:207], a[140:143], v[32:35]
buffer_load_dwordx4 a[88:91], v19, s[44:47], s63 offen offset:2048 sc1
ds_read_b64_tr_b16 a[200:201], v6 offset:49152
ds_read_b64_tr_b16 a[202:203], v6 offset:53248
s_waitcnt lgkmcnt(4)
v_mfma_f32_16x16x32_bf16 v[32:35], a[192:195], a[144:147], v[32:35]
buffer_load_dwordx4 a[28:31], v18, s[44:47], s63 offen offset:3072 sc1
ds_read_b64_tr_b16 a[204:205], v6 offset:57344
ds_read_b64_tr_b16 a[206:207], v6 offset:61440
s_waitcnt lgkmcnt(4)
v_mfma_f32_16x16x32_bf16 v[32:35], a[196:199], a[148:151], v[32:35]
buffer_load_dwordx4 a[92:95], v19, s[44:47], s63 offen offset:3072 sc1
ds_read_b64_tr_b16 a[192:193], v7 offset:0
ds_read_b64_tr_b16 a[194:195], v7 offset:4096
s_waitcnt lgkmcnt(4)
v_mfma_f32_16x16x32_bf16 v[32:35], a[200:203], a[152:155], v[32:35]
s_mov_b32 s63, 8192
buffer_load_dwordx4 a[32:35], v18, s[44:47], s63 offen offset:0 sc1
ds_read_b64_tr_b16 a[196:197], v7 offset:8192
ds_read_b64_tr_b16 a[198:199], v7 offset:12288
s_waitcnt lgkmcnt(4)
v_mfma_f32_16x16x32_bf16 v[32:35], a[204:207], a[156:159], v[32:35]
buffer_load_dwordx4 a[96:99], v19, s[44:47], s63 offen offset:0 sc1
ds_read_b64_tr_b16 a[200:201], v7 offset:16384
ds_read_b64_tr_b16 a[202:203], v7 offset:20480
s_waitcnt lgkmcnt(4)
v_mfma_f32_16x16x32_bf16 v[32:35], a[192:195], a[160:163], v[32:35]
buffer_load_dwordx4 a[36:39], v18, s[44:47], s63 offen offset:1024 sc1
ds_read_b64_tr_b16 a[204:205], v7 offset:24576
ds_read_b64_tr_b16 a[206:207], v7 offset:28672
s_waitcnt lgkmcnt(4)
v_mfma_f32_16x16x32_bf16 v[32:35], a[196:199], a[164:167], v[32:35]
buffer_load_dwordx4 a[100:103], v19, s[44:47], s63 offen offset:1024 sc1
ds_read_b64_tr_b16 a[192:193], v5 offset:0
ds_read_b64_tr_b16 a[194:195], v5 offset:4096
s_waitcnt lgkmcnt(4)
v_mfma_f32_16x16x32_bf16 v[32:35], a[200:203], a[168:171], v[32:35]
buffer_load_dwordx4 a[40:43], v18, s[44:47], s63 offen offset:2048 sc1
ds_read_b64_tr_b16 a[196:197], v5 offset:8192
ds_read_b64_tr_b16 a[198:199], v5 offset:12288
s_waitcnt lgkmcnt(4)
v_mfma_f32_16x16x32_bf16 v[32:35], a[204:207], a[172:175], v[32:35]
buffer_load_dwordx4 a[104:107], v19, s[44:47], s63 offen offset:2048 sc1
ds_read_b64_tr_b16 a[200:201], v5 offset:32768
ds_read_b64_tr_b16 a[202:203], v5 offset:36864
s_waitcnt lgkmcnt(4)
v_mfma_f32_16x16x32_bf16 v[32:35], a[192:195], a[176:179], v[32:35]
buffer_load_dwordx4 a[44:47], v18, s[44:47], s63 offen offset:3072 sc1
ds_read_b64_tr_b16 a[204:205], v5 offset:40960
ds_read_b64_tr_b16 a[206:207], v5 offset:45056
s_waitcnt lgkmcnt(4)
v_mfma_f32_16x16x32_bf16 v[32:35], a[196:199], a[180:183], v[32:35]
buffer_load_dwordx4 a[108:111], v19, s[44:47], s63 offen offset:3072 sc1
ds_read_b64_tr_b16 a[192:193], v8 offset:16384
ds_read_b64_tr_b16 a[194:195], v8 offset:20480
s_waitcnt lgkmcnt(4)
v_mfma_f32_16x16x32_bf16 v[32:35], a[200:203], a[184:187], v[32:35]
s_mov_b32 s63, 12288
buffer_load_dwordx4 a[48:51], v18, s[44:47], s63 offen offset:0 sc1
ds_read_b64_tr_b16 a[196:197], v8 offset:24576
ds_read_b64_tr_b16 a[198:199], v8 offset:28672
s_waitcnt lgkmcnt(4)
v_mfma_f32_16x16x32_bf16 v[32:35], a[204:207], a[188:191], v[32:35]
buffer_load_dwordx4 a[112:115], v19, s[44:47], s63 offen offset:0 sc1
ds_read_b64_tr_b16 a[200:201], v8 offset:49152
ds_read_b64_tr_b16 a[202:203], v8 offset:53248
s_waitcnt lgkmcnt(4)
v_mfma_f32_16x16x32_bf16 v[36:39], a[192:195], a[128:131], 0
buffer_load_dwordx4 a[52:55], v18, s[44:47], s63 offen offset:1024 sc1
ds_read_b64_tr_b16 a[204:205], v8 offset:57344
ds_read_b64_tr_b16 a[206:207], v8 offset:61440
s_waitcnt lgkmcnt(4)
v_mfma_f32_16x16x32_bf16 v[36:39], a[196:199], a[132:135], v[36:39]
buffer_load_dwordx4 a[116:119], v19, s[44:47], s63 offen offset:1024 sc1
ds_read_b64_tr_b16 a[192:193], v9 offset:16384
ds_read_b64_tr_b16 a[194:195], v9 offset:20480
s_waitcnt lgkmcnt(4)
v_mfma_f32_16x16x32_bf16 v[36:39], a[200:203], a[136:139], v[36:39]
buffer_load_dwordx4 a[56:59], v18, s[44:47], s63 offen offset:2048 sc1
ds_read_b64_tr_b16 a[196:197], v9 offset:24576
ds_read_b64_tr_b16 a[198:199], v9 offset:28672
s_waitcnt lgkmcnt(4)
v_mfma_f32_16x16x32_bf16 v[36:39], a[204:207], a[140:143], v[36:39]
buffer_load_dwordx4 a[120:123], v19, s[44:47], s63 offen offset:2048 sc1
ds_read_b64_tr_b16 a[200:201], v9 offset:49152
ds_read_b64_tr_b16 a[202:203], v9 offset:53248
s_waitcnt lgkmcnt(4)
v_mfma_f32_16x16x32_bf16 v[36:39], a[192:195], a[144:147], v[36:39]
buffer_load_dwordx4 a[60:63], v18, s[44:47], s63 offen offset:3072 sc1
ds_read_b64_tr_b16 a[204:205], v9 offset:57344
ds_read_b64_tr_b16 a[206:207], v9 offset:61440
s_waitcnt lgkmcnt(4)
v_mfma_f32_16x16x32_bf16 v[36:39], a[196:199], a[148:151], v[36:39]
buffer_load_dwordx4 a[124:127], v19, s[44:47], s63 offen offset:3072 sc1
ds_read_b64_tr_b16 a[192:193], v10 offset:0
ds_read_b64_tr_b16 a[194:195], v10 offset:4096
s_waitcnt lgkmcnt(4)
v_mfma_f32_16x16x32_bf16 v[36:39], a[200:203], a[152:155], v[36:39]
ds_read_b64_tr_b16 a[196:197], v10 offset:8192
ds_read_b64_tr_b16 a[198:199], v10 offset:12288
s_waitcnt lgkmcnt(4)
v_mfma_f32_16x16x32_bf16 v[36:39], a[204:207], a[156:159], v[36:39]
ds_read_b64_tr_b16 a[200:201], v10 offset:16384
ds_read_b64_tr_b16 a[202:203], v10 offset:20480
s_waitcnt lgkmcnt(4)
v_mfma_f32_16x16x32_bf16 v[36:39], a[192:195], a[160:163], v[36:39]
ds_read_b64_tr_b16 a[204:205], v10 offset:24576
ds_read_b64_tr_b16 a[206:207], v10 offset:28672
s_waitcnt lgkmcnt(4)
v_mfma_f32_16x16x32_bf16 v[36:39], a[196:199], a[164:167], v[36:39]
ds_read_b64_tr_b16 a[192:193], v8 offset:0
ds_read_b64_tr_b16 a[194:195], v8 offset:4096
s_waitcnt lgkmcnt(4)
v_mfma_f32_16x16x32_bf16 v[36:39], a[200:203], a[168:171], v[36:39]
ds_read_b64_tr_b16 a[196:197], v8 offset:8192
ds_read_b64_tr_b16 a[198:199], v8 offset:12288
s_waitcnt lgkmcnt(4)
v_mfma_f32_16x16x32_bf16 v[36:39], a[204:207], a[172:175], v[36:39]
ds_read_b64_tr_b16 a[200:201], v8 offset:32768
ds_read_b64_tr_b16 a[202:203], v8 offset:36864
s_waitcnt lgkmcnt(4)
v_mfma_f32_16x16x32_bf16 v[36:39], a[192:195], a[176:179], v[36:39]
ds_read_b64_tr_b16 a[204:205], v8 offset:40960
ds_read_b64_tr_b16 a[206:207], v8 offset:45056
s_waitcnt lgkmcnt(4)
v_mfma_f32_16x16x32_bf16 v[36:39], a[196:199], a[180:183], v[36:39]
s_waitcnt lgkmcnt(2)
v_mfma_f32_16x16x32_bf16 v[36:39], a[200:203], a[184:187], v[36:39]
)ASM"
R"ASM(s_waitcnt lgkmcnt(0)
v_mfma_f32_16x16x32_bf16 v[36:39], a[204:207], a[188:191], v[36:39]
s_nop 7
v_and_b32 v27, 15, v26
v_cmp_eq_u32 vcc, 1, v27
v_mov_b32 v28, 1.0
v_mov_b32 v29, 0xbfb8aa3b
v_cndmask_b32 v28, v28, v29, vcc
v_mul_f32 v32, v28, v32
v_mul_f32 v33, v28, v33
v_mul_f32 v34, v28, v34
v_mul_f32 v35, v28, v35
v_mul_f32 v36, v28, v36
v_mul_f32 v37, v28, v37
v_mul_f32 v38, v28, v38
v_mul_f32 v39, v28, v39
v_lshlrev_b32 v27, 9, v27
v_lshrrev_b32 v28, 4, v26
v_lshl_add_u32 v27, v28, 4, v27
s_lshl_b32 s62, s39, 7
s_add_u32 s62, s62, 65536
v_add_u32 v27, s62, v27
s_mov_b32 exec_lo, 0x00030003
s_mov_b32 exec_hi, 0x00030003
ds_write_b128 v27, v[32:35] offset:0
ds_write_b128 v27, v[36:39] offset:64
s_mov_b64 exec, -1
s_waitcnt lgkmcnt(0)
s_barrier
v_lshrrev_b32 v28, 4, v26
v_lshlrev_b32 v27, 4, v28
v_add_u32 v27, 65536, v27
ds_read_b128 v[64:67], v27 offset:0
ds_read_b128 v[96:99], v27 offset:512
ds_read_b128 v[68:71], v27 offset:64
ds_read_b128 v[100:103], v27 offset:576
ds_read_b128 v[72:75], v27 offset:128
ds_read_b128 v[104:107], v27 offset:640
ds_read_b128 v[76:79], v27 offset:192
ds_read_b128 v[108:111], v27 offset:704
ds_read_b128 v[80:83], v27 offset:256
ds_read_b128 v[112:115], v27 offset:768
ds_read_b128 v[84:87], v27 offset:320
ds_read_b128 v[116:119], v27 offset:832
ds_read_b128 v[88:91], v27 offset:384
ds_read_b128 v[120:123], v27 offset:896
ds_read_b128 v[92:95], v27 offset:448
ds_read_b128 v[124:127], v27 offset:960
s_waitcnt vmcnt(33)
v_add_f32 v24, v128, v130
v_mov_b32 v25, v24
s_nop 1
v_permlane32_swap_b32 v24, v25
s_nop 0
v_add_f32 v24, v24, v25
v_mov_b32 v25, v24
s_nop 1
v_permlane16_swap_b32 v24, v25
s_nop 0
v_add_f32 v24, v24, v25
v_mul_f32 v30, 0x3e000000, v24
v_add_f32 v24, v129, v131
v_sub_f32 v31, v128, v30
v_mul_f32 v31, v31, v31
v_sub_f32 v25, v130, v30
v_fmac_f32 v31, v25, v25
v_fmac_f32 v24, 0x42800000, v31
v_mov_b32 v25, v24
s_nop 1
v_permlane32_swap_b32 v24, v25
s_nop 0
v_add_f32 v24, v24, v25
v_mov_b32 v25, v24
s_nop 1
v_permlane16_swap_b32 v24, v25
s_nop 0
v_add_f32 v24, v24, v25
v_mov_b32 v25, 0x3727c5ac
v_fmac_f32 v25, 0x3b000000, v24
v_rsq_f32 v25, v25
s_nop 0
v_mul_f32 v20, 0xbfb8aa3b, v25
v_mul_f32 v22, v30, v20
v_sub_f32 v22, 0, v22
s_waitcnt vmcnt(32)
v_add_f32 v24, v132, v134
v_mov_b32 v25, v24
s_nop 1
v_permlane32_swap_b32 v24, v25
s_nop 0
v_add_f32 v24, v24, v25
v_mov_b32 v25, v24
s_nop 1
v_permlane16_swap_b32 v24, v25
s_nop 0
v_add_f32 v24, v24, v25
v_mul_f32 v30, 0x3e000000, v24
v_add_f32 v24, v133, v135
v_sub_f32 v31, v132, v30
v_mul_f32 v31, v31, v31
v_sub_f32 v25, v134, v30
v_fmac_f32 v31, v25, v25
v_fmac_f32 v24, 0x42800000, v31
v_mov_b32 v25, v24
s_nop 1
v_permlane32_swap_b32 v24, v25
s_nop 0
v_add_f32 v24, v24, v25
v_mov_b32 v25, v24
s_nop 1
v_permlane16_swap_b32 v24, v25
s_nop 0
v_add_f32 v24, v24, v25
v_mov_b32 v25, 0x3727c5ac
v_fmac_f32 v25, 0x3b000000, v24
v_rsq_f32 v25, v25
s_nop 0
v_mul_f32 v21, 0xbfb8aa3b, v25
v_mul_f32 v23, v30, v21
v_sub_f32 v23, 0, v23
s_add_u32 s52, s30, 59768832
s_addc_u32 s53, s31, 0
s_and_b32 s53, s53, 0xffff
s_mov_b32 s54, 7340032
s_mov_b32 s55, 0x20000
s_mul_i32 s73, s71, 57344
s_lshl_b32 s62, s77, 12
s_add_u32 s73, s73, s62
s_add_u32 s66, s73, 57344
s_lshr_b32 s62, s38, 5
s_mul_i32 s62, s62, 56
s_mul_i32 s63, s39, 14
s_add_u32 s62, s62, s63
s_lshl_b32 s62, s62, 12
s_add_u32 s62, s62, 79691776
s_add_u32 s48, s30, s62
s_addc_u32 s49, s31, 0
s_and_b32 s49, s49, 0xffff
s_mov_b32 s50, 57344
s_mov_b32 s51, 0x20000
v_xor_b32 v5, 0, v4
v_add_u32 v6, 0x10000, v5
v_add_u32 v7, 0x20000, v5
v_xor_b32 v8, 32, v4
v_add_u32 v9, 0x10000, v8
v_add_u32 v10, 0x20000, v8
ds_read_b64_tr_b16 a[192:193], v5 offset:16384
ds_read_b64_tr_b16 a[194:195], v5 offset:20480
ds_read_b64_tr_b16 a[196:197], v5 offset:24576
ds_read_b64_tr_b16 a[198:199], v5 offset:28672
ds_read_b64_tr_b16 a[200:201], v5 offset:49152
ds_read_b64_tr_b16 a[202:203], v5 offset:53248
s_waitcnt vmcnt(31) lgkmcnt(4)
v_mfma_f32_16x16x32_bf16 v[32:35], a[192:195], a[0:3], 0
ds_read_b64_tr_b16 a[204:205], v5 offset:57344
ds_read_b64_tr_b16 a[206:207], v5 offset:61440
s_waitcnt vmcnt(30)
v_mfma_f32_16x16x32_bf16 v[36:39], a[192:195], a[64:67], 0
s_waitcnt vmcnt(29) lgkmcnt(4)
v_mfma_f32_16x16x32_bf16 v[32:35], a[196:199], a[4:7], v[32:35]
ds_read_b64_tr_b16 a[192:193], v6 offset:16384
ds_read_b64_tr_b16 a[194:195], v6 offset:20480
s_waitcnt vmcnt(28)
v_mfma_f32_16x16x32_bf16 v[36:39], a[196:199], a[68:71], v[36:39]
s_waitcnt vmcnt(27) lgkmcnt(4)
v_mfma_f32_16x16x32_bf16 v[32:35], a[200:203], a[8:11], v[32:35]
ds_read_b64_tr_b16 a[196:197], v6 offset:24576
ds_read_b64_tr_b16 a[198:199], v6 offset:28672
s_waitcnt vmcnt(26)
v_mfma_f32_16x16x32_bf16 v[36:39], a[200:203], a[72:75], v[36:39]
s_waitcnt vmcnt(25) lgkmcnt(4)
v_mfma_f32_16x16x32_bf16 v[32:35], a[204:207], a[12:15], v[32:35]
ds_read_b64_tr_b16 a[200:201], v6 offset:49152
ds_read_b64_tr_b16 a[202:203], v6 offset:53248
s_waitcnt vmcnt(24)
v_mfma_f32_16x16x32_bf16 v[36:39], a[204:207], a[76:79], v[36:39]
s_waitcnt vmcnt(23) lgkmcnt(4)
v_mfma_f32_16x16x32_bf16 v[32:35], a[192:195], a[16:19], v[32:35]
ds_read_b64_tr_b16 a[204:205], v6 offset:57344
ds_read_b64_tr_b16 a[206:207], v6 offset:61440
s_waitcnt vmcnt(22)
v_mfma_f32_16x16x32_bf16 v[36:39], a[192:195], a[80:83], v[36:39]
s_waitcnt vmcnt(21) lgkmcnt(4)
v_mfma_f32_16x16x32_bf16 v[32:35], a[196:199], a[20:23], v[32:35]
ds_read_b64_tr_b16 a[192:193], v7 offset:0
ds_read_b64_tr_b16 a[194:195], v7 offset:4096
s_waitcnt vmcnt(20)
v_mfma_f32_16x16x32_bf16 v[36:39], a[196:199], a[84:87], v[36:39]
s_waitcnt vmcnt(19) lgkmcnt(4)
v_mfma_f32_16x16x32_bf16 v[32:35], a[200:203], a[24:27], v[32:35]
ds_read_b64_tr_b16 a[196:197], v7 offset:8192
ds_read_b64_tr_b16 a[198:199], v7 offset:12288
s_waitcnt vmcnt(18)
v_mfma_f32_16x16x32_bf16 v[36:39], a[200:203], a[88:91], v[36:39]
s_waitcnt vmcnt(17) lgkmcnt(4)
v_mfma_f32_16x16x32_bf16 v[32:35], a[204:207], a[28:31], v[32:35]
ds_read_b64_tr_b16 a[200:201], v7 offset:16384
ds_read_b64_tr_b16 a[202:203], v7 offset:20480
s_waitcnt vmcnt(16)
v_mfma_f32_16x16x32_bf16 v[36:39], a[204:207], a[92:95], v[36:39]
s_waitcnt vmcnt(15) lgkmcnt(4)
v_mfma_f32_16x16x32_bf16 v[32:35], a[192:195], a[32:35], v[32:35]
ds_read_b64_tr_b16 a[204:205], v7 offset:24576
ds_read_b64_tr_b16 a[206:207], v7 offset:28672
s_waitcnt vmcnt(14)
v_mfma_f32_16x16x32_bf16 v[36:39], a[192:195], a[96:99], v[36:39]
s_waitcnt vmcnt(13) lgkmcnt(4)
v_mfma_f32_16x16x32_bf16 v[32:35], a[196:199], a[36:39], v[32:35]
ds_read_b64_tr_b16 a[192:193], v5 offset:0
ds_read_b64_tr_b16 a[194:195], v5 offset:4096
s_waitcnt vmcnt(12)
v_mfma_f32_16x16x32_bf16 v[36:39], a[196:199], a[100:103], v[36:39]
s_waitcnt vmcnt(11) lgkmcnt(4)
v_mfma_f32_16x16x32_bf16 v[32:35], a[200:203], a[40:43], v[32:35]
ds_read_b64_tr_b16 a[196:197], v5 offset:8192
ds_read_b64_tr_b16 a[198:199], v5 offset:12288
s_waitcnt vmcnt(10)
v_mfma_f32_16x16x32_bf16 v[36:39], a[200:203], a[104:107], v[36:39]
s_waitcnt vmcnt(9) lgkmcnt(4)
v_mfma_f32_16x16x32_bf16 v[32:35], a[204:207], a[44:47], v[32:35]
ds_read_b64_tr_b16 a[200:201], v5 offset:32768
ds_read_b64_tr_b16 a[202:203], v5 offset:36864
s_waitcnt vmcnt(8)
v_mfma_f32_16x16x32_bf16 v[36:39], a[204:207], a[108:111], v[36:39]
s_waitcnt vmcnt(7) lgkmcnt(4)
v_mfma_f32_16x16x32_bf16 v[32:35], a[192:195], a[48:51], v[32:35]
ds_read_b64_tr_b16 a[204:205], v5 offset:40960
ds_read_b64_tr_b16 a[206:207], v5 offset:45056
s_waitcnt vmcnt(6)
v_mfma_f32_16x16x32_bf16 v[36:39], a[192:195], a[112:115], v[36:39]
s_waitcnt vmcnt(5) lgkmcnt(4)
v_mfma_f32_16x16x32_bf16 v[32:35], a[196:199], a[52:55], v[32:35]
ds_read_b64_tr_b16 a[192:193], v8 offset:16384
ds_read_b64_tr_b16 a[194:195], v8 offset:20480
s_waitcnt vmcnt(4)
v_mfma_f32_16x16x32_bf16 v[36:39], a[196:199], a[116:119], v[36:39]
v_xor_b32 v5, 64, v4
v_add_u32 v6, 0x10000, v5
v_add_u32 v7, 0x20000, v5
s_waitcnt vmcnt(3) lgkmcnt(4)
v_mfma_f32_16x16x32_bf16 v[32:35], a[200:203], a[56:59], v[32:35]
ds_read_b64_tr_b16 a[196:197], v8 offset:24576
ds_read_b64_tr_b16 a[198:199], v8 offset:28672
s_waitcnt vmcnt(2)
v_mfma_f32_16x16x32_bf16 v[36:39], a[200:203], a[120:123], v[36:39]
s_waitcnt vmcnt(1) lgkmcnt(4)
v_mfma_f32_16x16x32_bf16 v[32:35], a[204:207], a[60:63], v[32:35]
ds_read_b64_tr_b16 a[200:201], v8 offset:49152
ds_read_b64_tr_b16 a[202:203], v8 offset:53248
s_waitcnt vmcnt(0)
v_mfma_f32_16x16x32_bf16 v[36:39], a[204:207], a[124:127], v[36:39]
s_waitcnt lgkmcnt(4)
v_mfma_f32_16x16x32_bf16 v[40:43], a[192:195], a[0:3], 0
ds_read_b64_tr_b16 a[204:205], v8 offset:57344
ds_read_b64_tr_b16 a[206:207], v8 offset:61440
v_mfma_f32_16x16x32_bf16 v[44:47], a[192:195], a[64:67], 0
s_waitcnt lgkmcnt(4)
v_mfma_f32_16x16x32_bf16 v[40:43], a[196:199], a[4:7], v[40:43]
ds_read_b64_tr_b16 a[192:193], v9 offset:16384
ds_read_b64_tr_b16 a[194:195], v9 offset:20480
v_mfma_f32_16x16x32_bf16 v[44:47], a[196:199], a[68:71], v[44:47]
v_fma_f32 v128, v22, v64, v96
v_fma_f32 v129, v22, v65, v97
v_fma_f32 v130, v22, v66, v98
v_fma_f32 v131, v22, v67, v99
s_mov_b32 s64, 0
buffer_load_dwordx4 a[128:131], v1, s[48:51], s64 offen offset:0 sc1
s_waitcnt lgkmcnt(4)
v_mfma_f32_16x16x32_bf16 v[40:43], a[200:203], a[8:11], v[40:43]
ds_read_b64_tr_b16 a[196:197], v9 offset:24576
ds_read_b64_tr_b16 a[198:199], v9 offset:28672
v_mfma_f32_16x16x32_bf16 v[44:47], a[200:203], a[72:75], v[44:47]
v_fma_f32 v132, v23, v64, v96
v_fma_f32 v133, v23, v65, v97
v_fma_f32 v134, v23, v66, v98
v_fma_f32 v135, v23, v67, v99
s_waitcnt lgkmcnt(4)
v_mfma_f32_16x16x32_bf16 v[40:43], a[204:207], a[12:15], v[40:43]
ds_read_b64_tr_b16 a[200:201], v9 offset:49152
ds_read_b64_tr_b16 a[202:203], v9 offset:53248
v_mfma_f32_16x16x32_bf16 v[44:47], a[204:207], a[76:79], v[44:47]
v_fma_f32 v56, v20, v32, v128
v_fma_f32 v57, v20, v33, v129
v_fma_f32 v58, v20, v34, v130
v_fma_f32 v59, v20, v35, v131
buffer_load_dwordx4 a[132:135], v1, s[48:51], s64 offen offset:1024 sc1
s_waitcnt lgkmcnt(4)
v_mfma_f32_16x16x32_bf16 v[40:43], a[192:195], a[16:19], v[40:43]
ds_read_b64_tr_b16 a[204:205], v9 offset:57344
ds_read_b64_tr_b16 a[206:207], v9 offset:61440
v_mfma_f32_16x16x32_bf16 v[44:47], a[192:195], a[80:83], v[44:47]
v_fma_f32 v60, v21, v36, v132
v_fma_f32 v61, v21, v37, v133
v_fma_f32 v62, v21, v38, v134
v_fma_f32 v63, v21, v39, v135
s_waitcnt lgkmcnt(4)
v_mfma_f32_16x16x32_bf16 v[40:43], a[196:199], a[20:23], v[40:43]
ds_read_b64_tr_b16 a[192:193], v10 offset:0
ds_read_b64_tr_b16 a[194:195], v10 offset:4096
v_mfma_f32_16x16x32_bf16 v[44:47], a[196:199], a[84:87], v[44:47]
)ASM"
R"ASM(v_exp_f32 v128, v56
v_exp_f32 v129, v57
v_exp_f32 v130, v58
v_exp_f32 v131, v59
buffer_load_dwordx4 a[136:139], v1, s[48:51], s64 offen offset:2048 sc1
s_waitcnt lgkmcnt(4)
v_mfma_f32_16x16x32_bf16 v[40:43], a[200:203], a[24:27], v[40:43]
ds_read_b64_tr_b16 a[196:197], v10 offset:8192
ds_read_b64_tr_b16 a[198:199], v10 offset:12288
v_mfma_f32_16x16x32_bf16 v[44:47], a[200:203], a[88:91], v[44:47]
v_exp_f32 v132, v60
v_exp_f32 v133, v61
v_exp_f32 v134, v62
v_exp_f32 v135, v63
s_waitcnt lgkmcnt(4)
v_mfma_f32_16x16x32_bf16 v[40:43], a[204:207], a[28:31], v[40:43]
ds_read_b64_tr_b16 a[200:201], v10 offset:16384
ds_read_b64_tr_b16 a[202:203], v10 offset:20480
v_mfma_f32_16x16x32_bf16 v[44:47], a[204:207], a[92:95], v[44:47]
v_add_f32 v128, 1.0, v128
v_add_f32 v129, 1.0, v129
v_add_f32 v130, 1.0, v130
v_add_f32 v131, 1.0, v131
buffer_load_dwordx4 a[140:143], v1, s[48:51], s64 offen offset:3072 sc1
s_waitcnt lgkmcnt(4)
v_mfma_f32_16x16x32_bf16 v[40:43], a[192:195], a[32:35], v[40:43]
ds_read_b64_tr_b16 a[204:205], v10 offset:24576
ds_read_b64_tr_b16 a[206:207], v10 offset:28672
v_mfma_f32_16x16x32_bf16 v[44:47], a[192:195], a[96:99], v[44:47]
v_add_f32 v132, 1.0, v132
v_add_f32 v133, 1.0, v133
v_add_f32 v134, 1.0, v134
s_waitcnt lgkmcnt(4)
v_mfma_f32_16x16x32_bf16 v[40:43], a[196:199], a[36:39], v[40:43]
ds_read_b64_tr_b16 a[192:193], v8 offset:0
ds_read_b64_tr_b16 a[194:195], v8 offset:4096
v_mfma_f32_16x16x32_bf16 v[44:47], a[196:199], a[100:103], v[44:47]
v_add_f32 v135, 1.0, v135
v_rcp_f32 v128, v128
v_rcp_f32 v129, v129
s_mov_b32 s64, 4096
buffer_load_dwordx4 a[144:147], v1, s[48:51], s64 offen offset:0 sc1
s_waitcnt lgkmcnt(4)
v_mfma_f32_16x16x32_bf16 v[40:43], a[200:203], a[40:43], v[40:43]
ds_read_b64_tr_b16 a[196:197], v8 offset:8192
ds_read_b64_tr_b16 a[198:199], v8 offset:12288
v_mfma_f32_16x16x32_bf16 v[44:47], a[200:203], a[104:107], v[44:47]
v_rcp_f32 v130, v130
v_rcp_f32 v131, v131
v_rcp_f32 v132, v132
s_waitcnt lgkmcnt(4)
v_mfma_f32_16x16x32_bf16 v[40:43], a[204:207], a[44:47], v[40:43]
ds_read_b64_tr_b16 a[200:201], v8 offset:32768
ds_read_b64_tr_b16 a[202:203], v8 offset:36864
v_mfma_f32_16x16x32_bf16 v[44:47], a[204:207], a[108:111], v[44:47]
v_rcp_f32 v133, v133
v_rcp_f32 v134, v134
v_rcp_f32 v135, v135
buffer_load_dwordx4 a[148:151], v1, s[48:51], s64 offen offset:1024 sc1
s_waitcnt lgkmcnt(4)
v_mfma_f32_16x16x32_bf16 v[40:43], a[192:195], a[48:51], v[40:43]
ds_read_b64_tr_b16 a[204:205], v8 offset:40960
ds_read_b64_tr_b16 a[206:207], v8 offset:45056
v_mfma_f32_16x16x32_bf16 v[44:47], a[192:195], a[112:115], v[44:47]
v_mul_f32 v56, v56, v128
v_mul_f32 v57, v57, v129
v_mul_f32 v58, v58, v130
s_waitcnt lgkmcnt(4)
v_mfma_f32_16x16x32_bf16 v[40:43], a[196:199], a[52:55], v[40:43]
ds_read_b64_tr_b16 a[192:193], v5 offset:16384
ds_read_b64_tr_b16 a[194:195], v5 offset:20480
v_mfma_f32_16x16x32_bf16 v[44:47], a[196:199], a[116:119], v[44:47]
v_mul_f32 v59, v59, v131
v_mul_f32 v60, v60, v132
v_mul_f32 v61, v61, v133
v_xor_b32 v8, 96, v4
v_add_u32 v9, 0x10000, v8
v_add_u32 v10, 0x20000, v8
buffer_load_dwordx4 a[152:155], v1, s[48:51], s64 offen offset:2048 sc1
s_waitcnt lgkmcnt(4)
v_mfma_f32_16x16x32_bf16 v[40:43], a[200:203], a[56:59], v[40:43]
ds_read_b64_tr_b16 a[196:197], v5 offset:24576
ds_read_b64_tr_b16 a[198:199], v5 offset:28672
v_mfma_f32_16x16x32_bf16 v[44:47], a[200:203], a[120:123], v[44:47]
v_mul_f32 v62, v62, v134
v_mul_f32 v63, v63, v135
v_cvt_pk_bf16_f32 v48, v56, v57
s_waitcnt lgkmcnt(4)
v_mfma_f32_16x16x32_bf16 v[40:43], a[204:207], a[60:63], v[40:43]
ds_read_b64_tr_b16 a[200:201], v5 offset:49152
ds_read_b64_tr_b16 a[202:203], v5 offset:53248
v_mfma_f32_16x16x32_bf16 v[44:47], a[204:207], a[124:127], v[44:47]
v_cvt_pk_bf16_f32 v49, v58, v59
v_cvt_pk_bf16_f32 v52, v60, v61
v_cvt_pk_bf16_f32 v53, v62, v63
buffer_load_dwordx4 a[156:159], v1, s[48:51], s64 offen offset:3072 sc1
s_waitcnt lgkmcnt(4)
v_mfma_f32_16x16x32_bf16 v[32:35], a[192:195], a[0:3], 0
ds_read_b64_tr_b16 a[204:205], v5 offset:57344
ds_read_b64_tr_b16 a[206:207], v5 offset:61440
v_mfma_f32_16x16x32_bf16 v[36:39], a[192:195], a[64:67], 0
s_waitcnt lgkmcnt(4)
v_mfma_f32_16x16x32_bf16 v[32:35], a[196:199], a[4:7], v[32:35]
ds_read_b64_tr_b16 a[192:193], v6 offset:16384
ds_read_b64_tr_b16 a[194:195], v6 offset:20480
v_mfma_f32_16x16x32_bf16 v[36:39], a[196:199], a[68:71], v[36:39]
v_fma_f32 v128, v22, v68, v100
v_fma_f32 v129, v22, v69, v101
v_fma_f32 v130, v22, v70, v102
v_fma_f32 v131, v22, v71, v103
s_mov_b32 s64, 8192
buffer_load_dwordx4 a[160:163], v1, s[48:51], s64 offen offset:0 sc1
s_waitcnt lgkmcnt(4)
v_mfma_f32_16x16x32_bf16 v[32:35], a[200:203], a[8:11], v[32:35]
ds_read_b64_tr_b16 a[196:197], v6 offset:24576
ds_read_b64_tr_b16 a[198:199], v6 offset:28672
v_mfma_f32_16x16x32_bf16 v[36:39], a[200:203], a[72:75], v[36:39]
v_fma_f32 v132, v23, v68, v100
v_fma_f32 v133, v23, v69, v101
v_fma_f32 v134, v23, v70, v102
v_fma_f32 v135, v23, v71, v103
s_waitcnt lgkmcnt(4)
v_mfma_f32_16x16x32_bf16 v[32:35], a[204:207], a[12:15], v[32:35]
ds_read_b64_tr_b16 a[200:201], v6 offset:49152
ds_read_b64_tr_b16 a[202:203], v6 offset:53248
v_mfma_f32_16x16x32_bf16 v[36:39], a[204:207], a[76:79], v[36:39]
v_fma_f32 v56, v20, v40, v128
v_fma_f32 v57, v20, v41, v129
v_fma_f32 v58, v20, v42, v130
v_fma_f32 v59, v20, v43, v131
buffer_load_dwordx4 a[164:167], v1, s[48:51], s64 offen offset:1024 sc1
s_waitcnt lgkmcnt(4)
v_mfma_f32_16x16x32_bf16 v[32:35], a[192:195], a[16:19], v[32:35]
ds_read_b64_tr_b16 a[204:205], v6 offset:57344
ds_read_b64_tr_b16 a[206:207], v6 offset:61440
v_mfma_f32_16x16x32_bf16 v[36:39], a[192:195], a[80:83], v[36:39]
v_fma_f32 v60, v21, v44, v132
v_fma_f32 v61, v21, v45, v133
v_fma_f32 v62, v21, v46, v134
v_fma_f32 v63, v21, v47, v135
s_waitcnt lgkmcnt(4)
v_mfma_f32_16x16x32_bf16 v[32:35], a[196:199], a[20:23], v[32:35]
ds_read_b64_tr_b16 a[192:193], v7 offset:0
ds_read_b64_tr_b16 a[194:195], v7 offset:4096
v_mfma_f32_16x16x32_bf16 v[36:39], a[196:199], a[84:87], v[36:39]
v_exp_f32 v128, v56
v_exp_f32 v129, v57
v_exp_f32 v130, v58
v_exp_f32 v131, v59
buffer_load_dwordx4 a[168:171], v1, s[48:51], s64 offen offset:2048 sc1
s_waitcnt lgkmcnt(4)
v_mfma_f32_16x16x32_bf16 v[32:35], a[200:203], a[24:27], v[32:35]
ds_read_b64_tr_b16 a[196:197], v7 offset:8192
ds_read_b64_tr_b16 a[198:199], v7 offset:12288
v_mfma_f32_16x16x32_bf16 v[36:39], a[200:203], a[88:91], v[36:39]
v_exp_f32 v132, v60
v_exp_f32 v133, v61
v_exp_f32 v134, v62
v_exp_f32 v135, v63
s_waitcnt lgkmcnt(4)
v_mfma_f32_16x16x32_bf16 v[32:35], a[204:207], a[28:31], v[32:35]
ds_read_b64_tr_b16 a[200:201], v7 offset:16384
ds_read_b64_tr_b16 a[202:203], v7 offset:20480
v_mfma_f32_16x16x32_bf16 v[36:39], a[204:207], a[92:95], v[36:39]
v_add_f32 v128, 1.0, v128
v_add_f32 v129, 1.0, v129
v_add_f32 v130, 1.0, v130
v_add_f32 v131, 1.0, v131
buffer_load_dwordx4 a[172:175], v1, s[48:51], s64 offen offset:3072 sc1
s_waitcnt lgkmcnt(4)
v_mfma_f32_16x16x32_bf16 v[32:35], a[192:195], a[32:35], v[32:35]
ds_read_b64_tr_b16 a[204:205], v7 offset:24576
ds_read_b64_tr_b16 a[206:207], v7 offset:28672
v_mfma_f32_16x16x32_bf16 v[36:39], a[192:195], a[96:99], v[36:39]
v_add_f32 v132, 1.0, v132
v_add_f32 v133, 1.0, v133
v_add_f32 v134, 1.0, v134
v_add_f32 v135, 1.0, v135
s_waitcnt lgkmcnt(4)
v_mfma_f32_16x16x32_bf16 v[32:35], a[196:199], a[36:39], v[32:35]
ds_read_b64_tr_b16 a[192:193], v5 offset:0
ds_read_b64_tr_b16 a[194:195], v5 offset:4096
v_mfma_f32_16x16x32_bf16 v[36:39], a[196:199], a[100:103], v[36:39]
v_rcp_f32 v128, v128
v_rcp_f32 v129, v129
v_rcp_f32 v130, v130
v_rcp_f32 v131, v131
s_mov_b32 s64, 12288
buffer_load_dwordx4 a[176:179], v1, s[48:51], s64 offen offset:0 sc1
s_waitcnt lgkmcnt(4)
v_mfma_f32_16x16x32_bf16 v[32:35], a[200:203], a[40:43], v[32:35]
ds_read_b64_tr_b16 a[196:197], v5 offset:8192
ds_read_b64_tr_b16 a[198:199], v5 offset:12288
v_mfma_f32_16x16x32_bf16 v[36:39], a[200:203], a[104:107], v[36:39]
v_rcp_f32 v132, v132
v_rcp_f32 v133, v133
v_rcp_f32 v134, v134
s_waitcnt lgkmcnt(4)
v_mfma_f32_16x16x32_bf16 v[32:35], a[204:207], a[44:47], v[32:35]
ds_read_b64_tr_b16 a[200:201], v5 offset:32768
ds_read_b64_tr_b16 a[202:203], v5 offset:36864
v_mfma_f32_16x16x32_bf16 v[36:39], a[204:207], a[108:111], v[36:39]
v_rcp_f32 v135, v135
v_mul_f32 v56, v56, v128
v_mul_f32 v57, v57, v129
buffer_load_dwordx4 a[180:183], v1, s[48:51], s64 offen offset:1024 sc1
s_waitcnt lgkmcnt(4)
v_mfma_f32_16x16x32_bf16 v[32:35], a[192:195], a[48:51], v[32:35]
ds_read_b64_tr_b16 a[204:205], v5 offset:40960
ds_read_b64_tr_b16 a[206:207], v5 offset:45056
v_mfma_f32_16x16x32_bf16 v[36:39], a[192:195], a[112:115], v[36:39]
v_mul_f32 v58, v58, v130
v_mul_f32 v59, v59, v131
v_mul_f32 v60, v60, v132
s_waitcnt lgkmcnt(4)
v_mfma_f32_16x16x32_bf16 v[32:35], a[196:199], a[52:55], v[32:35]
ds_read_b64_tr_b16 a[192:193], v8 offset:16384
ds_read_b64_tr_b16 a[194:195], v8 offset:20480
v_mfma_f32_16x16x32_bf16 v[36:39], a[196:199], a[116:119], v[36:39]
v_mul_f32 v61, v61, v133
v_mul_f32 v62, v62, v134
v_mul_f32 v63, v63, v135
v_xor_b32 v5, 128, v4
v_add_u32 v6, 0x10000, v5
v_add_u32 v7, 0x20000, v5
buffer_load_dwordx4 a[184:187], v1, s[48:51], s64 offen offset:2048 sc1
s_waitcnt lgkmcnt(4)
v_mfma_f32_16x16x32_bf16 v[32:35], a[200:203], a[56:59], v[32:35]
ds_read_b64_tr_b16 a[196:197], v8 offset:24576
ds_read_b64_tr_b16 a[198:199], v8 offset:28672
v_mfma_f32_16x16x32_bf16 v[36:39], a[200:203], a[120:123], v[36:39]
v_cvt_pk_bf16_f32 v50, v56, v57
v_cvt_pk_bf16_f32 v51, v58, v59
v_cvt_pk_bf16_f32 v54, v60, v61
s_waitcnt lgkmcnt(4)
v_mfma_f32_16x16x32_bf16 v[32:35], a[204:207], a[60:63], v[32:35]
ds_read_b64_tr_b16 a[200:201], v8 offset:49152
ds_read_b64_tr_b16 a[202:203], v8 offset:53248
v_mfma_f32_16x16x32_bf16 v[36:39], a[204:207], a[124:127], v[36:39]
v_cvt_pk_bf16_f32 v55, v62, v63
buffer_store_dwordx4 v[48:51], v1, s[52:55], s73 offen offset:0 sc1
buffer_store_dwordx4 v[52:55], v1, s[52:55], s66 offen offset:0 sc1
buffer_load_dwordx4 a[188:191], v1, s[48:51], s64 offen offset:3072 sc1
s_waitcnt lgkmcnt(4)
v_mfma_f32_16x16x32_bf16 v[40:43], a[192:195], a[0:3], 0
ds_read_b64_tr_b16 a[204:205], v8 offset:57344
ds_read_b64_tr_b16 a[206:207], v8 offset:61440
v_mfma_f32_16x16x32_bf16 v[44:47], a[192:195], a[64:67], 0
s_waitcnt lgkmcnt(4)
v_mfma_f32_16x16x32_bf16 v[40:43], a[196:199], a[4:7], v[40:43]
ds_read_b64_tr_b16 a[192:193], v9 offset:16384
ds_read_b64_tr_b16 a[194:195], v9 offset:20480
v_mfma_f32_16x16x32_bf16 v[44:47], a[196:199], a[68:71], v[44:47]
v_fma_f32 v128, v22, v72, v104
v_fma_f32 v129, v22, v73, v105
v_fma_f32 v130, v22, v74, v106
v_fma_f32 v131, v22, v75, v107
s_mov_b32 s64, 16384
buffer_load_dwordx4 a[208:211], v1, s[48:51], s64 offen offset:0 sc1
s_waitcnt lgkmcnt(4)
v_mfma_f32_16x16x32_bf16 v[40:43], a[200:203], a[8:11], v[40:43]
ds_read_b64_tr_b16 a[196:197], v9 offset:24576
ds_read_b64_tr_b16 a[198:199], v9 offset:28672
v_mfma_f32_16x16x32_bf16 v[44:47], a[200:203], a[72:75], v[44:47]
v_fma_f32 v132, v23, v72, v104
v_fma_f32 v133, v23, v73, v105
v_fma_f32 v134, v23, v74, v106
v_fma_f32 v135, v23, v75, v107
s_waitcnt lgkmcnt(4)
v_mfma_f32_16x16x32_bf16 v[40:43], a[204:207], a[12:15], v[40:43]
ds_read_b64_tr_b16 a[200:201], v9 offset:49152
ds_read_b64_tr_b16 a[202:203], v9 offset:53248
v_mfma_f32_16x16x32_bf16 v[44:47], a[204:207], a[76:79], v[44:47]
v_fma_f32 v56, v20, v32, v128
v_fma_f32 v57, v20, v33, v129
v_fma_f32 v58, v20, v34, v130
v_fma_f32 v59, v20, v35, v131
buffer_load_dwordx4 a[212:215], v1, s[48:51], s64 offen offset:1024 sc1
s_waitcnt lgkmcnt(4)
v_mfma_f32_16x16x32_bf16 v[40:43], a[192:195], a[16:19], v[40:43]
ds_read_b64_tr_b16 a[204:205], v9 offset:57344
ds_read_b64_tr_b16 a[206:207], v9 offset:61440
v_mfma_f32_16x16x32_bf16 v[44:47], a[192:195], a[80:83], v[44:47]
v_fma_f32 v60, v21, v36, v132
v_fma_f32 v61, v21, v37, v133
v_fma_f32 v62, v21, v38, v134
v_fma_f32 v63, v21, v39, v135
s_waitcnt lgkmcnt(4)
v_mfma_f32_16x16x32_bf16 v[40:43], a[196:199], a[20:23], v[40:43]
ds_read_b64_tr_b16 a[192:193], v10 offset:0
ds_read_b64_tr_b16 a[194:195], v10 offset:4096
v_mfma_f32_16x16x32_bf16 v[44:47], a[196:199], a[84:87], v[44:47]
v_exp_f32 v128, v56
v_exp_f32 v129, v57
v_exp_f32 v130, v58
v_exp_f32 v131, v59
buffer_load_dwordx4 a[216:219], v1, s[48:51], s64 offen offset:2048 sc1
s_waitcnt lgkmcnt(4)
v_mfma_f32_16x16x32_bf16 v[40:43], a[200:203], a[24:27], v[40:43]
ds_read_b64_tr_b16 a[196:197], v10 offset:8192
)ASM"
R"ASM(ds_read_b64_tr_b16 a[198:199], v10 offset:12288
v_mfma_f32_16x16x32_bf16 v[44:47], a[200:203], a[88:91], v[44:47]
v_exp_f32 v132, v60
v_exp_f32 v133, v61
v_exp_f32 v134, v62
v_exp_f32 v135, v63
s_waitcnt lgkmcnt(4)
v_mfma_f32_16x16x32_bf16 v[40:43], a[204:207], a[28:31], v[40:43]
ds_read_b64_tr_b16 a[200:201], v10 offset:16384
ds_read_b64_tr_b16 a[202:203], v10 offset:20480
v_mfma_f32_16x16x32_bf16 v[44:47], a[204:207], a[92:95], v[44:47]
v_add_f32 v128, 1.0, v128
v_add_f32 v129, 1.0, v129
v_add_f32 v130, 1.0, v130
v_add_f32 v131, 1.0, v131
buffer_load_dwordx4 a[220:223], v1, s[48:51], s64 offen offset:3072 sc1
s_waitcnt lgkmcnt(4)
v_mfma_f32_16x16x32_bf16 v[40:43], a[192:195], a[32:35], v[40:43]
ds_read_b64_tr_b16 a[204:205], v10 offset:24576
ds_read_b64_tr_b16 a[206:207], v10 offset:28672
v_mfma_f32_16x16x32_bf16 v[44:47], a[192:195], a[96:99], v[44:47]
v_add_f32 v132, 1.0, v132
v_add_f32 v133, 1.0, v133
v_add_f32 v134, 1.0, v134
s_waitcnt lgkmcnt(4)
v_mfma_f32_16x16x32_bf16 v[40:43], a[196:199], a[36:39], v[40:43]
ds_read_b64_tr_b16 a[192:193], v8 offset:0
ds_read_b64_tr_b16 a[194:195], v8 offset:4096
v_mfma_f32_16x16x32_bf16 v[44:47], a[196:199], a[100:103], v[44:47]
v_add_f32 v135, 1.0, v135
v_rcp_f32 v128, v128
v_rcp_f32 v129, v129
s_mov_b32 s64, 20480
buffer_load_dwordx4 a[224:227], v1, s[48:51], s64 offen offset:0 sc1
s_waitcnt lgkmcnt(4)
v_mfma_f32_16x16x32_bf16 v[40:43], a[200:203], a[40:43], v[40:43]
ds_read_b64_tr_b16 a[196:197], v8 offset:8192
ds_read_b64_tr_b16 a[198:199], v8 offset:12288
v_mfma_f32_16x16x32_bf16 v[44:47], a[200:203], a[104:107], v[44:47]
v_rcp_f32 v130, v130
v_rcp_f32 v131, v131
v_rcp_f32 v132, v132
s_waitcnt lgkmcnt(4)
v_mfma_f32_16x16x32_bf16 v[40:43], a[204:207], a[44:47], v[40:43]
ds_read_b64_tr_b16 a[200:201], v8 offset:32768
ds_read_b64_tr_b16 a[202:203], v8 offset:36864
v_mfma_f32_16x16x32_bf16 v[44:47], a[204:207], a[108:111], v[44:47]
v_rcp_f32 v133, v133
v_rcp_f32 v134, v134
v_rcp_f32 v135, v135
buffer_load_dwordx4 a[228:231], v1, s[48:51], s64 offen offset:1024 sc1
s_waitcnt lgkmcnt(4)
v_mfma_f32_16x16x32_bf16 v[40:43], a[192:195], a[48:51], v[40:43]
ds_read_b64_tr_b16 a[204:205], v8 offset:40960
ds_read_b64_tr_b16 a[206:207], v8 offset:45056
v_mfma_f32_16x16x32_bf16 v[44:47], a[192:195], a[112:115], v[44:47]
v_mul_f32 v56, v56, v128
v_mul_f32 v57, v57, v129
v_mul_f32 v58, v58, v130
s_waitcnt lgkmcnt(4)
v_mfma_f32_16x16x32_bf16 v[40:43], a[196:199], a[52:55], v[40:43]
ds_read_b64_tr_b16 a[192:193], v5 offset:16384
ds_read_b64_tr_b16 a[194:195], v5 offset:20480
v_mfma_f32_16x16x32_bf16 v[44:47], a[196:199], a[116:119], v[44:47]
v_mul_f32 v59, v59, v131
v_mul_f32 v60, v60, v132
v_mul_f32 v61, v61, v133
v_xor_b32 v8, 160, v4
v_add_u32 v9, 0x10000, v8
v_add_u32 v10, 0x20000, v8
buffer_load_dwordx4 a[232:235], v1, s[48:51], s64 offen offset:2048 sc1
s_waitcnt lgkmcnt(4)
v_mfma_f32_16x16x32_bf16 v[40:43], a[200:203], a[56:59], v[40:43]
ds_read_b64_tr_b16 a[196:197], v5 offset:24576
ds_read_b64_tr_b16 a[198:199], v5 offset:28672
v_mfma_f32_16x16x32_bf16 v[44:47], a[200:203], a[120:123], v[44:47]
v_mul_f32 v62, v62, v134
v_mul_f32 v63, v63, v135
v_cvt_pk_bf16_f32 v48, v56, v57
s_waitcnt lgkmcnt(4)
v_mfma_f32_16x16x32_bf16 v[40:43], a[204:207], a[60:63], v[40:43]
ds_read_b64_tr_b16 a[200:201], v5 offset:49152
ds_read_b64_tr_b16 a[202:203], v5 offset:53248
v_mfma_f32_16x16x32_bf16 v[44:47], a[204:207], a[124:127], v[44:47]
v_cvt_pk_bf16_f32 v49, v58, v59
v_cvt_pk_bf16_f32 v52, v60, v61
v_cvt_pk_bf16_f32 v53, v62, v63
buffer_load_dwordx4 a[236:239], v1, s[48:51], s64 offen offset:3072 sc1
s_waitcnt lgkmcnt(4)
v_mfma_f32_16x16x32_bf16 v[32:35], a[192:195], a[0:3], 0
ds_read_b64_tr_b16 a[204:205], v5 offset:57344
ds_read_b64_tr_b16 a[206:207], v5 offset:61440
v_mfma_f32_16x16x32_bf16 v[36:39], a[192:195], a[64:67], 0
s_waitcnt lgkmcnt(4)
v_mfma_f32_16x16x32_bf16 v[32:35], a[196:199], a[4:7], v[32:35]
ds_read_b64_tr_b16 a[192:193], v6 offset:16384
ds_read_b64_tr_b16 a[194:195], v6 offset:20480
v_mfma_f32_16x16x32_bf16 v[36:39], a[196:199], a[68:71], v[36:39]
v_fma_f32 v128, v22, v76, v108
v_fma_f32 v129, v22, v77, v109
v_fma_f32 v130, v22, v78, v110
v_fma_f32 v131, v22, v79, v111
s_mov_b32 s64, 24576
buffer_load_dwordx4 a[240:243], v1, s[48:51], s64 offen offset:0 sc1
s_waitcnt lgkmcnt(4)
v_mfma_f32_16x16x32_bf16 v[32:35], a[200:203], a[8:11], v[32:35]
ds_read_b64_tr_b16 a[196:197], v6 offset:24576
ds_read_b64_tr_b16 a[198:199], v6 offset:28672
v_mfma_f32_16x16x32_bf16 v[36:39], a[200:203], a[72:75], v[36:39]
v_fma_f32 v132, v23, v76, v108
v_fma_f32 v133, v23, v77, v109
v_fma_f32 v134, v23, v78, v110
v_fma_f32 v135, v23, v79, v111
s_waitcnt lgkmcnt(4)
v_mfma_f32_16x16x32_bf16 v[32:35], a[204:207], a[12:15], v[32:35]
ds_read_b64_tr_b16 a[200:201], v6 offset:49152
ds_read_b64_tr_b16 a[202:203], v6 offset:53248
v_mfma_f32_16x16x32_bf16 v[36:39], a[204:207], a[76:79], v[36:39]
v_fma_f32 v56, v20, v40, v128
v_fma_f32 v57, v20, v41, v129
v_fma_f32 v58, v20, v42, v130
v_fma_f32 v59, v20, v43, v131
buffer_load_dwordx4 a[244:247], v1, s[48:51], s64 offen offset:1024 sc1
s_waitcnt lgkmcnt(4)
v_mfma_f32_16x16x32_bf16 v[32:35], a[192:195], a[16:19], v[32:35]
ds_read_b64_tr_b16 a[204:205], v6 offset:57344
ds_read_b64_tr_b16 a[206:207], v6 offset:61440
v_mfma_f32_16x16x32_bf16 v[36:39], a[192:195], a[80:83], v[36:39]
v_fma_f32 v60, v21, v44, v132
v_fma_f32 v61, v21, v45, v133
v_fma_f32 v62, v21, v46, v134
v_fma_f32 v63, v21, v47, v135
s_waitcnt lgkmcnt(4)
v_mfma_f32_16x16x32_bf16 v[32:35], a[196:199], a[20:23], v[32:35]
ds_read_b64_tr_b16 a[192:193], v7 offset:0
ds_read_b64_tr_b16 a[194:195], v7 offset:4096
v_mfma_f32_16x16x32_bf16 v[36:39], a[196:199], a[84:87], v[36:39]
v_exp_f32 v128, v56
v_exp_f32 v129, v57
v_exp_f32 v130, v58
v_exp_f32 v131, v59
buffer_load_dwordx4 a[248:251], v1, s[48:51], s64 offen offset:2048 sc1
s_waitcnt lgkmcnt(4)
v_mfma_f32_16x16x32_bf16 v[32:35], a[200:203], a[24:27], v[32:35]
ds_read_b64_tr_b16 a[196:197], v7 offset:8192
ds_read_b64_tr_b16 a[198:199], v7 offset:12288
v_mfma_f32_16x16x32_bf16 v[36:39], a[200:203], a[88:91], v[36:39]
v_exp_f32 v132, v60
v_exp_f32 v133, v61
v_exp_f32 v134, v62
v_exp_f32 v135, v63
s_waitcnt lgkmcnt(4)
v_mfma_f32_16x16x32_bf16 v[32:35], a[204:207], a[28:31], v[32:35]
ds_read_b64_tr_b16 a[200:201], v7 offset:16384
ds_read_b64_tr_b16 a[202:203], v7 offset:20480
v_mfma_f32_16x16x32_bf16 v[36:39], a[204:207], a[92:95], v[36:39]
v_add_f32 v128, 1.0, v128
v_add_f32 v129, 1.0, v129
v_add_f32 v130, 1.0, v130
v_add_f32 v131, 1.0, v131
buffer_load_dwordx4 a[252:255], v1, s[48:51], s64 offen offset:3072 sc1
s_waitcnt lgkmcnt(4)
v_mfma_f32_16x16x32_bf16 v[32:35], a[192:195], a[32:35], v[32:35]
ds_read_b64_tr_b16 a[204:205], v7 offset:24576
ds_read_b64_tr_b16 a[206:207], v7 offset:28672
v_mfma_f32_16x16x32_bf16 v[36:39], a[192:195], a[96:99], v[36:39]
v_add_f32 v132, 1.0, v132
v_add_f32 v133, 1.0, v133
v_add_f32 v134, 1.0, v134
v_add_f32 v135, 1.0, v135
s_waitcnt lgkmcnt(4)
v_mfma_f32_16x16x32_bf16 v[32:35], a[196:199], a[36:39], v[32:35]
ds_read_b64_tr_b16 a[192:193], v5 offset:0
ds_read_b64_tr_b16 a[194:195], v5 offset:4096
v_mfma_f32_16x16x32_bf16 v[36:39], a[196:199], a[100:103], v[36:39]
v_rcp_f32 v128, v128
v_rcp_f32 v129, v129
v_rcp_f32 v130, v130
v_rcp_f32 v131, v131
s_mov_b32 s64, 28672
buffer_load_dwordx4 v[144:147], v1, s[48:51], s64 offen offset:0 sc1
s_waitcnt lgkmcnt(4)
v_mfma_f32_16x16x32_bf16 v[32:35], a[200:203], a[40:43], v[32:35]
ds_read_b64_tr_b16 a[196:197], v5 offset:8192
ds_read_b64_tr_b16 a[198:199], v5 offset:12288
v_mfma_f32_16x16x32_bf16 v[36:39], a[200:203], a[104:107], v[36:39]
v_rcp_f32 v132, v132
v_rcp_f32 v133, v133
v_rcp_f32 v134, v134
s_waitcnt lgkmcnt(4)
v_mfma_f32_16x16x32_bf16 v[32:35], a[204:207], a[44:47], v[32:35]
ds_read_b64_tr_b16 a[200:201], v5 offset:32768
ds_read_b64_tr_b16 a[202:203], v5 offset:36864
v_mfma_f32_16x16x32_bf16 v[36:39], a[204:207], a[108:111], v[36:39]
v_rcp_f32 v135, v135
v_mul_f32 v56, v56, v128
v_mul_f32 v57, v57, v129
buffer_load_dwordx4 v[148:151], v1, s[48:51], s64 offen offset:1024 sc1
s_waitcnt lgkmcnt(4)
v_mfma_f32_16x16x32_bf16 v[32:35], a[192:195], a[48:51], v[32:35]
ds_read_b64_tr_b16 a[204:205], v5 offset:40960
ds_read_b64_tr_b16 a[206:207], v5 offset:45056
v_mfma_f32_16x16x32_bf16 v[36:39], a[192:195], a[112:115], v[36:39]
v_mul_f32 v58, v58, v130
v_mul_f32 v59, v59, v131
v_mul_f32 v60, v60, v132
s_waitcnt lgkmcnt(4)
v_mfma_f32_16x16x32_bf16 v[32:35], a[196:199], a[52:55], v[32:35]
ds_read_b64_tr_b16 a[192:193], v8 offset:16384
ds_read_b64_tr_b16 a[194:195], v8 offset:20480
v_mfma_f32_16x16x32_bf16 v[36:39], a[196:199], a[116:119], v[36:39]
v_mul_f32 v61, v61, v133
v_mul_f32 v62, v62, v134
v_mul_f32 v63, v63, v135
v_xor_b32 v5, 192, v4
v_add_u32 v6, 0x10000, v5
v_add_u32 v7, 0x20000, v5
buffer_load_dwordx4 v[152:155], v1, s[48:51], s64 offen offset:2048 sc1
s_waitcnt lgkmcnt(4)
v_mfma_f32_16x16x32_bf16 v[32:35], a[200:203], a[56:59], v[32:35]
ds_read_b64_tr_b16 a[196:197], v8 offset:24576
ds_read_b64_tr_b16 a[198:199], v8 offset:28672
v_mfma_f32_16x16x32_bf16 v[36:39], a[200:203], a[120:123], v[36:39]
v_cvt_pk_bf16_f32 v50, v56, v57
v_cvt_pk_bf16_f32 v51, v58, v59
v_cvt_pk_bf16_f32 v54, v60, v61
s_waitcnt lgkmcnt(4)
v_mfma_f32_16x16x32_bf16 v[32:35], a[204:207], a[60:63], v[32:35]
ds_read_b64_tr_b16 a[200:201], v8 offset:49152
ds_read_b64_tr_b16 a[202:203], v8 offset:53248
v_mfma_f32_16x16x32_bf16 v[36:39], a[204:207], a[124:127], v[36:39]
v_cvt_pk_bf16_f32 v55, v62, v63
buffer_store_dwordx4 v[48:51], v1, s[52:55], s73 offen offset:1024 sc1
buffer_store_dwordx4 v[52:55], v1, s[52:55], s66 offen offset:1024 sc1
buffer_load_dwordx4 v[156:159], v1, s[48:51], s64 offen offset:3072 sc1
s_waitcnt lgkmcnt(4)
v_mfma_f32_16x16x32_bf16 v[40:43], a[192:195], a[0:3], 0
ds_read_b64_tr_b16 a[204:205], v8 offset:57344
ds_read_b64_tr_b16 a[206:207], v8 offset:61440
v_mfma_f32_16x16x32_bf16 v[44:47], a[192:195], a[64:67], 0
s_waitcnt lgkmcnt(4)
v_mfma_f32_16x16x32_bf16 v[40:43], a[196:199], a[4:7], v[40:43]
ds_read_b64_tr_b16 a[192:193], v9 offset:16384
ds_read_b64_tr_b16 a[194:195], v9 offset:20480
v_mfma_f32_16x16x32_bf16 v[44:47], a[196:199], a[68:71], v[44:47]
v_fma_f32 v128, v22, v80, v112
v_fma_f32 v129, v22, v81, v113
v_fma_f32 v130, v22, v82, v114
v_fma_f32 v131, v22, v83, v115
s_mov_b32 s64, 32768
buffer_load_dwordx4 v[160:163], v1, s[48:51], s64 offen offset:0 sc1
s_waitcnt lgkmcnt(4)
v_mfma_f32_16x16x32_bf16 v[40:43], a[200:203], a[8:11], v[40:43]
ds_read_b64_tr_b16 a[196:197], v9 offset:24576
ds_read_b64_tr_b16 a[198:199], v9 offset:28672
v_mfma_f32_16x16x32_bf16 v[44:47], a[200:203], a[72:75], v[44:47]
v_fma_f32 v132, v23, v80, v112
v_fma_f32 v133, v23, v81, v113
v_fma_f32 v134, v23, v82, v114
v_fma_f32 v135, v23, v83, v115
s_waitcnt lgkmcnt(4)
v_mfma_f32_16x16x32_bf16 v[40:43], a[204:207], a[12:15], v[40:43]
ds_read_b64_tr_b16 a[200:201], v9 offset:49152
ds_read_b64_tr_b16 a[202:203], v9 offset:53248
v_mfma_f32_16x16x32_bf16 v[44:47], a[204:207], a[76:79], v[44:47]
v_fma_f32 v56, v20, v32, v128
v_fma_f32 v57, v20, v33, v129
v_fma_f32 v58, v20, v34, v130
v_fma_f32 v59, v20, v35, v131
buffer_load_dwordx4 v[164:167], v1, s[48:51], s64 offen offset:1024 sc1
s_waitcnt lgkmcnt(4)
v_mfma_f32_16x16x32_bf16 v[40:43], a[192:195], a[16:19], v[40:43]
ds_read_b64_tr_b16 a[204:205], v9 offset:57344
ds_read_b64_tr_b16 a[206:207], v9 offset:61440
v_mfma_f32_16x16x32_bf16 v[44:47], a[192:195], a[80:83], v[44:47]
v_fma_f32 v60, v21, v36, v132
v_fma_f32 v61, v21, v37, v133
v_fma_f32 v62, v21, v38, v134
v_fma_f32 v63, v21, v39, v135
s_waitcnt lgkmcnt(4)
v_mfma_f32_16x16x32_bf16 v[40:43], a[196:199], a[20:23], v[40:43]
ds_read_b64_tr_b16 a[192:193], v10 offset:0
ds_read_b64_tr_b16 a[194:195], v10 offset:4096
v_mfma_f32_16x16x32_bf16 v[44:47], a[196:199], a[84:87], v[44:47]
v_exp_f32 v128, v56
v_exp_f32 v129, v57
v_exp_f32 v130, v58
v_exp_f32 v131, v59
buffer_load_dwordx4 v[168:171], v1, s[48:51], s64 offen offset:2048 sc1
s_waitcnt lgkmcnt(4)
v_mfma_f32_16x16x32_bf16 v[40:43], a[200:203], a[24:27], v[40:43]
ds_read_b64_tr_b16 a[196:197], v10 offset:8192
ds_read_b64_tr_b16 a[198:199], v10 offset:12288
v_mfma_f32_16x16x32_bf16 v[44:47], a[200:203], a[88:91], v[44:47]
v_exp_f32 v132, v60
v_exp_f32 v133, v61
v_exp_f32 v134, v62
v_exp_f32 v135, v63
s_waitcnt lgkmcnt(4)
v_mfma_f32_16x16x32_bf16 v[40:43], a[204:207], a[28:31], v[40:43]
)ASM"
R"ASM(ds_read_b64_tr_b16 a[200:201], v10 offset:16384
ds_read_b64_tr_b16 a[202:203], v10 offset:20480
v_mfma_f32_16x16x32_bf16 v[44:47], a[204:207], a[92:95], v[44:47]
v_add_f32 v128, 1.0, v128
v_add_f32 v129, 1.0, v129
v_add_f32 v130, 1.0, v130
v_add_f32 v131, 1.0, v131
buffer_load_dwordx4 v[172:175], v1, s[48:51], s64 offen offset:3072 sc1
s_waitcnt lgkmcnt(4)
v_mfma_f32_16x16x32_bf16 v[40:43], a[192:195], a[32:35], v[40:43]
ds_read_b64_tr_b16 a[204:205], v10 offset:24576
ds_read_b64_tr_b16 a[206:207], v10 offset:28672
v_mfma_f32_16x16x32_bf16 v[44:47], a[192:195], a[96:99], v[44:47]
v_add_f32 v132, 1.0, v132
v_add_f32 v133, 1.0, v133
v_add_f32 v134, 1.0, v134
s_waitcnt lgkmcnt(4)
v_mfma_f32_16x16x32_bf16 v[40:43], a[196:199], a[36:39], v[40:43]
ds_read_b64_tr_b16 a[192:193], v8 offset:0
ds_read_b64_tr_b16 a[194:195], v8 offset:4096
v_mfma_f32_16x16x32_bf16 v[44:47], a[196:199], a[100:103], v[44:47]
v_add_f32 v135, 1.0, v135
v_rcp_f32 v128, v128
v_rcp_f32 v129, v129
s_mov_b32 s64, 36864
buffer_load_dwordx4 v[192:195], v1, s[48:51], s64 offen offset:0 sc1
s_waitcnt lgkmcnt(4)
v_mfma_f32_16x16x32_bf16 v[40:43], a[200:203], a[40:43], v[40:43]
ds_read_b64_tr_b16 a[196:197], v8 offset:8192
ds_read_b64_tr_b16 a[198:199], v8 offset:12288
v_mfma_f32_16x16x32_bf16 v[44:47], a[200:203], a[104:107], v[44:47]
v_rcp_f32 v130, v130
v_rcp_f32 v131, v131
v_rcp_f32 v132, v132
s_waitcnt lgkmcnt(4)
v_mfma_f32_16x16x32_bf16 v[40:43], a[204:207], a[44:47], v[40:43]
ds_read_b64_tr_b16 a[200:201], v8 offset:32768
ds_read_b64_tr_b16 a[202:203], v8 offset:36864
v_mfma_f32_16x16x32_bf16 v[44:47], a[204:207], a[108:111], v[44:47]
v_rcp_f32 v133, v133
v_rcp_f32 v134, v134
v_rcp_f32 v135, v135
buffer_load_dwordx4 v[196:199], v1, s[48:51], s64 offen offset:1024 sc1
s_waitcnt lgkmcnt(4)
v_mfma_f32_16x16x32_bf16 v[40:43], a[192:195], a[48:51], v[40:43]
ds_read_b64_tr_b16 a[204:205], v8 offset:40960
ds_read_b64_tr_b16 a[206:207], v8 offset:45056
v_mfma_f32_16x16x32_bf16 v[44:47], a[192:195], a[112:115], v[44:47]
v_mul_f32 v56, v56, v128
v_mul_f32 v57, v57, v129
v_mul_f32 v58, v58, v130
s_waitcnt lgkmcnt(4)
v_mfma_f32_16x16x32_bf16 v[40:43], a[196:199], a[52:55], v[40:43]
ds_read_b64_tr_b16 a[192:193], v5 offset:16384
ds_read_b64_tr_b16 a[194:195], v5 offset:20480
v_mfma_f32_16x16x32_bf16 v[44:47], a[196:199], a[116:119], v[44:47]
v_mul_f32 v59, v59, v131
v_mul_f32 v60, v60, v132
v_mul_f32 v61, v61, v133
v_xor_b32 v8, 224, v4
v_add_u32 v9, 0x10000, v8
v_add_u32 v10, 0x20000, v8
buffer_load_dwordx4 v[200:203], v1, s[48:51], s64 offen offset:2048 sc1
s_waitcnt lgkmcnt(4)
v_mfma_f32_16x16x32_bf16 v[40:43], a[200:203], a[56:59], v[40:43]
ds_read_b64_tr_b16 a[196:197], v5 offset:24576
ds_read_b64_tr_b16 a[198:199], v5 offset:28672
v_mfma_f32_16x16x32_bf16 v[44:47], a[200:203], a[120:123], v[44:47]
v_mul_f32 v62, v62, v134
v_mul_f32 v63, v63, v135
v_cvt_pk_bf16_f32 v48, v56, v57
s_waitcnt lgkmcnt(4)
v_mfma_f32_16x16x32_bf16 v[40:43], a[204:207], a[60:63], v[40:43]
ds_read_b64_tr_b16 a[200:201], v5 offset:49152
ds_read_b64_tr_b16 a[202:203], v5 offset:53248
v_mfma_f32_16x16x32_bf16 v[44:47], a[204:207], a[124:127], v[44:47]
v_cvt_pk_bf16_f32 v49, v58, v59
v_cvt_pk_bf16_f32 v52, v60, v61
v_cvt_pk_bf16_f32 v53, v62, v63
buffer_load_dwordx4 v[204:207], v1, s[48:51], s64 offen offset:3072 sc1
s_waitcnt lgkmcnt(4)
v_mfma_f32_16x16x32_bf16 v[32:35], a[192:195], a[0:3], 0
ds_read_b64_tr_b16 a[204:205], v5 offset:57344
ds_read_b64_tr_b16 a[206:207], v5 offset:61440
v_mfma_f32_16x16x32_bf16 v[36:39], a[192:195], a[64:67], 0
s_waitcnt lgkmcnt(4)
v_mfma_f32_16x16x32_bf16 v[32:35], a[196:199], a[4:7], v[32:35]
ds_read_b64_tr_b16 a[192:193], v6 offset:16384
ds_read_b64_tr_b16 a[194:195], v6 offset:20480
v_mfma_f32_16x16x32_bf16 v[36:39], a[196:199], a[68:71], v[36:39]
v_fma_f32 v128, v22, v84, v116
v_fma_f32 v129, v22, v85, v117
v_fma_f32 v130, v22, v86, v118
v_fma_f32 v131, v22, v87, v119
s_mov_b32 s64, 40960
buffer_load_dwordx4 v[208:211], v1, s[48:51], s64 offen offset:0 sc1
s_waitcnt lgkmcnt(4)
v_mfma_f32_16x16x32_bf16 v[32:35], a[200:203], a[8:11], v[32:35]
ds_read_b64_tr_b16 a[196:197], v6 offset:24576
ds_read_b64_tr_b16 a[198:199], v6 offset:28672
v_mfma_f32_16x16x32_bf16 v[36:39], a[200:203], a[72:75], v[36:39]
v_fma_f32 v132, v23, v84, v116
v_fma_f32 v133, v23, v85, v117
v_fma_f32 v134, v23, v86, v118
v_fma_f32 v135, v23, v87, v119
s_waitcnt lgkmcnt(4)
v_mfma_f32_16x16x32_bf16 v[32:35], a[204:207], a[12:15], v[32:35]
ds_read_b64_tr_b16 a[200:201], v6 offset:49152
ds_read_b64_tr_b16 a[202:203], v6 offset:53248
v_mfma_f32_16x16x32_bf16 v[36:39], a[204:207], a[76:79], v[36:39]
v_fma_f32 v56, v20, v40, v128
v_fma_f32 v57, v20, v41, v129
v_fma_f32 v58, v20, v42, v130
v_fma_f32 v59, v20, v43, v131
buffer_load_dwordx4 v[212:215], v1, s[48:51], s64 offen offset:1024 sc1
s_waitcnt vmcnt(30)
s_waitcnt lgkmcnt(4)
v_mfma_f32_16x16x32_bf16 v[32:35], a[192:195], a[16:19], v[32:35]
ds_read_b64_tr_b16 a[204:205], v6 offset:57344
ds_read_b64_tr_b16 a[206:207], v6 offset:61440
v_mfma_f32_16x16x32_bf16 v[36:39], a[192:195], a[80:83], v[36:39]
v_fma_f32 v60, v21, v44, v132
v_fma_f32 v61, v21, v45, v133
v_fma_f32 v62, v21, v46, v134
v_fma_f32 v63, v21, v47, v135
s_waitcnt lgkmcnt(4)
v_mfma_f32_16x16x32_bf16 v[32:35], a[196:199], a[20:23], v[32:35]
ds_read_b64_tr_b16 a[192:193], v7 offset:0
ds_read_b64_tr_b16 a[194:195], v7 offset:4096
v_mfma_f32_16x16x32_bf16 v[36:39], a[196:199], a[84:87], v[36:39]
v_exp_f32 v128, v56
v_exp_f32 v129, v57
v_exp_f32 v130, v58
v_exp_f32 v131, v59
buffer_load_dwordx4 v[216:219], v1, s[48:51], s64 offen offset:2048 sc1
s_waitcnt lgkmcnt(4)
v_mfma_f32_16x16x32_bf16 v[32:35], a[200:203], a[24:27], v[32:35]
ds_read_b64_tr_b16 a[196:197], v7 offset:8192
ds_read_b64_tr_b16 a[198:199], v7 offset:12288
v_mfma_f32_16x16x32_bf16 v[36:39], a[200:203], a[88:91], v[36:39]
v_exp_f32 v132, v60
v_exp_f32 v133, v61
v_exp_f32 v134, v62
v_exp_f32 v135, v63
s_waitcnt lgkmcnt(4)
v_mfma_f32_16x16x32_bf16 v[32:35], a[204:207], a[28:31], v[32:35]
ds_read_b64_tr_b16 a[200:201], v7 offset:16384
ds_read_b64_tr_b16 a[202:203], v7 offset:20480
v_mfma_f32_16x16x32_bf16 v[36:39], a[204:207], a[92:95], v[36:39]
v_add_f32 v128, 1.0, v128
v_add_f32 v129, 1.0, v129
v_add_f32 v130, 1.0, v130
v_add_f32 v131, 1.0, v131
buffer_load_dwordx4 v[220:223], v1, s[48:51], s64 offen offset:3072 sc1
s_waitcnt lgkmcnt(4)
v_mfma_f32_16x16x32_bf16 v[32:35], a[192:195], a[32:35], v[32:35]
ds_read_b64_tr_b16 a[204:205], v7 offset:24576
ds_read_b64_tr_b16 a[206:207], v7 offset:28672
v_mfma_f32_16x16x32_bf16 v[36:39], a[192:195], a[96:99], v[36:39]
v_add_f32 v132, 1.0, v132
v_add_f32 v133, 1.0, v133
v_add_f32 v134, 1.0, v134
v_add_f32 v135, 1.0, v135
s_waitcnt lgkmcnt(4)
v_mfma_f32_16x16x32_bf16 v[32:35], a[196:199], a[36:39], v[32:35]
ds_read_b64_tr_b16 a[192:193], v5 offset:0
ds_read_b64_tr_b16 a[194:195], v5 offset:4096
v_mfma_f32_16x16x32_bf16 v[36:39], a[196:199], a[100:103], v[36:39]
v_rcp_f32 v128, v128
v_rcp_f32 v129, v129
v_rcp_f32 v130, v130
v_rcp_f32 v131, v131
s_mov_b32 s64, 45056
buffer_load_dwordx4 v[224:227], v1, s[48:51], s64 offen offset:0 sc1
s_waitcnt lgkmcnt(4)
v_mfma_f32_16x16x32_bf16 v[32:35], a[200:203], a[40:43], v[32:35]
ds_read_b64_tr_b16 a[196:197], v5 offset:8192
ds_read_b64_tr_b16 a[198:199], v5 offset:12288
v_mfma_f32_16x16x32_bf16 v[36:39], a[200:203], a[104:107], v[36:39]
v_rcp_f32 v132, v132
v_rcp_f32 v133, v133
v_rcp_f32 v134, v134
s_waitcnt lgkmcnt(4)
v_mfma_f32_16x16x32_bf16 v[32:35], a[204:207], a[44:47], v[32:35]
ds_read_b64_tr_b16 a[200:201], v5 offset:32768
ds_read_b64_tr_b16 a[202:203], v5 offset:36864
v_mfma_f32_16x16x32_bf16 v[36:39], a[204:207], a[108:111], v[36:39]
v_rcp_f32 v135, v135
v_mul_f32 v56, v56, v128
v_mul_f32 v57, v57, v129
buffer_load_dwordx4 v[228:231], v1, s[48:51], s64 offen offset:1024 sc1
s_waitcnt lgkmcnt(4)
v_mfma_f32_16x16x32_bf16 v[32:35], a[192:195], a[48:51], v[32:35]
ds_read_b64_tr_b16 a[204:205], v5 offset:40960
ds_read_b64_tr_b16 a[206:207], v5 offset:45056
v_mfma_f32_16x16x32_bf16 v[36:39], a[192:195], a[112:115], v[36:39]
v_mul_f32 v58, v58, v130
v_mul_f32 v59, v59, v131
v_mul_f32 v60, v60, v132
s_waitcnt lgkmcnt(4)
v_mfma_f32_16x16x32_bf16 v[32:35], a[196:199], a[52:55], v[32:35]
ds_read_b64_tr_b16 a[192:193], v8 offset:16384
ds_read_b64_tr_b16 a[194:195], v8 offset:20480
v_mfma_f32_16x16x32_bf16 v[36:39], a[196:199], a[116:119], v[36:39]
v_mul_f32 v61, v61, v133
v_mul_f32 v62, v62, v134
v_mul_f32 v63, v63, v135
buffer_load_dwordx4 v[232:235], v1, s[48:51], s64 offen offset:2048 sc1
s_waitcnt lgkmcnt(4)
v_mfma_f32_16x16x32_bf16 v[32:35], a[200:203], a[56:59], v[32:35]
ds_read_b64_tr_b16 a[196:197], v8 offset:24576
ds_read_b64_tr_b16 a[198:199], v8 offset:28672
v_mfma_f32_16x16x32_bf16 v[36:39], a[200:203], a[120:123], v[36:39]
v_cvt_pk_bf16_f32 v50, v56, v57
v_cvt_pk_bf16_f32 v51, v58, v59
v_cvt_pk_bf16_f32 v54, v60, v61
s_waitcnt lgkmcnt(4)
v_mfma_f32_16x16x32_bf16 v[32:35], a[204:207], a[60:63], v[32:35]
ds_read_b64_tr_b16 a[200:201], v8 offset:49152
ds_read_b64_tr_b16 a[202:203], v8 offset:53248
v_mfma_f32_16x16x32_bf16 v[36:39], a[204:207], a[124:127], v[36:39]
v_cvt_pk_bf16_f32 v55, v62, v63
buffer_store_dwordx4 v[48:51], v1, s[52:55], s73 offen offset:2048 sc1
buffer_store_dwordx4 v[52:55], v1, s[52:55], s66 offen offset:2048 sc1
buffer_load_dwordx4 v[236:239], v1, s[48:51], s64 offen offset:3072 sc1
s_waitcnt lgkmcnt(4)
v_mfma_f32_16x16x32_bf16 v[40:43], a[192:195], a[0:3], 0
ds_read_b64_tr_b16 a[204:205], v8 offset:57344
ds_read_b64_tr_b16 a[206:207], v8 offset:61440
v_mfma_f32_16x16x32_bf16 v[44:47], a[192:195], a[64:67], 0
s_waitcnt lgkmcnt(4)
v_mfma_f32_16x16x32_bf16 v[40:43], a[196:199], a[4:7], v[40:43]
ds_read_b64_tr_b16 a[192:193], v9 offset:16384
ds_read_b64_tr_b16 a[194:195], v9 offset:20480
v_mfma_f32_16x16x32_bf16 v[44:47], a[196:199], a[68:71], v[44:47]
v_fma_f32 v128, v22, v88, v120
v_fma_f32 v129, v22, v89, v121
v_fma_f32 v130, v22, v90, v122
v_fma_f32 v131, v22, v91, v123
s_waitcnt lgkmcnt(4)
v_mfma_f32_16x16x32_bf16 v[40:43], a[200:203], a[8:11], v[40:43]
ds_read_b64_tr_b16 a[196:197], v9 offset:24576
ds_read_b64_tr_b16 a[198:199], v9 offset:28672
v_mfma_f32_16x16x32_bf16 v[44:47], a[200:203], a[72:75], v[44:47]
v_fma_f32 v132, v23, v88, v120
v_fma_f32 v133, v23, v89, v121
v_fma_f32 v134, v23, v90, v122
v_fma_f32 v135, v23, v91, v123
s_waitcnt lgkmcnt(4)
v_mfma_f32_16x16x32_bf16 v[40:43], a[204:207], a[12:15], v[40:43]
ds_read_b64_tr_b16 a[200:201], v9 offset:49152
ds_read_b64_tr_b16 a[202:203], v9 offset:53248
v_mfma_f32_16x16x32_bf16 v[44:47], a[204:207], a[76:79], v[44:47]
v_fma_f32 v56, v20, v32, v128
v_fma_f32 v57, v20, v33, v129
v_fma_f32 v58, v20, v34, v130
v_fma_f32 v59, v20, v35, v131
s_waitcnt lgkmcnt(4)
v_mfma_f32_16x16x32_bf16 v[40:43], a[192:195], a[16:19], v[40:43]
ds_read_b64_tr_b16 a[204:205], v9 offset:57344
ds_read_b64_tr_b16 a[206:207], v9 offset:61440
v_mfma_f32_16x16x32_bf16 v[44:47], a[192:195], a[80:83], v[44:47]
v_fma_f32 v60, v21, v36, v132
v_fma_f32 v61, v21, v37, v133
v_fma_f32 v62, v21, v38, v134
v_fma_f32 v63, v21, v39, v135
s_waitcnt lgkmcnt(4)
v_mfma_f32_16x16x32_bf16 v[40:43], a[196:199], a[20:23], v[40:43]
ds_read_b64_tr_b16 a[192:193], v10 offset:0
ds_read_b64_tr_b16 a[194:195], v10 offset:4096
v_mfma_f32_16x16x32_bf16 v[44:47], a[196:199], a[84:87], v[44:47]
v_exp_f32 v128, v56
v_exp_f32 v129, v57
v_exp_f32 v130, v58
v_exp_f32 v131, v59
s_waitcnt lgkmcnt(4)
v_mfma_f32_16x16x32_bf16 v[40:43], a[200:203], a[24:27], v[40:43]
ds_read_b64_tr_b16 a[196:197], v10 offset:8192
ds_read_b64_tr_b16 a[198:199], v10 offset:12288
v_mfma_f32_16x16x32_bf16 v[44:47], a[200:203], a[88:91], v[44:47]
v_exp_f32 v132, v60
v_exp_f32 v133, v61
v_exp_f32 v134, v62
v_exp_f32 v135, v63
s_waitcnt lgkmcnt(4)
v_mfma_f32_16x16x32_bf16 v[40:43], a[204:207], a[28:31], v[40:43]
ds_read_b64_tr_b16 a[200:201], v10 offset:16384
ds_read_b64_tr_b16 a[202:203], v10 offset:20480
v_mfma_f32_16x16x32_bf16 v[44:47], a[204:207], a[92:95], v[44:47]
v_add_f32 v128, 1.0, v128
v_add_f32 v129, 1.0, v129
v_add_f32 v130, 1.0, v130
v_add_f32 v131, 1.0, v131
s_waitcnt lgkmcnt(4)
v_mfma_f32_16x16x32_bf16 v[40:43], a[192:195], a[32:35], v[40:43]
ds_read_b64_tr_b16 a[204:205], v10 offset:24576
ds_read_b64_tr_b16 a[206:207], v10 offset:28672
v_mfma_f32_16x16x32_bf16 v[44:47], a[192:195], a[96:99], v[44:47]
v_add_f32 v132, 1.0, v132
v_add_f32 v133, 1.0, v133
)ASM"
R"ASM(v_add_f32 v134, 1.0, v134
s_waitcnt lgkmcnt(4)
v_mfma_f32_16x16x32_bf16 v[40:43], a[196:199], a[36:39], v[40:43]
ds_read_b64_tr_b16 a[192:193], v8 offset:0
ds_read_b64_tr_b16 a[194:195], v8 offset:4096
v_mfma_f32_16x16x32_bf16 v[44:47], a[196:199], a[100:103], v[44:47]
v_add_f32 v135, 1.0, v135
v_rcp_f32 v128, v128
v_rcp_f32 v129, v129
s_waitcnt lgkmcnt(4)
v_mfma_f32_16x16x32_bf16 v[40:43], a[200:203], a[40:43], v[40:43]
ds_read_b64_tr_b16 a[196:197], v8 offset:8192
ds_read_b64_tr_b16 a[198:199], v8 offset:12288
v_mfma_f32_16x16x32_bf16 v[44:47], a[200:203], a[104:107], v[44:47]
v_rcp_f32 v130, v130
v_rcp_f32 v131, v131
v_rcp_f32 v132, v132
s_waitcnt lgkmcnt(4)
v_mfma_f32_16x16x32_bf16 v[40:43], a[204:207], a[44:47], v[40:43]
ds_read_b64_tr_b16 a[200:201], v8 offset:32768
ds_read_b64_tr_b16 a[202:203], v8 offset:36864
v_mfma_f32_16x16x32_bf16 v[44:47], a[204:207], a[108:111], v[44:47]
v_rcp_f32 v133, v133
v_rcp_f32 v134, v134
v_rcp_f32 v135, v135
s_waitcnt lgkmcnt(4)
v_mfma_f32_16x16x32_bf16 v[40:43], a[192:195], a[48:51], v[40:43]
ds_read_b64_tr_b16 a[204:205], v8 offset:40960
ds_read_b64_tr_b16 a[206:207], v8 offset:45056
v_mfma_f32_16x16x32_bf16 v[44:47], a[192:195], a[112:115], v[44:47]
v_mul_f32 v56, v56, v128
v_mul_f32 v57, v57, v129
v_mul_f32 v58, v58, v130
s_waitcnt lgkmcnt(4)
v_mfma_f32_16x16x32_bf16 v[40:43], a[196:199], a[52:55], v[40:43]
v_mfma_f32_16x16x32_bf16 v[44:47], a[196:199], a[116:119], v[44:47]
v_mul_f32 v59, v59, v131
v_mul_f32 v60, v60, v132
v_mul_f32 v61, v61, v133
s_waitcnt lgkmcnt(2)
v_mfma_f32_16x16x32_bf16 v[40:43], a[200:203], a[56:59], v[40:43]
v_mfma_f32_16x16x32_bf16 v[44:47], a[200:203], a[120:123], v[44:47]
v_mul_f32 v62, v62, v134
v_mul_f32 v63, v63, v135
v_cvt_pk_bf16_f32 v48, v56, v57
s_waitcnt lgkmcnt(0)
v_mfma_f32_16x16x32_bf16 v[40:43], a[204:207], a[60:63], v[40:43]
v_mfma_f32_16x16x32_bf16 v[44:47], a[204:207], a[124:127], v[44:47]
v_cvt_pk_bf16_f32 v49, v58, v59
v_cvt_pk_bf16_f32 v52, v60, v61
v_cvt_pk_bf16_f32 v53, v62, v63
s_nop 7
v_fma_f32 v128, v22, v92, v124
v_fma_f32 v129, v22, v93, v125
v_fma_f32 v130, v22, v94, v126
v_fma_f32 v131, v22, v95, v127
v_fma_f32 v132, v23, v92, v124
v_fma_f32 v133, v23, v93, v125
v_fma_f32 v134, v23, v94, v126
v_fma_f32 v135, v23, v95, v127
v_fma_f32 v56, v20, v40, v128
v_fma_f32 v57, v20, v41, v129
v_fma_f32 v58, v20, v42, v130
v_fma_f32 v59, v20, v43, v131
v_fma_f32 v60, v21, v44, v132
v_fma_f32 v61, v21, v45, v133
v_fma_f32 v62, v21, v46, v134
v_fma_f32 v63, v21, v47, v135
v_exp_f32 v128, v56
v_exp_f32 v129, v57
v_exp_f32 v130, v58
v_exp_f32 v131, v59
v_exp_f32 v132, v60
v_exp_f32 v133, v61
v_exp_f32 v134, v62
v_exp_f32 v135, v63
v_add_f32 v128, 1.0, v128
v_add_f32 v129, 1.0, v129
v_add_f32 v130, 1.0, v130
v_add_f32 v131, 1.0, v131
v_add_f32 v132, 1.0, v132
v_add_f32 v133, 1.0, v133
v_add_f32 v134, 1.0, v134
v_add_f32 v135, 1.0, v135
v_rcp_f32 v128, v128
v_rcp_f32 v129, v129
v_rcp_f32 v130, v130
v_rcp_f32 v131, v131
v_rcp_f32 v132, v132
v_rcp_f32 v133, v133
v_rcp_f32 v134, v134
v_rcp_f32 v135, v135
v_mul_f32 v56, v56, v128
v_mul_f32 v57, v57, v129
v_mul_f32 v58, v58, v130
v_mul_f32 v59, v59, v131
v_mul_f32 v60, v60, v132
v_mul_f32 v61, v61, v133
v_mul_f32 v62, v62, v134
v_mul_f32 v63, v63, v135
v_cvt_pk_bf16_f32 v50, v56, v57
v_cvt_pk_bf16_f32 v51, v58, v59
v_cvt_pk_bf16_f32 v54, v60, v61
v_cvt_pk_bf16_f32 v55, v62, v63
buffer_store_dwordx4 v[48:51], v1, s[52:55], s73 offen offset:3072 sc1
buffer_store_dwordx4 v[52:55], v1, s[52:55], s66 offen offset:3072 sc1
s_branch L_m2%=
L_skip%=:
s_waitcnt vmcnt(0)
s_barrier
v_mov_b32 v20, s33
v_mov_b32 v21, 0
s_lshl_b32 s79, s38, 7
s_add_u32 s79, s79, 131072
s_mov_b64 exec, s[34:35]
buffer_store_dword v20, v21, s[40:43], s79 offen sc1
s_mov_b64 exec, -1
s_lshr_b32 s62, s38, 5
s_mul_i32 s62, s62, 56
s_mul_i32 s63, s39, 14
s_add_u32 s62, s62, s63
s_lshl_b32 s62, s62, 12
s_add_u32 s62, s62, 79691776
s_add_u32 s48, s30, s62
s_addc_u32 s49, s31, 0
s_and_b32 s49, s49, 0xffff
s_mov_b32 s50, 57344
s_mov_b32 s51, 0x20000
s_mov_b32 s63, 0
buffer_load_dwordx4 a[128:131], v1, s[48:51], s63 offen offset:0 sc1
buffer_load_dwordx4 a[132:135], v1, s[48:51], s63 offen offset:1024 sc1
buffer_load_dwordx4 a[136:139], v1, s[48:51], s63 offen offset:2048 sc1
buffer_load_dwordx4 a[140:143], v1, s[48:51], s63 offen offset:3072 sc1
s_mov_b32 s63, 4096
buffer_load_dwordx4 a[144:147], v1, s[48:51], s63 offen offset:0 sc1
buffer_load_dwordx4 a[148:151], v1, s[48:51], s63 offen offset:1024 sc1
buffer_load_dwordx4 a[152:155], v1, s[48:51], s63 offen offset:2048 sc1
buffer_load_dwordx4 a[156:159], v1, s[48:51], s63 offen offset:3072 sc1
s_mov_b32 s63, 8192
buffer_load_dwordx4 a[160:163], v1, s[48:51], s63 offen offset:0 sc1
buffer_load_dwordx4 a[164:167], v1, s[48:51], s63 offen offset:1024 sc1
buffer_load_dwordx4 a[168:171], v1, s[48:51], s63 offen offset:2048 sc1
buffer_load_dwordx4 a[172:175], v1, s[48:51], s63 offen offset:3072 sc1
s_mov_b32 s63, 12288
buffer_load_dwordx4 a[176:179], v1, s[48:51], s63 offen offset:0 sc1
buffer_load_dwordx4 a[180:183], v1, s[48:51], s63 offen offset:1024 sc1
buffer_load_dwordx4 a[184:187], v1, s[48:51], s63 offen offset:2048 sc1
buffer_load_dwordx4 a[188:191], v1, s[48:51], s63 offen offset:3072 sc1
s_mov_b32 s63, 16384
buffer_load_dwordx4 a[208:211], v1, s[48:51], s63 offen offset:0 sc1
buffer_load_dwordx4 a[212:215], v1, s[48:51], s63 offen offset:1024 sc1
buffer_load_dwordx4 a[216:219], v1, s[48:51], s63 offen offset:2048 sc1
buffer_load_dwordx4 a[220:223], v1, s[48:51], s63 offen offset:3072 sc1
s_mov_b32 s63, 20480
buffer_load_dwordx4 a[224:227], v1, s[48:51], s63 offen offset:0 sc1
buffer_load_dwordx4 a[228:231], v1, s[48:51], s63 offen offset:1024 sc1
buffer_load_dwordx4 a[232:235], v1, s[48:51], s63 offen offset:2048 sc1
buffer_load_dwordx4 a[236:239], v1, s[48:51], s63 offen offset:3072 sc1
s_mov_b32 s63, 24576
buffer_load_dwordx4 a[240:243], v1, s[48:51], s63 offen offset:0 sc1
buffer_load_dwordx4 a[244:247], v1, s[48:51], s63 offen offset:1024 sc1
buffer_load_dwordx4 a[248:251], v1, s[48:51], s63 offen offset:2048 sc1
buffer_load_dwordx4 a[252:255], v1, s[48:51], s63 offen offset:3072 sc1
s_mov_b32 s63, 28672
buffer_load_dwordx4 v[144:147], v1, s[48:51], s63 offen offset:0 sc1
buffer_load_dwordx4 v[148:151], v1, s[48:51], s63 offen offset:1024 sc1
buffer_load_dwordx4 v[152:155], v1, s[48:51], s63 offen offset:2048 sc1
buffer_load_dwordx4 v[156:159], v1, s[48:51], s63 offen offset:3072 sc1
s_mov_b32 s63, 32768
buffer_load_dwordx4 v[160:163], v1, s[48:51], s63 offen offset:0 sc1
buffer_load_dwordx4 v[164:167], v1, s[48:51], s63 offen offset:1024 sc1
buffer_load_dwordx4 v[168:171], v1, s[48:51], s63 offen offset:2048 sc1
buffer_load_dwordx4 v[172:175], v1, s[48:51], s63 offen offset:3072 sc1
s_mov_b32 s63, 36864
buffer_load_dwordx4 v[192:195], v1, s[48:51], s63 offen offset:0 sc1
buffer_load_dwordx4 v[196:199], v1, s[48:51], s63 offen offset:1024 sc1
buffer_load_dwordx4 v[200:203], v1, s[48:51], s63 offen offset:2048 sc1
buffer_load_dwordx4 v[204:207], v1, s[48:51], s63 offen offset:3072 sc1
s_mov_b32 s63, 40960
buffer_load_dwordx4 v[208:211], v1, s[48:51], s63 offen offset:0 sc1
buffer_load_dwordx4 v[212:215], v1, s[48:51], s63 offen offset:1024 sc1
buffer_load_dwordx4 v[216:219], v1, s[48:51], s63 offen offset:2048 sc1
buffer_load_dwordx4 v[220:223], v1, s[48:51], s63 offen offset:3072 sc1
s_mov_b32 s63, 45056
buffer_load_dwordx4 v[224:227], v1, s[48:51], s63 offen offset:0 sc1
buffer_load_dwordx4 v[228:231], v1, s[48:51], s63 offen offset:1024 sc1
buffer_load_dwordx4 v[232:235], v1, s[48:51], s63 offen offset:2048 sc1
buffer_load_dwordx4 v[236:239], v1, s[48:51], s63 offen offset:3072 sc1
L_m2%=:
s_mov_b32 s63, 49152
buffer_load_dwordx4 a[192:195], v1, s[48:51], s63 offen offset:0 sc1
buffer_load_dwordx4 a[196:199], v1, s[48:51], s63 offen offset:1024 sc1
buffer_load_dwordx4 a[200:203], v1, s[48:51], s63 offen offset:2048 sc1
buffer_load_dwordx4 a[204:207], v1, s[48:51], s63 offen offset:3072 sc1
s_mov_b32 s63, 53248
buffer_load_dwordx4 v[128:131], v1, s[48:51], s63 offen offset:0 sc1
buffer_load_dwordx4 v[132:135], v1, s[48:51], s63 offen offset:1024 sc1
buffer_load_dwordx4 v[136:139], v1, s[48:51], s63 offen offset:2048 sc1
buffer_load_dwordx4 v[140:143], v1, s[48:51], s63 offen offset:3072 sc1
s_waitcnt vmcnt(8) lgkmcnt(0)
s_barrier
v_mov_b32 v20, s33
v_mov_b32 v21, 0
s_lshl_b32 s79, s38, 7
s_add_u32 s79, s79, 163840
s_mov_b64 exec, s[34:35]
buffer_store_dword v20, v21, s[40:43], s79 offen sc1
s_mov_b64 exec, -1
s_and_b32 s62, s38, 7
s_lshr_b32 s63, s38, 3
s_lshr_b32 s65, s63, 2
s_and_b32 s63, s63, 3
s_lshl_b32 s62, s62, 2
s_add_u32 s64, s62, s63
s_lshl_b32 s67, s39, 12
s_lshl_b32 s68, s39, 15
s_mul_i32 s69, s39, 14336
s_add_u32 s52, s30, 59768832
s_addc_u32 s53, s31, 0
s_and_b32 s53, s53, 0xffff
s_mov_b32 s54, 7340032
s_mov_b32 s55, 0x20000
s_lshl_b32 s62, s64, 2
s_mul_i32 s62, s62, 57344
s_add_u32 s63, s39, 0
s_and_b32 s63, s63, 3
s_mul_i32 s63, s63, 57344
s_add_u32 s63, s63, s62
v_add_u32 v2, s63, v1
s_add_u32 s63, s39, 1
s_and_b32 s63, s63, 3
s_mul_i32 s63, s63, 57344
s_add_u32 s63, s63, s62
v_add_u32 v3, s63, v1
s_add_u32 s63, s39, 2
s_and_b32 s63, s63, 3
s_mul_i32 s63, s63, 57344
s_add_u32 s63, s63, s62
v_add_u32 v4, s63, v1
s_add_u32 s63, s39, 3
s_and_b32 s63, s63, 3
s_mul_i32 s63, s63, 57344
s_add_u32 s63, s63, s62
v_add_u32 v5, s63, v1
s_lshr_b32 s62, s64, 1
s_and_b32 s63, s62, 3
s_mul_i32 s63, s63, 56
s_lshr_b32 s62, s62, 2
s_lshl_b32 s62, s62, 1
s_add_u32 s62, s62, s63
v_lshrrev_b32 v10, 4, v1
v_cmp_lt_u32 vcc, 6, v10
s_nop 1
v_cndmask_b32 v11, 0, 1, vcc
v_mul_u32_u24 v11, 55, v11
v_lshlrev_b32 v10, 3, v10
v_sub_u32 v10, v10, v11
v_add_u32 v10, s62, v10
v_lshlrev_b32 v10, 7, v10
s_cmp_eq_u32 s39, 0
s_mov_b32 s74, 0x3fff
s_cselect_b32 s74, s74, 0
s_mov_b32 s75, 0x0
s_cselect_b32 s75, s75, 0
s_mov_b64 exec, s[74:75]
s_mov_b32 s79, 163840
buffer_load_dword v11, v10, s[40:43], s79 offen sc1
s_mov_b64 exec, -1
s_waitcnt vmcnt(0)
s_mov_b64 vcc, 0
s_mov_b64 exec, s[74:75]
v_cmp_ne_u32 vcc, s33, v11
s_mov_b64 exec, -1
s_nop 4
s_cbranch_vccnz L_slow5%=
L_back5%=:
s_barrier
s_add_u32 s70, s69, 0
buffer_load_dwordx4 a[0:3], v2, s[52:55], s70 offen offset:0 sc1
buffer_load_dwordx4 a[4:7], v3, s[52:55], s70 offen offset:0 sc1
buffer_load_dwordx4 a[8:11], v4, s[52:55], s70 offen offset:0 sc1
buffer_load_dwordx4 a[12:15], v5, s[52:55], s70 offen offset:0 sc1
s_add_u32 s70, s69, 0
buffer_load_dwordx4 a[16:19], v2, s[52:55], s70 offen offset:1024 sc1
buffer_load_dwordx4 a[20:23], v3, s[52:55], s70 offen offset:1024 sc1
buffer_load_dwordx4 a[24:27], v4, s[52:55], s70 offen offset:1024 sc1
buffer_load_dwordx4 a[28:31], v5, s[52:55], s70 offen offset:1024 sc1
s_add_u32 s70, s69, 0
buffer_load_dwordx4 a[32:35], v2, s[52:55], s70 offen offset:2048 sc1
buffer_load_dwordx4 a[36:39], v3, s[52:55], s70 offen offset:2048 sc1
buffer_load_dwordx4 a[40:43], v4, s[52:55], s70 offen offset:2048 sc1
buffer_load_dwordx4 a[44:47], v5, s[52:55], s70 offen offset:2048 sc1
s_add_u32 s70, s69, 0
buffer_load_dwordx4 a[48:51], v2, s[52:55], s70 offen offset:3072 sc1
buffer_load_dwordx4 a[52:55], v3, s[52:55], s70 offen offset:3072 sc1
buffer_load_dwordx4 a[56:59], v4, s[52:55], s70 offen offset:3072 sc1
buffer_load_dwordx4 a[60:63], v5, s[52:55], s70 offen offset:3072 sc1
s_add_u32 s70, s69, 4096
buffer_load_dwordx4 a[64:67], v2, s[52:55], s70 offen offset:0 sc1
buffer_load_dwordx4 a[68:71], v3, s[52:55], s70 offen offset:0 sc1
)ASM"
R"ASM(buffer_load_dwordx4 a[72:75], v4, s[52:55], s70 offen offset:0 sc1
buffer_load_dwordx4 a[76:79], v5, s[52:55], s70 offen offset:0 sc1
s_add_u32 s70, s69, 4096
buffer_load_dwordx4 a[80:83], v2, s[52:55], s70 offen offset:1024 sc1
buffer_load_dwordx4 a[84:87], v3, s[52:55], s70 offen offset:1024 sc1
buffer_load_dwordx4 a[88:91], v4, s[52:55], s70 offen offset:1024 sc1
buffer_load_dwordx4 a[92:95], v5, s[52:55], s70 offen offset:1024 sc1
s_add_u32 s70, s69, 4096
buffer_load_dwordx4 a[96:99], v2, s[52:55], s70 offen offset:2048 sc1
buffer_load_dwordx4 a[100:103], v3, s[52:55], s70 offen offset:2048 sc1
buffer_load_dwordx4 a[104:107], v4, s[52:55], s70 offen offset:2048 sc1
buffer_load_dwordx4 a[108:111], v5, s[52:55], s70 offen offset:2048 sc1
v_lshrrev_b32 v27, 4, v1
s_lshl_b32 s71, s64, 2
s_add_u32 s71, s71, s39
s_add_u32 s44, s30, 52428800
s_addc_u32 s45, s31, 0
s_and_b32 s45, s45, 0xffff
s_mov_b32 s46, 12288
s_mov_b32 s47, 0x20000
v_lshrrev_b32 v24, 4, v27
v_lshlrev_b32 v24, 4, v24
s_lshl_b32 s63, s65, 8
v_add_u32 v24, s63, v24
s_mov_b32 s72, 10240
s_waitcnt vmcnt(24)
v_mfma_f32_16x16x32_bf16 v[64:67], a[128:131], a[0:3], 0
s_add_u32 s70, s69, 4096
buffer_load_dwordx4 a[112:115], v2, s[52:55], s70 offen offset:3072 sc1
v_mfma_f32_16x16x32_bf16 v[80:83], a[128:131], a[4:7], 0
buffer_load_dwordx4 a[116:119], v3, s[52:55], s70 offen offset:3072 sc1
v_mfma_f32_16x16x32_bf16 v[96:99], a[128:131], a[8:11], 0
buffer_load_dwordx4 a[120:123], v4, s[52:55], s70 offen offset:3072 sc1
v_mfma_f32_16x16x32_bf16 v[112:115], a[128:131], a[12:15], 0
buffer_load_dwordx4 a[124:127], v5, s[52:55], s70 offen offset:3072 sc1
v_mfma_f32_16x16x32_bf16 v[68:71], a[132:135], a[0:3], 0
v_mfma_f32_16x16x32_bf16 v[84:87], a[132:135], a[4:7], 0
v_mfma_f32_16x16x32_bf16 v[100:103], a[132:135], a[8:11], 0
v_mfma_f32_16x16x32_bf16 v[116:119], a[132:135], a[12:15], 0
v_mfma_f32_16x16x32_bf16 v[72:75], a[136:139], a[0:3], 0
v_mfma_f32_16x16x32_bf16 v[88:91], a[136:139], a[4:7], 0
v_mfma_f32_16x16x32_bf16 v[104:107], a[136:139], a[8:11], 0
v_mfma_f32_16x16x32_bf16 v[120:123], a[136:139], a[12:15], 0
v_mfma_f32_16x16x32_bf16 v[76:79], a[140:143], a[0:3], 0
v_mfma_f32_16x16x32_bf16 v[92:95], a[140:143], a[4:7], 0
v_mfma_f32_16x16x32_bf16 v[108:111], a[140:143], a[8:11], 0
v_mfma_f32_16x16x32_bf16 v[124:127], a[140:143], a[12:15], 0
s_waitcnt vmcnt(24)
v_mfma_f32_16x16x32_bf16 v[64:67], a[144:147], a[16:19], v[64:67]
s_add_u32 s70, s69, 8192
buffer_load_dwordx4 a[0:3], v2, s[52:55], s70 offen offset:0 sc1
v_mfma_f32_16x16x32_bf16 v[80:83], a[144:147], a[20:23], v[80:83]
buffer_load_dwordx4 a[4:7], v3, s[52:55], s70 offen offset:0 sc1
v_mfma_f32_16x16x32_bf16 v[96:99], a[144:147], a[24:27], v[96:99]
buffer_load_dwordx4 a[8:11], v4, s[52:55], s70 offen offset:0 sc1
v_mfma_f32_16x16x32_bf16 v[112:115], a[144:147], a[28:31], v[112:115]
buffer_load_dwordx4 a[12:15], v5, s[52:55], s70 offen offset:0 sc1
v_mfma_f32_16x16x32_bf16 v[68:71], a[148:151], a[16:19], v[68:71]
v_mfma_f32_16x16x32_bf16 v[84:87], a[148:151], a[20:23], v[84:87]
v_mfma_f32_16x16x32_bf16 v[100:103], a[148:151], a[24:27], v[100:103]
v_mfma_f32_16x16x32_bf16 v[116:119], a[148:151], a[28:31], v[116:119]
v_mfma_f32_16x16x32_bf16 v[72:75], a[152:155], a[16:19], v[72:75]
v_mfma_f32_16x16x32_bf16 v[88:91], a[152:155], a[20:23], v[88:91]
v_mfma_f32_16x16x32_bf16 v[104:107], a[152:155], a[24:27], v[104:107]
v_mfma_f32_16x16x32_bf16 v[120:123], a[152:155], a[28:31], v[120:123]
v_mfma_f32_16x16x32_bf16 v[76:79], a[156:159], a[16:19], v[76:79]
v_mfma_f32_16x16x32_bf16 v[92:95], a[156:159], a[20:23], v[92:95]
v_mfma_f32_16x16x32_bf16 v[108:111], a[156:159], a[24:27], v[108:111]
v_mfma_f32_16x16x32_bf16 v[124:127], a[156:159], a[28:31], v[124:127]
s_waitcnt vmcnt(24)
v_mfma_f32_16x16x32_bf16 v[64:67], a[160:163], a[32:35], v[64:67]
s_add_u32 s70, s69, 8192
buffer_load_dwordx4 a[16:19], v2, s[52:55], s70 offen offset:1024 sc1
v_mfma_f32_16x16x32_bf16 v[80:83], a[160:163], a[36:39], v[80:83]
buffer_load_dwordx4 a[20:23], v3, s[52:55], s70 offen offset:1024 sc1
v_mfma_f32_16x16x32_bf16 v[96:99], a[160:163], a[40:43], v[96:99]
buffer_load_dwordx4 a[24:27], v4, s[52:55], s70 offen offset:1024 sc1
v_mfma_f32_16x16x32_bf16 v[112:115], a[160:163], a[44:47], v[112:115]
buffer_load_dwordx4 a[28:31], v5, s[52:55], s70 offen offset:1024 sc1
v_mfma_f32_16x16x32_bf16 v[68:71], a[164:167], a[32:35], v[68:71]
v_mfma_f32_16x16x32_bf16 v[84:87], a[164:167], a[36:39], v[84:87]
v_mfma_f32_16x16x32_bf16 v[100:103], a[164:167], a[40:43], v[100:103]
v_mfma_f32_16x16x32_bf16 v[116:119], a[164:167], a[44:47], v[116:119]
v_mfma_f32_16x16x32_bf16 v[72:75], a[168:171], a[32:35], v[72:75]
v_mfma_f32_16x16x32_bf16 v[88:91], a[168:171], a[36:39], v[88:91]
v_mfma_f32_16x16x32_bf16 v[104:107], a[168:171], a[40:43], v[104:107]
v_mfma_f32_16x16x32_bf16 v[120:123], a[168:171], a[44:47], v[120:123]
v_mfma_f32_16x16x32_bf16 v[76:79], a[172:175], a[32:35], v[76:79]
v_mfma_f32_16x16x32_bf16 v[92:95], a[172:175], a[36:39], v[92:95]
v_mfma_f32_16x16x32_bf16 v[108:111], a[172:175], a[40:43], v[108:111]
v_mfma_f32_16x16x32_bf16 v[124:127], a[172:175], a[44:47], v[124:127]
s_waitcnt vmcnt(24)
v_mfma_f32_16x16x32_bf16 v[64:67], a[176:179], a[48:51], v[64:67]
s_add_u32 s70, s69, 8192
buffer_load_dwordx4 a[32:35], v2, s[52:55], s70 offen offset:2048 sc1
v_mfma_f32_16x16x32_bf16 v[80:83], a[176:179], a[52:55], v[80:83]
buffer_load_dwordx4 a[36:39], v3, s[52:55], s70 offen offset:2048 sc1
v_mfma_f32_16x16x32_bf16 v[96:99], a[176:179], a[56:59], v[96:99]
buffer_load_dwordx4 a[40:43], v4, s[52:55], s70 offen offset:2048 sc1
v_mfma_f32_16x16x32_bf16 v[112:115], a[176:179], a[60:63], v[112:115]
buffer_load_dwordx4 a[44:47], v5, s[52:55], s70 offen offset:2048 sc1
v_mfma_f32_16x16x32_bf16 v[68:71], a[180:183], a[48:51], v[68:71]
v_mfma_f32_16x16x32_bf16 v[84:87], a[180:183], a[52:55], v[84:87]
v_mfma_f32_16x16x32_bf16 v[100:103], a[180:183], a[56:59], v[100:103]
v_mfma_f32_16x16x32_bf16 v[116:119], a[180:183], a[60:63], v[116:119]
v_mfma_f32_16x16x32_bf16 v[72:75], a[184:187], a[48:51], v[72:75]
v_mfma_f32_16x16x32_bf16 v[88:91], a[184:187], a[52:55], v[88:91]
v_mfma_f32_16x16x32_bf16 v[104:107], a[184:187], a[56:59], v[104:107]
v_mfma_f32_16x16x32_bf16 v[120:123], a[184:187], a[60:63], v[120:123]
v_mfma_f32_16x16x32_bf16 v[76:79], a[188:191], a[48:51], v[76:79]
v_mfma_f32_16x16x32_bf16 v[92:95], a[188:191], a[52:55], v[92:95]
v_mfma_f32_16x16x32_bf16 v[108:111], a[188:191], a[56:59], v[108:111]
v_mfma_f32_16x16x32_bf16 v[124:127], a[188:191], a[60:63], v[124:127]
s_waitcnt vmcnt(24)
v_mfma_f32_16x16x32_bf16 v[64:67], a[208:211], a[64:67], v[64:67]
s_add_u32 s70, s69, 8192
buffer_load_dwordx4 a[48:51], v2, s[52:55], s70 offen offset:3072 sc1
v_mfma_f32_16x16x32_bf16 v[80:83], a[208:211], a[68:71], v[80:83]
buffer_load_dwordx4 a[52:55], v3, s[52:55], s70 offen offset:3072 sc1
v_mfma_f32_16x16x32_bf16 v[96:99], a[208:211], a[72:75], v[96:99]
buffer_load_dwordx4 a[56:59], v4, s[52:55], s70 offen offset:3072 sc1
v_mfma_f32_16x16x32_bf16 v[112:115], a[208:211], a[76:79], v[112:115]
buffer_load_dwordx4 a[60:63], v5, s[52:55], s70 offen offset:3072 sc1
v_mfma_f32_16x16x32_bf16 v[68:71], a[212:215], a[64:67], v[68:71]
v_mfma_f32_16x16x32_bf16 v[84:87], a[212:215], a[68:71], v[84:87]
v_mfma_f32_16x16x32_bf16 v[100:103], a[212:215], a[72:75], v[100:103]
v_mfma_f32_16x16x32_bf16 v[116:119], a[212:215], a[76:79], v[116:119]
v_mfma_f32_16x16x32_bf16 v[72:75], a[216:219], a[64:67], v[72:75]
v_mfma_f32_16x16x32_bf16 v[88:91], a[216:219], a[68:71], v[88:91]
v_mfma_f32_16x16x32_bf16 v[104:107], a[216:219], a[72:75], v[104:107]
v_mfma_f32_16x16x32_bf16 v[120:123], a[216:219], a[76:79], v[120:123]
v_mfma_f32_16x16x32_bf16 v[76:79], a[220:223], a[64:67], v[76:79]
v_mfma_f32_16x16x32_bf16 v[92:95], a[220:223], a[68:71], v[92:95]
v_mfma_f32_16x16x32_bf16 v[108:111], a[220:223], a[72:75], v[108:111]
v_mfma_f32_16x16x32_bf16 v[124:127], a[220:223], a[76:79], v[124:127]
s_waitcnt vmcnt(24)
v_mfma_f32_16x16x32_bf16 v[64:67], a[224:227], a[80:83], v[64:67]
s_add_u32 s70, s69, 12288
buffer_load_dwordx4 a[64:67], v2, s[52:55], s70 offen offset:0 sc1
v_mfma_f32_16x16x32_bf16 v[80:83], a[224:227], a[84:87], v[80:83]
buffer_load_dwordx4 a[68:71], v3, s[52:55], s70 offen offset:0 sc1
v_mfma_f32_16x16x32_bf16 v[96:99], a[224:227], a[88:91], v[96:99]
buffer_load_dwordx4 a[72:75], v4, s[52:55], s70 offen offset:0 sc1
v_mfma_f32_16x16x32_bf16 v[112:115], a[224:227], a[92:95], v[112:115]
buffer_load_dwordx4 a[76:79], v5, s[52:55], s70 offen offset:0 sc1
v_mfma_f32_16x16x32_bf16 v[68:71], a[228:231], a[80:83], v[68:71]
v_mfma_f32_16x16x32_bf16 v[84:87], a[228:231], a[84:87], v[84:87]
v_mfma_f32_16x16x32_bf16 v[100:103], a[228:231], a[88:91], v[100:103]
v_mfma_f32_16x16x32_bf16 v[116:119], a[228:231], a[92:95], v[116:119]
v_mfma_f32_16x16x32_bf16 v[72:75], a[232:235], a[80:83], v[72:75]
v_mfma_f32_16x16x32_bf16 v[88:91], a[232:235], a[84:87], v[88:91]
v_mfma_f32_16x16x32_bf16 v[104:107], a[232:235], a[88:91], v[104:107]
v_mfma_f32_16x16x32_bf16 v[120:123], a[232:235], a[92:95], v[120:123]
v_mfma_f32_16x16x32_bf16 v[76:79], a[236:239], a[80:83], v[76:79]
v_mfma_f32_16x16x32_bf16 v[92:95], a[236:239], a[84:87], v[92:95]
v_mfma_f32_16x16x32_bf16 v[108:111], a[236:239], a[88:91], v[108:111]
v_mfma_f32_16x16x32_bf16 v[124:127], a[236:239], a[92:95], v[124:127]
s_waitcnt vmcnt(24)
v_mfma_f32_16x16x32_bf16 v[64:67], a[240:243], a[96:99], v[64:67]
s_add_u32 s70, s69, 12288
buffer_load_dwordx4 a[80:83], v2, s[52:55], s70 offen offset:1024 sc1
v_mfma_f32_16x16x32_bf16 v[80:83], a[240:243], a[100:103], v[80:83]
buffer_load_dwordx4 a[84:87], v3, s[52:55], s70 offen offset:1024 sc1
v_mfma_f32_16x16x32_bf16 v[96:99], a[240:243], a[104:107], v[96:99]
buffer_load_dwordx4 a[88:91], v4, s[52:55], s70 offen offset:1024 sc1
v_mfma_f32_16x16x32_bf16 v[112:115], a[240:243], a[108:111], v[112:115]
buffer_load_dwordx4 a[92:95], v5, s[52:55], s70 offen offset:1024 sc1
v_mfma_f32_16x16x32_bf16 v[68:71], a[244:247], a[96:99], v[68:71]
v_mfma_f32_16x16x32_bf16 v[84:87], a[244:247], a[100:103], v[84:87]
v_mfma_f32_16x16x32_bf16 v[100:103], a[244:247], a[104:107], v[100:103]
v_mfma_f32_16x16x32_bf16 v[116:119], a[244:247], a[108:111], v[116:119]
v_mfma_f32_16x16x32_bf16 v[72:75], a[248:251], a[96:99], v[72:75]
v_mfma_f32_16x16x32_bf16 v[88:91], a[248:251], a[100:103], v[88:91]
v_mfma_f32_16x16x32_bf16 v[104:107], a[248:251], a[104:107], v[104:107]
v_mfma_f32_16x16x32_bf16 v[120:123], a[248:251], a[108:111], v[120:123]
v_mfma_f32_16x16x32_bf16 v[76:79], a[252:255], a[96:99], v[76:79]
v_mfma_f32_16x16x32_bf16 v[92:95], a[252:255], a[100:103], v[92:95]
v_mfma_f32_16x16x32_bf16 v[108:111], a[252:255], a[104:107], v[108:111]
v_mfma_f32_16x16x32_bf16 v[124:127], a[252:255], a[108:111], v[124:127]
s_waitcnt vmcnt(24)
v_mfma_f32_16x16x32_bf16 v[64:67], v[144:147], a[112:115], v[64:67]
buffer_load_dwordx4 v[176:179], v24, s[44:47], s72 offen offset:0 sc1
v_mfma_f32_16x16x32_bf16 v[80:83], v[144:147], a[116:119], v[80:83]
buffer_load_dwordx4 v[180:183], v24, s[44:47], s72 offen offset:64 sc1
v_mfma_f32_16x16x32_bf16 v[96:99], v[144:147], a[120:123], v[96:99]
buffer_load_dwordx4 v[184:187], v24, s[44:47], s72 offen offset:128 sc1
v_mfma_f32_16x16x32_bf16 v[112:115], v[144:147], a[124:127], v[112:115]
buffer_load_dwordx4 v[188:191], v24, s[44:47], s72 offen offset:192 sc1
v_mfma_f32_16x16x32_bf16 v[68:71], v[148:151], a[112:115], v[68:71]
v_mfma_f32_16x16x32_bf16 v[84:87], v[148:151], a[116:119], v[84:87]
v_mfma_f32_16x16x32_bf16 v[100:103], v[148:151], a[120:123], v[100:103]
v_mfma_f32_16x16x32_bf16 v[116:119], v[148:151], a[124:127], v[116:119]
v_mfma_f32_16x16x32_bf16 v[72:75], v[152:155], a[112:115], v[72:75]
v_mfma_f32_16x16x32_bf16 v[88:91], v[152:155], a[116:119], v[88:91]
v_mfma_f32_16x16x32_bf16 v[104:107], v[152:155], a[120:123], v[104:107]
v_mfma_f32_16x16x32_bf16 v[120:123], v[152:155], a[124:127], v[120:123]
v_mfma_f32_16x16x32_bf16 v[76:79], v[156:159], a[112:115], v[76:79]
v_mfma_f32_16x16x32_bf16 v[92:95], v[156:159], a[116:119], v[92:95]
v_mfma_f32_16x16x32_bf16 v[108:111], v[156:159], a[120:123], v[108:111]
v_mfma_f32_16x16x32_bf16 v[124:127], v[156:159], a[124:127], v[124:127]
s_waitcnt vmcnt(24)
v_mfma_f32_16x16x32_bf16 v[64:67], v[160:163], a[0:3], v[64:67]
v_mfma_f32_16x16x32_bf16 v[80:83], v[160:163], a[4:7], v[80:83]
v_mfma_f32_16x16x32_bf16 v[96:99], v[160:163], a[8:11], v[96:99]
v_mfma_f32_16x16x32_bf16 v[112:115], v[160:163], a[12:15], v[112:115]
v_mfma_f32_16x16x32_bf16 v[68:71], v[164:167], a[0:3], v[68:71]
v_mfma_f32_16x16x32_bf16 v[84:87], v[164:167], a[4:7], v[84:87]
v_mfma_f32_16x16x32_bf16 v[100:103], v[164:167], a[8:11], v[100:103]
v_mfma_f32_16x16x32_bf16 v[116:119], v[164:167], a[12:15], v[116:119]
v_mfma_f32_16x16x32_bf16 v[72:75], v[168:171], a[0:3], v[72:75]
v_mfma_f32_16x16x32_bf16 v[88:91], v[168:171], a[4:7], v[88:91]
v_mfma_f32_16x16x32_bf16 v[104:107], v[168:171], a[8:11], v[104:107]
v_mfma_f32_16x16x32_bf16 v[120:123], v[168:171], a[12:15], v[120:123]
v_mfma_f32_16x16x32_bf16 v[76:79], v[172:175], a[0:3], v[76:79]
v_mfma_f32_16x16x32_bf16 v[92:95], v[172:175], a[4:7], v[92:95]
v_mfma_f32_16x16x32_bf16 v[108:111], v[172:175], a[8:11], v[108:111]
v_mfma_f32_16x16x32_bf16 v[124:127], v[172:175], a[12:15], v[124:127]
s_waitcnt vmcnt(20)
v_mfma_f32_16x16x32_bf16 v[64:67], v[192:195], a[16:19], v[64:67]
v_mfma_f32_16x16x32_bf16 v[80:83], v[192:195], a[20:23], v[80:83]
v_mfma_f32_16x16x32_bf16 v[96:99], v[192:195], a[24:27], v[96:99]
v_mfma_f32_16x16x32_bf16 v[112:115], v[192:195], a[28:31], v[112:115]
v_mfma_f32_16x16x32_bf16 v[68:71], v[196:199], a[16:19], v[68:71]
v_mfma_f32_16x16x32_bf16 v[84:87], v[196:199], a[20:23], v[84:87]
v_mfma_f32_16x16x32_bf16 v[100:103], v[196:199], a[24:27], v[100:103]
v_mfma_f32_16x16x32_bf16 v[116:119], v[196:199], a[28:31], v[116:119]
v_mfma_f32_16x16x32_bf16 v[72:75], v[200:203], a[16:19], v[72:75]
v_mfma_f32_16x16x32_bf16 v[88:91], v[200:203], a[20:23], v[88:91]
v_mfma_f32_16x16x32_bf16 v[104:107], v[200:203], a[24:27], v[104:107]
v_mfma_f32_16x16x32_bf16 v[120:123], v[200:203], a[28:31], v[120:123]
v_mfma_f32_16x16x32_bf16 v[76:79], v[204:207], a[16:19], v[76:79]
v_mfma_f32_16x16x32_bf16 v[92:95], v[204:207], a[20:23], v[92:95]
v_mfma_f32_16x16x32_bf16 v[108:111], v[204:207], a[24:27], v[108:111]
v_mfma_f32_16x16x32_bf16 v[124:127], v[204:207], a[28:31], v[124:127]
s_waitcnt vmcnt(16)
v_mfma_f32_16x16x32_bf16 v[64:67], v[208:211], a[32:35], v[64:67]
v_mfma_f32_16x16x32_bf16 v[80:83], v[208:211], a[36:39], v[80:83]
v_mfma_f32_16x16x32_bf16 v[96:99], v[208:211], a[40:43], v[96:99]
v_mfma_f32_16x16x32_bf16 v[112:115], v[208:211], a[44:47], v[112:115]
v_mfma_f32_16x16x32_bf16 v[68:71], v[212:215], a[32:35], v[68:71]
v_mfma_f32_16x16x32_bf16 v[84:87], v[212:215], a[36:39], v[84:87]
v_mfma_f32_16x16x32_bf16 v[100:103], v[212:215], a[40:43], v[100:103]
v_mfma_f32_16x16x32_bf16 v[116:119], v[212:215], a[44:47], v[116:119]
v_mfma_f32_16x16x32_bf16 v[72:75], v[216:219], a[32:35], v[72:75]
v_mfma_f32_16x16x32_bf16 v[88:91], v[216:219], a[36:39], v[88:91]
v_mfma_f32_16x16x32_bf16 v[104:107], v[216:219], a[40:43], v[104:107]
v_mfma_f32_16x16x32_bf16 v[120:123], v[216:219], a[44:47], v[120:123]
v_mfma_f32_16x16x32_bf16 v[76:79], v[220:223], a[32:35], v[76:79]
v_mfma_f32_16x16x32_bf16 v[92:95], v[220:223], a[36:39], v[92:95]
v_mfma_f32_16x16x32_bf16 v[108:111], v[220:223], a[40:43], v[108:111]
v_mfma_f32_16x16x32_bf16 v[124:127], v[220:223], a[44:47], v[124:127]
s_waitcnt vmcnt(12)
v_mfma_f32_16x16x32_bf16 v[64:67], v[224:227], a[48:51], v[64:67]
v_mfma_f32_16x16x32_bf16 v[80:83], v[224:227], a[52:55], v[80:83]
v_mfma_f32_16x16x32_bf16 v[96:99], v[224:227], a[56:59], v[96:99]
v_mfma_f32_16x16x32_bf16 v[112:115], v[224:227], a[60:63], v[112:115]
v_mfma_f32_16x16x32_bf16 v[68:71], v[228:231], a[48:51], v[68:71]
v_mfma_f32_16x16x32_bf16 v[84:87], v[228:231], a[52:55], v[84:87]
v_mfma_f32_16x16x32_bf16 v[100:103], v[228:231], a[56:59], v[100:103]
v_mfma_f32_16x16x32_bf16 v[116:119], v[228:231], a[60:63], v[116:119]
v_mfma_f32_16x16x32_bf16 v[72:75], v[232:235], a[48:51], v[72:75]
v_mfma_f32_16x16x32_bf16 v[88:91], v[232:235], a[52:55], v[88:91]
v_mfma_f32_16x16x32_bf16 v[104:107], v[232:235], a[56:59], v[104:107]
v_mfma_f32_16x16x32_bf16 v[120:123], v[232:235], a[60:63], v[120:123]
v_mfma_f32_16x16x32_bf16 v[76:79], v[236:239], a[48:51], v[76:79]
v_mfma_f32_16x16x32_bf16 v[92:95], v[236:239], a[52:55], v[92:95]
v_mfma_f32_16x16x32_bf16 v[108:111], v[236:239], a[56:59], v[108:111]
v_mfma_f32_16x16x32_bf16 v[124:127], v[236:239], a[60:63], v[124:127]
s_waitcnt vmcnt(8)
v_mfma_f32_16x16x32_bf16 v[64:67], a[192:195], a[64:67], v[64:67]
v_mfma_f32_16x16x32_bf16 v[80:83], a[192:195], a[68:71], v[80:83]
v_mfma_f32_16x16x32_bf16 v[96:99], a[192:195], a[72:75], v[96:99]
v_mfma_f32_16x16x32_bf16 v[112:115], a[192:195], a[76:79], v[112:115]
v_mfma_f32_16x16x32_bf16 v[68:71], a[196:199], a[64:67], v[68:71]
v_mfma_f32_16x16x32_bf16 v[84:87], a[196:199], a[68:71], v[84:87]
v_mfma_f32_16x16x32_bf16 v[100:103], a[196:199], a[72:75], v[100:103]
v_mfma_f32_16x16x32_bf16 v[116:119], a[196:199], a[76:79], v[116:119]
v_mfma_f32_16x16x32_bf16 v[72:75], a[200:203], a[64:67], v[72:75]
v_mfma_f32_16x16x32_bf16 v[88:91], a[200:203], a[68:71], v[88:91]
v_mfma_f32_16x16x32_bf16 v[104:107], a[200:203], a[72:75], v[104:107]
v_mfma_f32_16x16x32_bf16 v[120:123], a[200:203], a[76:79], v[120:123]
v_mfma_f32_16x16x32_bf16 v[76:79], a[204:207], a[64:67], v[76:79]
v_mfma_f32_16x16x32_bf16 v[92:95], a[204:207], a[68:71], v[92:95]
v_mfma_f32_16x16x32_bf16 v[108:111], a[204:207], a[72:75], v[108:111]
v_mfma_f32_16x16x32_bf16 v[124:127], a[204:207], a[76:79], v[124:127]
s_waitcnt vmcnt(4)
v_mfma_f32_16x16x32_bf16 v[64:67], v[128:131], a[80:83], v[64:67]
s_add_u32 s62, s39, 1
v_mfma_f32_16x16x32_bf16 v[80:83], v[128:131], a[84:87], v[80:83]
s_and_b32 s62, s62, 3
v_mfma_f32_16x16x32_bf16 v[96:99], v[128:131], a[88:91], v[96:99]
s_lshl_b32 s62, s62, 15
v_mfma_f32_16x16x32_bf16 v[112:115], v[128:131], a[92:95], v[112:115]
s_add_u32 s62, s62, s67
v_mfma_f32_16x16x32_bf16 v[68:71], v[132:135], a[80:83], v[68:71]
v_add_u32 v3, s62, v1
v_mfma_f32_16x16x32_bf16 v[84:87], v[132:135], a[84:87], v[84:87]
s_add_u32 s62, s39, 2
v_mfma_f32_16x16x32_bf16 v[100:103], v[132:135], a[88:91], v[100:103]
s_and_b32 s62, s62, 3
)ASM"
R"ASM(v_mfma_f32_16x16x32_bf16 v[116:119], v[132:135], a[92:95], v[116:119]
s_lshl_b32 s62, s62, 15
v_mfma_f32_16x16x32_bf16 v[72:75], v[136:139], a[80:83], v[72:75]
s_add_u32 s62, s62, s67
v_mfma_f32_16x16x32_bf16 v[88:91], v[136:139], a[84:87], v[88:91]
v_add_u32 v4, s62, v1
v_mfma_f32_16x16x32_bf16 v[104:107], v[136:139], a[88:91], v[104:107]
s_add_u32 s62, s39, 3
v_mfma_f32_16x16x32_bf16 v[120:123], v[136:139], a[92:95], v[120:123]
s_and_b32 s62, s62, 3
v_mfma_f32_16x16x32_bf16 v[76:79], v[140:143], a[80:83], v[76:79]
s_lshl_b32 s62, s62, 15
v_mfma_f32_16x16x32_bf16 v[92:95], v[140:143], a[84:87], v[92:95]
s_add_u32 s62, s62, s67
v_mfma_f32_16x16x32_bf16 v[108:111], v[140:143], a[88:91], v[108:111]
v_add_u32 v5, s62, v1
v_mfma_f32_16x16x32_bf16 v[124:127], v[140:143], a[92:95], v[124:127]
s_nop 7
ds_write_b128 v3, v[80:83] offset:0
ds_write_b128 v3, v[84:87] offset:1024
ds_write_b128 v3, v[88:91] offset:2048
ds_write_b128 v3, v[92:95] offset:3072
ds_write_b128 v4, v[96:99] offset:0
ds_write_b128 v4, v[100:103] offset:1024
ds_write_b128 v4, v[104:107] offset:2048
ds_write_b128 v4, v[108:111] offset:3072
ds_write_b128 v5, v[112:115] offset:0
ds_write_b128 v5, v[116:119] offset:1024
ds_write_b128 v5, v[120:123] offset:2048
ds_write_b128 v5, v[124:127] offset:3072
s_add_u32 s62, s39, 1
s_and_b32 s62, s62, 3
s_lshl_b32 s62, s62, 12
s_add_u32 s62, s62, s68
v_add_u32 v3, s62, v1
s_add_u32 s62, s39, 2
s_and_b32 s62, s62, 3
s_lshl_b32 s62, s62, 12
s_add_u32 s62, s62, s68
v_add_u32 v4, s62, v1
s_add_u32 s62, s39, 3
s_and_b32 s62, s62, 3
s_lshl_b32 s62, s62, 12
s_add_u32 s62, s62, s68
v_add_u32 v5, s62, v1
s_waitcnt vmcnt(0) lgkmcnt(0)
s_barrier
ds_read_b128 v[80:83], v3 offset:0
ds_read_b128 v[84:87], v3 offset:1024
ds_read_b128 v[88:91], v3 offset:2048
ds_read_b128 v[92:95], v3 offset:3072
ds_read_b128 v[96:99], v4 offset:0
ds_read_b128 v[100:103], v4 offset:1024
ds_read_b128 v[104:107], v4 offset:2048
ds_read_b128 v[108:111], v4 offset:3072
ds_read_b128 v[112:115], v5 offset:0
ds_read_b128 v[116:119], v5 offset:1024
ds_read_b128 v[120:123], v5 offset:2048
ds_read_b128 v[124:127], v5 offset:3072
s_waitcnt lgkmcnt(8)
v_add_f32 v64, v64, v80
v_add_f32 v65, v65, v81
v_add_f32 v66, v66, v82
v_add_f32 v67, v67, v83
v_add_f32 v68, v68, v84
v_add_f32 v69, v69, v85
v_add_f32 v70, v70, v86
v_add_f32 v71, v71, v87
v_add_f32 v72, v72, v88
v_add_f32 v73, v73, v89
v_add_f32 v74, v74, v90
v_add_f32 v75, v75, v91
v_add_f32 v76, v76, v92
v_add_f32 v77, v77, v93
v_add_f32 v78, v78, v94
v_add_f32 v79, v79, v95
s_waitcnt lgkmcnt(0)
v_add_f32 v96, v96, v112
v_add_f32 v97, v97, v113
v_add_f32 v98, v98, v114
v_add_f32 v99, v99, v115
v_add_f32 v100, v100, v116
v_add_f32 v101, v101, v117
v_add_f32 v102, v102, v118
v_add_f32 v103, v103, v119
v_add_f32 v104, v104, v120
v_add_f32 v105, v105, v121
v_add_f32 v106, v106, v122
v_add_f32 v107, v107, v123
v_add_f32 v108, v108, v124
v_add_f32 v109, v109, v125
v_add_f32 v110, v110, v126
v_add_f32 v111, v111, v127
v_add_f32 v64, v64, v96
v_add_f32 v65, v65, v97
v_add_f32 v66, v66, v98
v_add_f32 v67, v67, v99
v_add_f32 v68, v68, v100
v_add_f32 v69, v69, v101
v_add_f32 v70, v70, v102
v_add_f32 v71, v71, v103
v_add_f32 v72, v72, v104
v_add_f32 v73, v73, v105
v_add_f32 v74, v74, v106
v_add_f32 v75, v75, v107
v_add_f32 v76, v76, v108
v_add_f32 v77, v77, v109
v_add_f32 v78, v78, v110
v_add_f32 v79, v79, v111
s_waitcnt vmcnt(0)
v_fmac_f32 v240, v176, v64
v_fmac_f32 v241, v177, v65
v_fmac_f32 v242, v178, v66
v_fmac_f32 v243, v179, v67
v_fmac_f32 v244, v180, v68
v_fmac_f32 v245, v181, v69
v_fmac_f32 v246, v182, v70
v_fmac_f32 v247, v183, v71
v_fmac_f32 v248, v184, v72
v_fmac_f32 v249, v185, v73
v_fmac_f32 v250, v186, v74
v_fmac_f32 v251, v187, v75
v_fmac_f32 v252, v188, v76
v_fmac_f32 v253, v189, v77
v_fmac_f32 v254, v190, v78
v_fmac_f32 v255, v191, v79
v_cvt_pk_bf16_f32 v80, v240, v241
v_cvt_pk_bf16_f32 v81, v242, v243
v_cvt_pk_bf16_f32 v82, v244, v245
v_cvt_pk_bf16_f32 v83, v246, v247
v_cvt_pk_bf16_f32 v84, v248, v249
v_cvt_pk_bf16_f32 v85, v250, v251
v_cvt_pk_bf16_f32 v86, v252, v253
v_cvt_pk_bf16_f32 v87, v254, v255
s_mov_b32 s48, s28
s_mov_b32 s49, s29
s_and_b32 s49, s49, 0xffff
s_mov_b32 s50, 2097152
s_mov_b32 s51, 0x20000
v_lshrrev_b32 v26, 4, v1
v_and_b32 v25, 15, v26
v_lshlrev_b32 v25, 10, v25
v_lshrrev_b32 v26, 4, v26
v_lshl_add_u32 v25, v26, 3, v25
s_lshl_b32 s63, s71, 14
s_lshl_b32 s62, s65, 7
s_add_u32 s63, s63, s62
buffer_store_dwordx2 v[80:81], v25, s[48:51], s63 offen offset:0
buffer_store_dwordx2 v[82:83], v25, s[48:51], s63 offen offset:32
buffer_store_dwordx2 v[84:85], v25, s[48:51], s63 offen offset:64
buffer_store_dwordx2 v[86:87], v25, s[48:51], s63 offen offset:96
s_cmp_lg_u32 s38, 0
s_cbranch_scc1 L_noep%=
v_mov_b32 v20, s33
v_mov_b32 v21, 0
s_mov_b32 s79, 196608
s_mov_b64 exec, s[34:35]
buffer_store_dword v20, v21, s[40:43], s79 offen
s_mov_b64 exec, -1
L_noep%=:
s_waitcnt vmcnt(0)
s_branch L_end%=
L_redo%=:
s_mov_b32 s73, 1
s_waitcnt vmcnt(0) lgkmcnt(0)
s_barrier
v_mov_b32 v16, s72
v_mov_b32 v17, s72
v_mov_b32 v18, s72
v_mov_b32 v19, s72
v_lshrrev_b32 v28, 4, v1
v_and_b32 v29, 15, v28
v_lshrrev_b32 v28, 4, v28
v_lshlrev_b32 v28, 2, v28
v_sub_u32 v28, v28, v29
s_lshl_b32 s62, s61, 7
s_lshl_b32 s63, s39, 5
s_add_u32 s62, s62, s63
s_add_u32 s62, s62, 0
v_subrev_u32 v24, s62, v28
s_lshl_b32 s62, s61, 7
s_lshl_b32 s63, s39, 5
s_add_u32 s62, s62, s63
s_add_u32 s62, s62, 16
v_subrev_u32 v25, s62, v28
s_sub_u32 s62, 15, s61
s_lshl_b32 s62, s62, 7
s_lshl_b32 s63, s39, 5
s_add_u32 s62, s62, s63
s_add_u32 s62, s62, 0
v_subrev_u32 v26, s62, v28
s_sub_u32 s62, 15, s61
s_lshl_b32 s62, s62, 7
s_lshl_b32 s63, s39, 5
s_add_u32 s62, s62, s63
s_add_u32 s62, s62, 16
v_subrev_u32 v27, s62, v28
v_mov_b32 v30, s72
s_mov_b32 s67, 0
s_lshl_b32 s66, s71, 2
L_mx%=:
s_lshr_b32 s62, s67, 2
s_lshl_b32 s62, s62, 15
s_and_b32 s63, s67, 3
s_lshl_b32 s63, s63, 12
s_add_u32 s62, s62, s63
buffer_load_dwordx4 a[0:3], v1, s[44:47], s62 offen offset:0 sc1
buffer_load_dwordx4 a[4:7], v1, s[44:47], s62 offen offset:1024 sc1
buffer_load_dwordx4 a[8:11], v1, s[44:47], s62 offen offset:2048 sc1
buffer_load_dwordx4 a[12:15], v1, s[44:47], s62 offen offset:3072 sc1
s_lshl_b32 s63, s67, 4
s_waitcnt vmcnt(0)
v_mfma_f32_16x16x32_bf16 v[20:23], a[0:3], a[64:67], 0
v_mfma_f32_16x16x32_bf16 v[20:23], a[4:7], a[68:71], v[20:23]
v_mfma_f32_16x16x32_bf16 v[20:23], a[8:11], a[72:75], v[20:23]
v_mfma_f32_16x16x32_bf16 v[20:23], a[12:15], a[76:79], v[20:23]
v_add_u32 v29, s63, v24
s_nop 7
v_cmp_lt_i32 vcc, 0, v29
s_nop 1
v_cndmask_b32 v20, v20, v30, vcc
v_cmp_lt_i32 vcc, -1, v29
s_nop 1
v_cndmask_b32 v21, v21, v30, vcc
v_cmp_lt_i32 vcc, -2, v29
s_nop 1
v_cndmask_b32 v22, v22, v30, vcc
v_cmp_lt_i32 vcc, -3, v29
s_nop 1
v_cndmask_b32 v23, v23, v30, vcc
v_max3_f32 v16, v20, v21, v16
v_max3_f32 v16, v22, v23, v16
s_nop 2
v_mfma_f32_16x16x32_bf16 v[20:23], a[0:3], a[80:83], 0
v_mfma_f32_16x16x32_bf16 v[20:23], a[4:7], a[84:87], v[20:23]
v_mfma_f32_16x16x32_bf16 v[20:23], a[8:11], a[88:91], v[20:23]
v_mfma_f32_16x16x32_bf16 v[20:23], a[12:15], a[92:95], v[20:23]
v_add_u32 v29, s63, v25
s_nop 7
v_cmp_lt_i32 vcc, 0, v29
s_nop 1
v_cndmask_b32 v20, v20, v30, vcc
v_cmp_lt_i32 vcc, -1, v29
s_nop 1
v_cndmask_b32 v21, v21, v30, vcc
v_cmp_lt_i32 vcc, -2, v29
s_nop 1
v_cndmask_b32 v22, v22, v30, vcc
v_cmp_lt_i32 vcc, -3, v29
s_nop 1
v_cndmask_b32 v23, v23, v30, vcc
v_max3_f32 v17, v20, v21, v17
v_max3_f32 v17, v22, v23, v17
s_nop 2
v_mfma_f32_16x16x32_bf16 v[20:23], a[0:3], a[96:99], 0
v_mfma_f32_16x16x32_bf16 v[20:23], a[4:7], a[100:103], v[20:23]
v_mfma_f32_16x16x32_bf16 v[20:23], a[8:11], a[104:107], v[20:23]
v_mfma_f32_16x16x32_bf16 v[20:23], a[12:15], a[108:111], v[20:23]
v_add_u32 v29, s63, v26
s_nop 7
v_cmp_lt_i32 vcc, 0, v29
s_nop 1
v_cndmask_b32 v20, v20, v30, vcc
v_cmp_lt_i32 vcc, -1, v29
s_nop 1
v_cndmask_b32 v21, v21, v30, vcc
v_cmp_lt_i32 vcc, -2, v29
s_nop 1
v_cndmask_b32 v22, v22, v30, vcc
v_cmp_lt_i32 vcc, -3, v29
s_nop 1
v_cndmask_b32 v23, v23, v30, vcc
v_max3_f32 v18, v20, v21, v18
v_max3_f32 v18, v22, v23, v18
s_nop 2
v_mfma_f32_16x16x32_bf16 v[20:23], a[0:3], a[112:115], 0
v_mfma_f32_16x16x32_bf16 v[20:23], a[4:7], a[116:119], v[20:23]
v_mfma_f32_16x16x32_bf16 v[20:23], a[8:11], a[120:123], v[20:23]
v_mfma_f32_16x16x32_bf16 v[20:23], a[12:15], a[124:127], v[20:23]
v_add_u32 v29, s63, v27
s_nop 7
v_cmp_lt_i32 vcc, 0, v29
s_nop 1
v_cndmask_b32 v20, v20, v30, vcc
v_cmp_lt_i32 vcc, -1, v29
s_nop 1
v_cndmask_b32 v21, v21, v30, vcc
v_cmp_lt_i32 vcc, -2, v29
s_nop 1
v_cndmask_b32 v22, v22, v30, vcc
v_cmp_lt_i32 vcc, -3, v29
s_nop 1
v_cndmask_b32 v23, v23, v30, vcc
v_max3_f32 v19, v20, v21, v19
v_max3_f32 v19, v22, v23, v19
s_nop 2
s_add_u32 s67, s67, 1
s_cmp_lt_u32 s67, s66
s_cbranch_scc1 L_mx%=
v_mov_b32 v20, v16
)ASM"
R"ASM(v_mov_b32 v21, v17
v_mov_b32 v22, v18
v_mov_b32 v23, v19
s_nop 1
v_permlane32_swap_b32 v16, v20
v_permlane32_swap_b32 v17, v21
v_permlane32_swap_b32 v18, v22
v_permlane32_swap_b32 v19, v23
s_nop 0
v_max_f32 v16, v16, v20
v_max_f32 v17, v17, v21
v_max_f32 v18, v18, v22
v_max_f32 v19, v19, v23
v_mov_b32 v20, v16
v_mov_b32 v21, v17
v_mov_b32 v22, v18
v_mov_b32 v23, v19
s_nop 1
v_permlane16_swap_b32 v16, v20
v_permlane16_swap_b32 v17, v21
v_permlane16_swap_b32 v18, v22
v_permlane16_swap_b32 v19, v23
s_nop 0
v_max_f32 v16, v16, v20
v_max_f32 v17, v17, v21
v_max_f32 v18, v18, v22
v_max_f32 v19, v19, v23
v_sub_f32 v6, 0, v16
v_sub_f32 v7, 0, v17
v_sub_f32 v8, 0, v18
v_sub_f32 v9, 0, v19
s_lshl_b32 s62, s39, 13
s_mov_b32 s66, s62
s_mov_b32 s64, 0
s_mov_b32 s65, 4096
s_mov_b32 m0, s66
s_nop 0
buffer_load_dwordx4 v12, s[44:47], s64 offen offset:0 sc1 lds
buffer_load_dwordx4 v12, s[44:47], s64 offen offset:1024 sc1 lds
buffer_load_dwordx4 v12, s[44:47], s64 offen offset:2048 sc1 lds
buffer_load_dwordx4 v12, s[44:47], s64 offen offset:3072 sc1 lds
s_add_u32 m0, s66, 4096
s_nop 0
buffer_load_dwordx4 v12, s[44:47], s65 offen offset:0 sc1 lds
buffer_load_dwordx4 v12, s[44:47], s65 offen offset:1024 sc1 lds
buffer_load_dwordx4 v12, s[44:47], s65 offen offset:2048 sc1 lds
buffer_load_dwordx4 v12, s[44:47], s65 offen offset:3072 sc1 lds
s_add_u32 s64, s64, 32768
s_add_u32 s65, s65, 32768
s_add_u32 s66, s66, 32768
s_and_b32 s66, s66, 0x1ffff
s_mov_b32 m0, s66
s_nop 0
buffer_load_dwordx4 v12, s[44:47], s64 offen offset:0 sc1 lds
buffer_load_dwordx4 v12, s[44:47], s64 offen offset:1024 sc1 lds
buffer_load_dwordx4 v12, s[44:47], s64 offen offset:2048 sc1 lds
buffer_load_dwordx4 v12, s[44:47], s64 offen offset:3072 sc1 lds
s_add_u32 m0, s66, 4096
s_nop 0
buffer_load_dwordx4 v12, s[44:47], s65 offen offset:0 sc1 lds
buffer_load_dwordx4 v12, s[44:47], s65 offen offset:1024 sc1 lds
buffer_load_dwordx4 v12, s[44:47], s65 offen offset:2048 sc1 lds
buffer_load_dwordx4 v12, s[44:47], s65 offen offset:3072 sc1 lds
s_add_u32 s64, s64, 32768
s_add_u32 s65, s65, 32768
s_add_u32 s66, s66, 32768
s_and_b32 s66, s66, 0x1ffff
s_mov_b32 m0, s66
s_nop 0
buffer_load_dwordx4 v12, s[44:47], s64 offen offset:0 sc1 lds
buffer_load_dwordx4 v12, s[44:47], s64 offen offset:1024 sc1 lds
buffer_load_dwordx4 v12, s[44:47], s64 offen offset:2048 sc1 lds
buffer_load_dwordx4 v12, s[44:47], s64 offen offset:3072 sc1 lds
s_add_u32 m0, s66, 4096
s_nop 0
buffer_load_dwordx4 v12, s[44:47], s65 offen offset:0 sc1 lds
buffer_load_dwordx4 v12, s[44:47], s65 offen offset:1024 sc1 lds
buffer_load_dwordx4 v12, s[44:47], s65 offen offset:2048 sc1 lds
buffer_load_dwordx4 v12, s[44:47], s65 offen offset:3072 sc1 lds
s_add_u32 s64, s64, 32768
s_add_u32 s65, s65, 32768
s_add_u32 s66, s66, 32768
s_and_b32 s66, s66, 0x1ffff
s_mov_b32 m0, s66
s_nop 0
buffer_load_dwordx4 v12, s[44:47], s64 offen offset:0 sc1 lds
buffer_load_dwordx4 v12, s[44:47], s64 offen offset:1024 sc1 lds
buffer_load_dwordx4 v12, s[44:47], s64 offen offset:2048 sc1 lds
buffer_load_dwordx4 v12, s[44:47], s64 offen offset:3072 sc1 lds
s_lshl_b32 s62, s61, 2
s_add_u32 s62, s62, s39
s_lshl_b32 s62, s62, 5
s_sub_u32 s68, 0, s62
s_sub_u32 s62, 15, s61
s_lshl_b32 s62, s62, 2
s_add_u32 s62, s62, s39
s_lshl_b32 s62, s62, 5
s_lshl_b32 s63, s70, 6
s_sub_u32 s69, s63, s62
s_sub_i32 s68, s68, 32
s_sub_i32 s69, s69, 32
v_mov_b32 v2, v1
v_add_u32 v3, 0x18000, v1
v_mov_b32 v96, v8
v_mov_b32 v97, v8
v_mov_b32 v98, v8
v_mov_b32 v99, v8
v_mov_b32 v100, v9
v_mov_b32 v101, v9
v_mov_b32 v102, v9
v_mov_b32 v103, v9
v_mov_b32 v104, v8
v_mov_b32 v105, v8
v_mov_b32 v106, v8
v_mov_b32 v107, v8
v_mov_b32 v108, v9
v_mov_b32 v109, v9
v_mov_b32 v110, v9
v_mov_b32 v111, v9
s_add_i32 s68, s68, 32
s_cmp_eq_i32 s68, 0
s_cselect_b32 s74, 1.0, 0
s_cmp_gt_i32 s68, 0
s_cselect_b32 s75, s72, 0
v_add_f32 v10, s75, v6
v_add_f32 v11, s75, v7
v_fma_f32 v80, s74, v112, v10
v_fma_f32 v81, s74, v113, v10
v_fma_f32 v82, s74, v114, v10
v_fma_f32 v83, s74, v115, v10
v_fma_f32 v84, s74, v116, v11
v_fma_f32 v85, s74, v117, v11
v_fma_f32 v86, s74, v118, v11
v_fma_f32 v87, s74, v119, v11
v_fma_f32 v88, s74, v120, v10
v_fma_f32 v89, s74, v121, v10
v_fma_f32 v90, s74, v122, v10
v_fma_f32 v91, s74, v123, v10
v_fma_f32 v92, s74, v124, v11
v_fma_f32 v93, s74, v125, v11
v_fma_f32 v94, s74, v126, v11
v_fma_f32 v95, s74, v127, v11
v_mov_b32 v128, 0
v_mov_b32 v129, 0
v_mov_b32 v130, 0
v_mov_b32 v131, 0
v_mov_b32 v132, 0
v_mov_b32 v133, 0
v_mov_b32 v134, 0
v_mov_b32 v135, 0
v_mov_b32 v136, 0
v_mov_b32 v137, 0
v_mov_b32 v138, 0
v_mov_b32 v139, 0
v_mov_b32 v140, 0
v_mov_b32 v141, 0
v_mov_b32 v142, 0
v_mov_b32 v143, 0
v_mov_b32 v144, 0
v_mov_b32 v145, 0
v_mov_b32 v146, 0
v_mov_b32 v147, 0
v_mov_b32 v148, 0
v_mov_b32 v149, 0
v_mov_b32 v150, 0
v_mov_b32 v151, 0
v_mov_b32 v152, 0
v_mov_b32 v153, 0
v_mov_b32 v154, 0
v_mov_b32 v155, 0
v_mov_b32 v156, 0
v_mov_b32 v157, 0
v_mov_b32 v158, 0
v_mov_b32 v159, 0
v_mov_b32 v160, 0
v_mov_b32 v161, 0
v_mov_b32 v162, 0
v_mov_b32 v163, 0
v_mov_b32 v164, 0
v_mov_b32 v165, 0
v_mov_b32 v166, 0
v_mov_b32 v167, 0
v_mov_b32 v168, 0
v_mov_b32 v169, 0
v_mov_b32 v170, 0
v_mov_b32 v171, 0
v_mov_b32 v172, 0
v_mov_b32 v173, 0
v_mov_b32 v174, 0
v_mov_b32 v175, 0
v_mov_b32 v176, 0
v_mov_b32 v177, 0
v_mov_b32 v178, 0
v_mov_b32 v179, 0
v_mov_b32 v180, 0
v_mov_b32 v181, 0
v_mov_b32 v182, 0
v_mov_b32 v183, 0
v_mov_b32 v184, 0
v_mov_b32 v185, 0
v_mov_b32 v186, 0
v_mov_b32 v187, 0
v_mov_b32 v188, 0
v_mov_b32 v189, 0
v_mov_b32 v190, 0
v_mov_b32 v191, 0
v_mov_b32 v192, 0
v_mov_b32 v193, 0
v_mov_b32 v194, 0
v_mov_b32 v195, 0
v_mov_b32 v196, 0
v_mov_b32 v197, 0
v_mov_b32 v198, 0
v_mov_b32 v199, 0
v_mov_b32 v200, 0
v_mov_b32 v201, 0
v_mov_b32 v202, 0
v_mov_b32 v203, 0
v_mov_b32 v204, 0
v_mov_b32 v205, 0
v_mov_b32 v206, 0
v_mov_b32 v207, 0
v_mov_b32 v208, 0
v_mov_b32 v209, 0
v_mov_b32 v210, 0
v_mov_b32 v211, 0
v_mov_b32 v212, 0
v_mov_b32 v213, 0
v_mov_b32 v214, 0
v_mov_b32 v215, 0
v_mov_b32 v216, 0
v_mov_b32 v217, 0
v_mov_b32 v218, 0
v_mov_b32 v219, 0
v_mov_b32 v220, 0
v_mov_b32 v221, 0
v_mov_b32 v222, 0
v_mov_b32 v223, 0
v_mov_b32 v224, 0
v_mov_b32 v225, 0
v_mov_b32 v226, 0
v_mov_b32 v227, 0
v_mov_b32 v228, 0
v_mov_b32 v229, 0
v_mov_b32 v230, 0
v_mov_b32 v231, 0
v_mov_b32 v232, 0
v_mov_b32 v233, 0
v_mov_b32 v234, 0
v_mov_b32 v235, 0
v_mov_b32 v236, 0
v_mov_b32 v237, 0
v_mov_b32 v238, 0
v_mov_b32 v239, 0
v_mov_b32 v240, 0
v_mov_b32 v241, 0
v_mov_b32 v242, 0
v_mov_b32 v243, 0
v_mov_b32 v244, 0
v_mov_b32 v245, 0
v_mov_b32 v246, 0
v_mov_b32 v247, 0
v_mov_b32 v248, 0
v_mov_b32 v249, 0
v_mov_b32 v250, 0
v_mov_b32 v251, 0
v_mov_b32 v252, 0
v_mov_b32 v253, 0
v_mov_b32 v254, 0
v_mov_b32 v255, 0
v_mov_b32 v64, 0
v_mov_b32 v65, 0
v_mov_b32 v66, 0
v_mov_b32 v67, 0
v_mov_b32 v68, 0
v_mov_b32 v69, 0
v_mov_b32 v70, 0
v_mov_b32 v71, 0
v_mov_b32 v72, 0
v_mov_b32 v73, 0
v_mov_b32 v74, 0
v_mov_b32 v75, 0
v_mov_b32 v76, 0
v_mov_b32 v77, 0
v_mov_b32 v78, 0
v_mov_b32 v79, 0
v_mov_b32 v48, 0
v_mov_b32 v49, 0
v_mov_b32 v50, 0
v_mov_b32 v51, 0
v_mov_b32 v52, 0
v_mov_b32 v53, 0
v_mov_b32 v54, 0
v_mov_b32 v55, 0
v_mov_b32 v56, 0
v_mov_b32 v57, 0
v_mov_b32 v58, 0
v_mov_b32 v59, 0
v_mov_b32 v60, 0
v_mov_b32 v61, 0
)ASM"
R"ASM(v_mov_b32 v62, 0
v_mov_b32 v63, 0
s_waitcnt vmcnt(12)
s_barrier
ds_read_b128 a[0:3], v2 offset:0
ds_read_b128 a[16:19], v2 offset:4096
ds_read_b128 a[4:7], v2 offset:1024
ds_read_b128 a[20:23], v2 offset:5120
ds_read_b128 a[8:11], v2 offset:2048
ds_read_b128 a[24:27], v2 offset:6144
ds_read_b128 a[12:15], v2 offset:3072
ds_read_b128 a[28:31], v2 offset:7168
s_waitcnt lgkmcnt(0)
s_branch L_pre%=
L_slow0%=:
s_mov_b32 s78, 0
L_spin0%=:
s_cmp_lg_u32 s32, 0
s_cbranch_scc1 L_back0%=
s_sleep 2
s_mov_b32 s79, 0
buffer_load_dword v31, v30, s[40:43], s79 offen sc1
s_add_u32 s78, s78, 1
s_cmp_ge_u32 s78, 20000
s_cselect_b32 s32, 1, s32
s_waitcnt vmcnt(0)
v_cmp_ne_u32 vcc, s33, v31
s_mov_b64 exec, -1
s_nop 4
s_cbranch_vccnz L_spin0%=
s_branch L_back0%=
L_slow1%=:
s_mov_b32 s78, 0
L_spin1%=:
s_cmp_lg_u32 s32, 0
s_cbranch_scc1 L_back1%=
s_sleep 2
s_mov_b32 s79, 32768
buffer_load_dword v31, v30, s[40:43], s79 offen sc1
s_add_u32 s78, s78, 1
s_cmp_ge_u32 s78, 20000
s_cselect_b32 s32, 1, s32
s_waitcnt vmcnt(0)
v_cmp_ne_u32 vcc, s33, v31
s_mov_b64 exec, -1
s_nop 4
s_cbranch_vccnz L_spin1%=
s_branch L_back1%=
L_slow2%=:
s_mov_b32 s78, 0
L_spin2%=:
s_cmp_lg_u32 s32, 0
s_cbranch_scc1 L_back2%=
s_sleep 2
s_mov_b32 s79, 65536
buffer_load_dword v19, v18, s[40:43], s79 offen sc1
s_add_u32 s78, s78, 1
s_cmp_ge_u32 s78, 20000
s_cselect_b32 s32, 1, s32
s_waitcnt vmcnt(0)
v_cmp_ne_u32 vcc, s33, v19
s_mov_b64 exec, -1
s_nop 4
s_cbranch_vccnz L_spin2%=
s_branch L_back2%=
L_slow3%=:
s_mov_b32 s78, 0
L_spin3%=:
s_cmp_lg_u32 s32, 0
s_cbranch_scc1 L_back3%=
s_sleep 2
s_mov_b32 s79, 98304
s_mov_b64 exec, s[74:75]
buffer_load_dword v11, v10, s[40:43], s79 offen sc1
s_add_u32 s78, s78, 1
s_cmp_ge_u32 s78, 20000
s_cselect_b32 s32, 1, s32
s_waitcnt vmcnt(0)
v_cmp_ne_u32 vcc, s33, v11
s_mov_b64 exec, -1
s_nop 4
s_cbranch_vccnz L_spin3%=
s_branch L_back3%=
L_slow4%=:
s_mov_b32 s78, 0
L_spin4%=:
s_cmp_lg_u32 s32, 0
s_cbranch_scc1 L_back4%=
s_sleep 2
s_mov_b32 s79, 131072
s_mov_b64 exec, s[74:75]
buffer_load_dword v11, v10, s[40:43], s79 offen sc1
s_add_u32 s78, s78, 1
s_cmp_ge_u32 s78, 20000
s_cselect_b32 s32, 1, s32
s_waitcnt vmcnt(0)
v_cmp_ne_u32 vcc, s33, v11
s_mov_b64 exec, -1
s_nop 4
s_cbranch_vccnz L_spin4%=
s_branch L_back4%=
L_slow5%=:
s_mov_b32 s78, 0
L_spin5%=:
s_cmp_lg_u32 s32, 0
s_cbranch_scc1 L_back5%=
s_sleep 2
s_mov_b32 s79, 163840
s_mov_b64 exec, s[74:75]
buffer_load_dword v11, v10, s[40:43], s79 offen sc1
s_add_u32 s78, s78, 1
s_cmp_ge_u32 s78, 20000
s_cselect_b32 s32, 1, s32
s_waitcnt vmcnt(0)
v_cmp_ne_u32 vcc, s33, v11
s_mov_b64 exec, -1
s_nop 4
s_cbranch_vccnz L_spin5%=
s_branch L_back5%=
L_end%=:
)ASM"
  :: "s"(__builtin_amdgcn_kernarg_segment_ptr()), "s"(blockIdx.x), "v"(threadIdx.x), "s"((unsigned)(size_t)lds) : "memory", "vcc", "scc", "v1", "v2", "v3", "v4", "v5", "v6", "v7", "v8", "v9", "v10", "v11", "v12", "v13", "v14", "v15", "v16", "v17", "v18", "v19", "v20", "v21", "v22", "v23", "v24", "v25", "v26", "v27", "v28", "v29", "v30", "v31", "v32", "v33", "v34", "v35", "v36", "v37", "v38", "v39", "v40", "v41", "v42", "v43", "v44", "v45", "v46", "v47", "v48", "v49", "v50", "v51", "v52", "v53", "v54", "v55", "v56", "v57", "v58", "v59", "v60", "v61", "v62", "v63", "v64", "v65", "v66", "v67", "v68", "v69", "v70", "v71", "v72", "v73", "v74", "v75", "v76", "v77", "v78", "v79", "v80", "v81", "v82", "v83", "v84", "v85", "v86", "v87", "v88", "v89", "v90", "v91", "v92", "v93", "v94", "v95", "v96", "v97", "v98", "v99", "v100", "v101", "v102", "v103", "v104", "v105", "v106", "v107", "v108", "v109", "v110", "v111", "v112", "v113", "v114", "v115", "v116", "v117", "v118", "v119", "v120", "v121", "v122", "v123", "v124", "v125", "v126", "v127", "v128", "v129", "v130", "v131", "v132", "v133", "v134", "v135", "v136", "v137", "v138", "v139", "v140", "v141", "v142", "v143", "v144", "v145", "v146", "v147", "v148", "v149", "v150", "v151", "v152", "v153", "v154", "v155", "v156", "v157", "v158", "v159", "v160", "v161", "v162", "v163", "v164", "v165", "v166", "v167", "v168", "v169", "v170", "v171", "v172", "v173", "v174", "v175", "v176", "v177", "v178", "v179", "v180", "v181", "v182", "v183", "v184", "v185", "v186", "v187", "v188", "v189", "v190", "v191", "v192", "v193", "v194", "v195", "v196", "v197", "v198", "v199", "v200", "v201", "v202", "v203", "v204", "v205", "v206", "v207", "v208", "v209", "v210", "v211", "v212", "v213", "v214", "v215", "v216", "v217", "v218", "v219", "v220", "v221", "v222", "v223", "v224", "v225", "v226", "v227", "v228", "v229", "v230", "v231", "v232", "v233", "v234", "v235", "v236", "v237", "v238", "v239", "v240", "v241", "v242", "v243", "v244", "v245", "v246", "v247", "v248", "v249", "v250", "v251", "v252", "v253", "v254", "v255", "a0", "a1", "a2", "a3", "a4", "a5", "a6", "a7", "a8", "a9", "a10", "a11", "a12", "a13", "a14", "a15", "a16", "a17", "a18", "a19", "a20", "a21", "a22", "a23", "a24", "a25", "a26", "a27", "a28", "a29", "a30", "a31", "a32", "a33", "a34", "a35", "a36", "a37", "a38", "a39", "a40", "a41", "a42", "a43", "a44", "a45", "a46", "a47", "a48", "a49", "a50", "a51", "a52", "a53", "a54", "a55", "a56", "a57", "a58", "a59", "a60", "a61", "a62", "a63", "a64", "a65", "a66", "a67", "a68", "a69", "a70", "a71", "a72", "a73", "a74", "a75", "a76", "a77", "a78", "a79", "a80", "a81", "a82", "a83", "a84", "a85", "a86", "a87", "a88", "a89", "a90", "a91", "a92", "a93", "a94", "a95", "a96", "a97", "a98", "a99", "a100", "a101", "a102", "a103", "a104", "a105", "a106", "a107", "a108", "a109", "a110", "a111", "a112", "a113", "a114", "a115", "a116", "a117", "a118", "a119", "a120", "a121", "a122", "a123", "a124", "a125", "a126", "a127", "a128", "a129", "a130", "a131", "a132", "a133", "a134", "a135", "a136", "a137", "a138", "a139", "a140", "a141", "a142", "a143", "a144", "a145", "a146", "a147", "a148", "a149", "a150", "a151", "a152", "a153", "a154", "a155", "a156", "a157", "a158", "a159", "a160", "a161", "a162", "a163", "a164", "a165", "a166", "a167", "a168", "a169", "a170", "a171", "a172", "a173", "a174", "a175", "a176", "a177", "a178", "a179", "a180", "a181", "a182", "a183", "a184", "a185", "a186", "a187", "a188", "a189", "a190", "a191", "a192", "a193", "a194", "a195", "a196", "a197", "a198", "a199", "a200", "a201", "a202", "a203", "a204", "a205", "a206", "a207", "a208", "a209", "a210", "a211", "a212", "a213", "a214", "a215", "a216", "a217", "a218", "a219", "a220", "a221", "a222", "a223", "a224", "a225", "a226", "a227", "a228", "a229", "a230", "a231", "a232", "a233", "a234", "a235", "a236", "a237", "a238", "a239", "a240", "a241", "a242", "a243", "a244", "a245", "a246", "a247", "a248", "a249", "a250", "a251", "a252", "a253", "a254", "a255", "s8", "s9", "s10", "s11", "s12", "s13", "s14", "s15", "s16", "s17", "s18", "s19", "s20", "s21", "s22", "s23", "s24", "s25", "s26", "s27", "s28", "s29", "s30", "s31", "s32", "s33", "s34", "s35", "s36", "s37", "s38", "s39", "s40", "s41", "s42", "s43", "s44", "s45", "s46", "s47", "s48", "s49", "s50", "s51", "s52", "s53", "s54", "s55", "s56", "s57", "s58", "s59", "s60", "s61", "s62", "s63", "s64", "s65", "s66", "s67", "s68", "s69", "s70", "s71", "s72", "s73", "s74", "s75", "s76", "s77", "s78", "s79", "s80", "s81", "s82", "s83", "s84", "s85", "s86", "s87", "s88", "s89", "s90", "s91", "s92", "s93", "s94", "s95", "s96", "s97", "s98", "s99");
}

extern "C" void kernel_launch(void* const* d_in, const int* in_sizes, int n_in, void* d_out, int out_size, void* d_ws, size_t ws_size, hipStream_t stream){
  char* ws = (char*)d_ws;
 k_f<<<256,256,0,stream>>>(d_in[0], d_in[1], d_in[2], d_in[3], d_in[4], d_in[5], d_in[7], d_in[8], d_in[9], d_in[10], d_out, d_ws);

}
```
